```python
import torch

def _softmax(x, axis=-1):
    return torch.softmax(x, dim=axis)

def _gelu(x):
    return torch.nn.functional.gelu(x, approximate='tanh')

def _silu(x):
    return torch.nn.functional.silu(x)

def _sigmoid(x):
    return torch.sigmoid(x)

def _layernorm(x, axis=-1, eps=1e-05):
    m = x.mean(dim=axis, keepdim=True)
    v = x.var(dim=axis, keepdim=True, unbiased=False)
    return (x - m) / torch.sqrt(v + eps)

def _scatter_add(tgt, idx, src, axis=0):
    return tgt.clone().index_add_(axis, idx, src)

def _segment_sum(src, seg, n):
    out = torch.zeros((n,) + src.shape[1:], dtype=src.dtype)
    return out.index_add_(0, seg, src)

def _pad(x, pw):
    flat = tuple((v for pair in reversed(pw) for v in pair))
    return torch.nn.functional.pad(x, flat)
kernel_name = 'gated_mix__memory_aug_recurrent_mem_tr__dense_activation_gelu__prompt'

def setup_inputs():

    _w = lambda a: (lambda t: t.to(torch.bfloat16) if t.is_floating_point() else t)(torch.as_tensor(a))

    def _A_setup():
        B, S, Dm = (1, 2048, 576)
        H, Dh = (32, 128)
        M, seg = (8, 32)
        seg = min(seg, S)
        S = S // seg * seg
        return {'x': _w((lambda *s: torch.randn(*s, dtype=torch.bfloat16) * 0.05)(B, S, Dm)), 'mem0': _w((lambda *s: torch.randn(*s, dtype=torch.bfloat16) * 0.4)(B, M, Dm)), 'Wq': _w((lambda *s: torch.randn(*s, dtype=torch.bfloat16) * 6.4)(Dm, H, Dh) * Dm ** (-0.5)), 'Wk': _w((lambda *s: torch.randn(*s, dtype=torch.bfloat16) * 6.4)(Dm, H, Dh) * Dm ** (-0.5)), 'Wv': _w((lambda *s: torch.randn(*s, dtype=torch.bfloat16) * 0.4)(Dm, H, Dh) * Dm ** (-0.5)), 'Wo': _w((lambda *s: torch.randn(*s, dtype=torch.bfloat16) * 0.4)(H, Dh, Dm) * (H * Dh) ** (-0.5)), 'seg_len': seg}
    _A_in = _A_setup()

    def _B_setup():
        B, S, D, F = (1, 2048, 576, 4608)
        return {'x': _w((lambda *s: torch.randn(*s, dtype=torch.bfloat16) * 0.05)(B, S, D)), 'w1': _w((lambda *s: torch.randn(*s, dtype=torch.bfloat16) * 3.2)(D, F) * D ** (-0.5)), 'b1': _w((lambda *s: torch.randn(*s, dtype=torch.bfloat16) * 3.2)(F) * 0.01), 'w2': _w((lambda *s: torch.randn(*s, dtype=torch.bfloat16) * 0.2)(F, D) * F ** (-0.5)), 'b2': _w((lambda *s: torch.randn(*s, dtype=torch.bfloat16) * 0.4)(D) * 0.01)}
    _B_in = _B_setup()
    return {'A_x': _A_in['x'], 'A_mem0': _A_in['mem0'], 'A_Wq': _A_in['Wq'], 'A_Wk': _A_in['Wk'], 'A_Wv': _A_in['Wv'], 'A_Wo': _A_in['Wo'], 'A_seg_len': _A_in['seg_len'], 'B_x': _B_in['x'], 'B_w1': _B_in['w1'], 'B_b1': _B_in['b1'], 'B_w2': _B_in['w2'], 'B_b2': _B_in['b2'], 'gate_w': _w((lambda *s: torch.randn(*s, dtype=torch.bfloat16) * 0.05)(576) * 16.0)}

def reference(A_x, A_mem0, A_Wq, A_Wk, A_Wv, A_Wo, A_seg_len, B_x, B_w1, B_b1, B_w2, B_b2, gate_w):

    def _A_ref(x, mem0, Wq, Wk, Wv, Wo, seg_len):
        B, S, Dm = x.shape
        H, Dh = (Wq.shape[1], Wq.shape[2])
        M = mem0.shape[1]
        scale = Dh ** (-0.5)
        n_seg = S // seg_len
        L = M + seg_len
        idx = torch.arange(L)
        mask = (idx[:, None] < M) | (idx[:, None] >= idx[None, :])
        mem = mem0
        outs = []
        for s in range(n_seg):
            xs = x[:, s * seg_len:(s + 1) * seg_len, :].clone()
            h = torch.concatenate([mem, xs], axis=1)
            q = torch.einsum('btd,dhe->bhte', h, Wq)
            k = torch.einsum('btd,dhe->bhte', h, Wk)
            v = torch.einsum('btd,dhe->bhte', h, Wv)
            scores = torch.einsum('bhqe,bhke->bhqk', q, k) * scale
            scores = torch.where(mask[None, None], scores, -1000000000.0)
            p = _softmax(scores, axis=-1)
            o = torch.einsum('bhqk,bhke->bhqe', p, v)
            o = torch.einsum('bhte,hed->btd', o, Wo)
            mem = o[:, :M, :].clone()
            outs.append(o[:, M:, :])
        return torch.concatenate(outs, axis=1)

    def _B_ref(x, w1, b1, w2, b2):
        h = torch.einsum('bsd,df->bsf', x, w1) + b1
        h = _gelu(h)
        return torch.einsum('bsf,fd->bsd', h, w2) + b2
    x = A_x
    g = _sigmoid((x * gate_w).sum(-1, keepdims=True))
    a = _A_ref(x, A_mem0, A_Wq, A_Wk, A_Wv, A_Wo, A_seg_len)
    b = _B_ref(x, B_w1, B_b1, B_w2, B_b2)
    return g * a + (1 - g) * b
```

```cpp
#include <hip/hip_runtime.h>
#include <hip/hip_bf16.h>
#include <stdint.h>

typedef unsigned u32;
typedef unsigned short u16;
typedef u32 u2 __attribute__((ext_vector_type(2)));
typedef u32 u4 __attribute__((ext_vector_type(4)));
typedef u32 u8v __attribute__((ext_vector_type(8)));
typedef u32 u32v __attribute__((ext_vector_type(32)));
typedef u32 u16v __attribute__((ext_vector_type(16)));
typedef float f4 __attribute__((ext_vector_type(4)));
typedef short s4v __attribute__((ext_vector_type(4)));
typedef __bf16 bf2 __attribute__((ext_vector_type(2)));
typedef float f2 __attribute__((ext_vector_type(2)));
#define LAS __attribute__((address_space(3)))
#define DI __device__ __forceinline__
#define US(x) __builtin_amdgcn_readfirstlane(x)

constexpr int NSEG = 64;
constexpr int FSETS = 1;
constexpr u32 SENT = 0xFFFFFFFFu;
constexpr u32 MB = 1u << 20;
constexpr u32 OFF_MP = 0;
constexpr u32 OFF_Y = 8 * MB;
constexpr u32 OFF_Z = 66 * MB;
constexpr u32 OFF_CTL = 138 * MB;
constexpr u32 OFF_PX = 140 * MB;
constexpr u32 OFF_H = 146 * MB;
constexpr u32 OFF_FP = 164 * MB;
constexpr u32 WS_SPAN = 186 * MB;
typedef unsigned long long u64;
constexpr int CTL_PAR = 0;
constexpr u32 OFF_FLAG = 64 * MB;
constexpr u32 OFF_GATE = OFF_CTL + 65536;
constexpr int CTL_STAMP = 32768;
constexpr u32 OOB = 0xFFFFFF00u;

struct Params {
  const u16* x; const u16* mem0; const u16* Wq; const u16* Wk; const u16* Wv; const u16* Wo;
  const u16* w1; const u16* b1; const u16* w2; const u16* b2; const u16* gw;
  u16* out; char* ws;
  int dM, dY, dP, dV, place, pad0, mask, pad1, st0, pad2;
};

DI float bf2f(u16 b) { return __uint_as_float((u32)b << 16); }
DI u32 cvtpk(float lo, float hi) { f2 v = {lo, hi}; bf2 r = __builtin_convertvector(v, bf2); return __builtin_bit_cast(u32, r); }
DI u16 f2bf(float f) { return (u16)(cvtpk(f, 0.f) & 0xFFFF); }
DI u32 clk() { return (u32)__builtin_amdgcn_s_memtime(); }
DI __amdgpu_buffer_rsrc_t mkrsrc(const void* p, u32 n) { return __builtin_amdgcn_make_buffer_rsrc((void*)p, 0, n, 0x00020000); }
DI u4 ld16(__amdgpu_buffer_rsrc_t r, u32 v, u32 s) { return __builtin_amdgcn_raw_buffer_load_b128(r, v, s, 16); }
DI u2 ld8(__amdgpu_buffer_rsrc_t r, u32 v, u32 s) { return __builtin_amdgcn_raw_buffer_load_b64(r, v, s, 16); }
DI void st16(u4 d, __amdgpu_buffer_rsrc_t r, u32 v, u32 s) { __builtin_amdgcn_raw_buffer_store_b128(d, r, v, s, 16); asm volatile("s_nop 1" :: "v"(d) : "memory"); }
DI void st8(u2 d, __amdgpu_buffer_rsrc_t r, u32 v, u32 s) { __builtin_amdgcn_raw_buffer_store_b64(d, r, v, s, 16); }
DI void st4(u32 d, __amdgpu_buffer_rsrc_t r, u32 v, u32 s) { __builtin_amdgcn_raw_buffer_store_b32(d, r, v, s, 16); }
DI u32 max3u(u32 a, u32 b, u32 c) { return max(max(a, b), c); }
DI u32 san(u32 d) { return min(d, 0xFFFFFFFEu); }
DI u2 trd(u32 ldsaddr) { s4v r = __builtin_amdgcn_ds_read_tr16_b64_v4i16((LAS s4v*)(uintptr_t)ldsaddr); return __builtin_bit_cast(u2, r); }
DI void glds16(const void* g, u32 lds_dst) {
  u32 keep;
  asm volatile("s_mov_b32 %0, m0\n\ts_mov_b32 m0, %2\n\ts_nop 0\n\tglobal_load_lds_dwordx4 %1, off\n\ts_mov_b32 m0, %0" : "=&s"(keep) : "v"(g), "s"(lds_dst) : "memory");
}
#define SMF_A(acc, a, w, idx) asm volatile("v_smfmac_f32_16x16x64_bf16 %0, %1, %2, %3" : "+v"(acc) : "v"(a), "a"(w), "v"(idx))
#define SMF_V(acc, a, w, idx) asm volatile("v_smfmac_f32_16x16x64_bf16 %0, %1, %2, %3" : "+v"(acc) : "v"(a), "v"(w), "v"(idx))
#define MFMA(acc, a, b) asm volatile("v_mfma_f32_16x16x32_bf16 %0, %1, %2, %0" : "+v"(acc) : "v"(a), "v"(b))
DI void swap32(float& a, float& b) { auto r = __builtin_amdgcn_permlane32_swap(__float_as_uint(a), __float_as_uint(b), false, false); a = __uint_as_float(r[0]); b = __uint_as_float(r[1]); }
DI float x16max(float x) { auto r = __builtin_amdgcn_permlane16_swap(__float_as_uint(x), __float_as_uint(x), false, false); return __builtin_elementwise_maximum(__uint_as_float(r[0]), __uint_as_float(r[1])); }
DI float x32max(float x) { auto r = __builtin_amdgcn_permlane32_swap(__float_as_uint(x), __float_as_uint(x), false, false); return __builtin_elementwise_maximum(__uint_as_float(r[0]), __uint_as_float(r[1])); }
DI float x16sum(float x) { auto r = __builtin_amdgcn_permlane16_swap(__float_as_uint(x), __float_as_uint(x), false, false); return __uint_as_float(r[0]) + __uint_as_float(r[1]); }
DI float x32sum(float x) { auto r = __builtin_amdgcn_permlane32_swap(__float_as_uint(x), __float_as_uint(x), false, false); return __uint_as_float(r[0]) + __uint_as_float(r[1]); }
template <int CTRL> DI float dpp(float x) { return __builtin_bit_cast(float, __builtin_amdgcn_mov_dpp(__builtin_bit_cast(int, x), CTRL, 0xf, 0xf, true)); }
DI u32 rtc() { return (u32)__builtin_amdgcn_s_memrealtime(); }
DI void wait_fine(u32 t) {
  int d;
  while ((d = (int)(t - rtc() * 8u)) > 0) {}
}
DI float mx(float a, float b) { return __builtin_elementwise_maximum(a, b); }
DI float mx3(float a, float b, float c) { return mx(mx(a, b), c); }
DI u4 mksrd(const void* p, u32 n) { u4 r; r[0] = (u32)(uintptr_t)p; r[1] = (u32)((uintptr_t)p >> 32) & 0xFFFFu; r[2] = n; r[3] = 0x00020000u; return r; }
#define RSH 400
#define RS0 300
#define LOOPSTART
#define EVT(i)
#define ZS1 7
#define ZS2 8
#define ZH 16
#define HLEAD 50
#define PFN 9
#define LTH 40
constexpr int SPIN_CAP = 1 << 16;
constexpr float CS = 0.08838834764831845f * 1.4426950408889634f;

DI u32 mp_base(int s, u32 par) { return OFF_MP + (u32)s * 131072u + ((u32)((par >> (s >> 4)) & 1u) << 13); }
DI u32 y_base(int s, u32 par) { int b = s / 3; return OFF_Y + (u32)b * (2 * MB) + (u32)(s - 3 * b) * (576u * 1024u) + ((u32)((par >> (4 + b)) & 1u) << 13); }
DI u32 lm(u32 x) { return (x & 0x1FFFu) | ((x >> 13) << 14); }
DI u32 kq_base(int s, u32 par) { return ((u32)s << 20) | (((~par >> (s >> 1)) & 1u) << 13); }
DI u32 fboth(int i, int tid) { return tid < 2 ? OFF_FLAG + tid * 8192 + lm(i * 128) : OOB; }
DI u32 fnear(int i, u32 par) { return OFF_FLAG + ((u32)((par >> 30) & 1u) << 13) + lm(i * 128); }
DI u32 ffar(int i, u32 par) { return OFF_FLAG + ((u32)((~par >> 30) & 1u) << 13) + lm(i * 128); }
DI u32 z_base(int s, int cc) { return OFF_Z + (u32)(s * 36 + cc) * 32768u; }

DI u32 fine_now() {
  u32 r;
  asm volatile(
      "s_memrealtime s[64:65]\n\t"
      "s_nop 1\n\t"
      "s_memrealtime s[66:67]\n\t"
      "s_nop 1\n\t"
      "s_memrealtime s[68:69]\n\t"
      "s_nop 1\n\t"
      "s_memrealtime s[70:71]\n\t"
      "s_nop 1\n\t"
      "s_memrealtime s[72:73]\n\t"
      "s_nop 1\n\t"
      "s_memrealtime s[74:75]\n\t"
      "s_nop 1\n\t"
      "s_memrealtime s[76:77]\n\t"
      "s_nop 1\n\t"
      "s_memrealtime s[78:79]\n\t"
      "s_waitcnt lgkmcnt(0)\n\t"
      "s_add_u32 s64, s64, s66\n\t"
      "s_add_u32 s68, s68, s70\n\t"
      "s_add_u32 s72, s72, s74\n\t"
      "s_add_u32 s76, s76, s78\n\t"
      "s_add_u32 s64, s64, s68\n\t"
      "s_add_u32 s72, s72, s76\n\t"
      "s_add_u32 %0, s64, s72\n\t"
      : "=&s"(r) :: "s64", "s65", "s66", "s67", "s68", "s69", "s70", "s71", "s72", "s73", "s74", "s75", "s76", "s77", "s78", "s79", "s80", "s81", "s82", "s83", "s84", "s85", "s86", "s87", "s88", "s89", "s90", "s91", "s92", "s93", "s94", "s95", "scc", "memory");
  return r;
}
constexpr u32 SCH_OFF = 104192u;
constexpr u32 ST_OFF = 81920u;
DI u32 st_addr(int i) { return ST_OFF + (u32)(i >> 6) * 16384u + (u32)(i & 63) * 128u; }
DI void put_stamp(__amdgpu_buffer_rsrc_t rs, u32 mb, int i, u32 te, u32 du, bool failed, int lane) {
  u2 v = {te & ~1u, failed ? 0x7FFFu : min(du, 0x6000u)};
  st8(v, rs, lane == 0 ? st_addr(i) : 0xFFFFFF00u, mb);
}
DI u32 get_sched(__amdgpu_buffer_rsrc_t rs, u32 mb, int i) {
  const u32 off = SCH_OFF + i * 4;
  for (int n = 0; n < 8192; ++n) {
    asm volatile("" ::: "memory");
    const u32 t = __builtin_amdgcn_readfirstlane(__builtin_amdgcn_raw_buffer_load_b32(rs, off, mb, 16));
    if (t != SENT) return (int)(t - rtc() * 8u) > 40000 ? t - 0x40000000u : t;
  }
  return rtc() * 8u - 800u;
}

DI u32 clampt(u32 t) { return (int)(t - rtc() * 8u) > 40000 ? t - 0x40000000u : t; }
DI u32 next_tgt(__amdgpu_buffer_rsrc_t rs, u4 srd, u32 mb, u32 mb1, u32 tgt, u32& prov, bool& have, int lane) {
  volatile LAS u32* box = (volatile LAS u32*)(uintptr_t)16384;
  if ((int)(rtc() * 8u - tgt) > 720) { const u32 g = get_sched(rs, mb1, 0); prov = get_sched(rs, mb1, 2); have = true; return g; }
  if (!have) prov = get_sched(rs, mb, 2);
  wait_fine(tgt + 640u);
  const u32 off = SCH_OFF + (lane & 3) * 4;
  u32 v, g;
  bool more;
  do {
    box[lane] = 1u;
    u32 keep;
    asm volatile("s_waitcnt lgkmcnt(0)\n\ts_mov_b32 %0, m0\n\ts_movk_i32 m0, 0x4000\n\ts_nop 2\n\tbuffer_load_dword %1, %2, %3 offen sc1 lds\n\ts_mov_b32 m0, %0" : "=&s"(keep) : "v"(off), "s"(srd), "s"(US(mb1)) : "memory");
    do { v = box[lane]; g = __builtin_amdgcn_readfirstlane(v); more = (int)(prov - 210u - rtc() * 8u) > 0; } while (g == 1u && more);
  } while (g == SENT && more);
  const u32 n = __builtin_amdgcn_readlane(v, 2);
  have = false;
  if (g & 1u) return prov;
  if (!(n & 1u)) { prov = clampt(n); have = true; }
  return clampt(g);
}

constexpr u32 PBASE = 81920;
DI u32 swz(int row) { return 2u * ((row & 3) | (((row >> 3) & 1) << 2)); }
DI u32 psrc(int ldb, bool packed, int hi, int w, int lane) {
  int row = 16 * w + (lane >> 4), ch = (lane & 15) ^ swz(row + 8 * hi);
  if (packed && ch >= 8) { ch -= 8; row += 64; }
  return row * ldb + ch * 16;
}
template <int PERM> DI u32 ptile(int col, int i, int lane) {
  const int g = lane >> 4, q = (lane >> 2) & 3, p = lane & 3;
  int row = 8 * g + i * 4 + q;
  if (PERM == 1) row = 16 * ((row >> 1) & 1) + 8 * ((row >> 4) & 1) + 2 * ((row >> 2) & 3) + (row & 1);
  if (PERM == 2) row = 16 * ((row >> 2) & 1) + 4 * ((row >> 3) & 3) + (row & 3);
  return PBASE + row * 256 + (((col >> 3) + (p >> 1)) ^ swz(row)) * 16 + (p & 1) * 8;
}
template <int N> DI void wait_vm() { asm volatile("s_waitcnt vmcnt(%0)" :: "i"(N) : "memory"); }
DI void bar() { asm volatile("s_waitcnt lgkmcnt(0)\n\ts_barrier" ::: "memory"); }

#define WOUT6(W) "=&{a[0:31]}"(W[0]), "=&{a[32:63]}"(W[1]), "=&{a[64:95]}"(W[2]), "=&{a[96:127]}"(W[3]), "=&{a[128:159]}"(W[4]), "=&{a[160:191]}"(W[5])
#define WOUT(W) WOUT6(W), "=&{a[192:223]}"(W[6]), "=&{a[224:255]}"(W[7]), "=&{v[224:255]}"(W[8])
#define WPIN(W) "{a[0:31]}"(W[0]), "{a[32:63]}"(W[1]), "{a[64:95]}"(W[2]), "{a[96:127]}"(W[3]), "{a[128:159]}"(W[4]), "{a[160:191]}"(W[5]), "{a[192:223]}"(W[6]), "{a[224:255]}"(W[7]), "{v[224:255]}"(W[8])
constexpr u32 C_PROBE = OFF_CTL + 1024, C_DONE = OFF_CTL + 4096, C_IN = OFF_CTL + 16384;
DI u64 ld8u(__amdgpu_buffer_rsrc_t rs, u32 off) { const u2 v = ld8(rs, off, 0); return ((u64)v[1] << 32) | v[0]; }
DI void st8u(u64 x, __amdgpu_buffer_rsrc_t rs, u32 off) { st8((u2){(u32)x, (u32)(x >> 32)}, rs, off, 0); }
DI u64 tag_of(u2 e) { return (((u64)(u32)US(e[1]) << 32) | (u32)US(e[0])) + 0x9E3779B97F4A7C16ull; }
DI u64 mk_tag(const Params& p, u2 e) {
  const u64 tag = tag_of(e);
  __builtin_amdgcn_s_barrier();
  st8u(tag, mkrsrc(p.ws, WS_SPAN), threadIdx.x == 0 ? C_IN + blockIdx.x * 8 : OOB);
  return tag;
}
DI u32 get_par(const Params& p, u64 tag, int level) {
  const __amdgpu_buffer_rsrc_t rs = mkrsrc(p.ws, WS_SPAN);
  const int lane = threadIdx.x & 63;
  for (int n = 0; n < (SPIN_CAP << 2); ++n) {
    asm volatile("" ::: "memory");
    const u64 v = ld8u(rs, C_PROBE + min(lane, 32) * 64), d = level ? ld8u(rs, C_DONE + lane * 64 + (level == 2 && lane >= 32 ? 8 : 0)) : tag;
    if (__builtin_amdgcn_ballot_w64((v >> 1) != (tag >> 1) || d != tag) == 0) {
      asm volatile("" ::: "memory");
      const u32 m = (u32)__builtin_amdgcn_ballot_w64((v & 1) != 0);
      const int k = __builtin_popcount(m ^ 0x55555555u);
      return k <= 9 ? 0x55555555u : k >= 23 ? 0xAAAAAAAAu : m;
    }
    __builtin_amdgcn_s_sleep(2);
  }
  __builtin_amdgcn_endpgm();
  return 0;
}
template <int LDB> DI void load_w2x(u32v (&W)[9], const u16* m0, const u16* m1, int w, int lane) {
  const u4 s0 = mksrd(m0, 575 * LDB + 256), s1 = mksrd(m1, 575 * LDB + 256);
  const u32 va = psrc(LDB, false, 0, w, lane), vb = psrc(LDB, false, 1, w, lane), t0 = ptile<0>(32 * w, 0, lane), t1 = ptile<0>(32 * w + 16, 0, lane), lb = PBASE + w * 4096;
  if constexpr (LDB == 8192) asm volatile(
      "s_mov_b32 s65, m0\n\t"
      "s_add_u32 m0, %[lb], 0\n\t"
      "s_mov_b32 s64, 0\n\t"
      "buffer_load_dwordx4 %[va], %[m0], s64 offen lds\n\t"
      "s_add_u32 m0, %[lb], 1024\n\t"
      "s_mov_b32 s64, 32768\n\t"
      "buffer_load_dwordx4 %[va], %[m0], s64 offen lds\n\t"
      "s_add_u32 m0, %[lb], 2048\n\t"
      "s_mov_b32 s64, 65536\n\t"
      "buffer_load_dwordx4 %[vb], %[m0], s64 offen lds\n\t"
      "s_add_u32 m0, %[lb], 3072\n\t"
      "s_mov_b32 s64, 98304\n\t"
      "buffer_load_dwordx4 %[vb], %[m0], s64 offen lds\n\t"
      "s_add_u32 m0, %[lb], 16384\n\t"
      "s_mov_b32 s64, 0\n\t"
      "buffer_load_dwordx4 %[va], %[m1], s64 offen lds\n\t"
      "s_add_u32 m0, %[lb], 17408\n\t"
      "s_mov_b32 s64, 32768\n\t"
      "buffer_load_dwordx4 %[va], %[m1], s64 offen lds\n\t"
      "s_add_u32 m0, %[lb], 18432\n\t"
      "s_mov_b32 s64, 65536\n\t"
      "buffer_load_dwordx4 %[vb], %[m1], s64 offen lds\n\t"
      "s_add_u32 m0, %[lb], 19456\n\t"
      "s_mov_b32 s64, 98304\n\t"
      "buffer_load_dwordx4 %[vb], %[m1], s64 offen lds\n\t"
      "s_add_u32 m0, %[lb], 32768\n\t"
      "s_mov_b32 s64, 2097152\n\t"
      "buffer_load_dwordx4 %[va], %[m1], s64 offen lds\n\t"
      "s_add_u32 m0, %[lb], 33792\n\t"
      "s_mov_b32 s64, 2129920\n\t"
      "buffer_load_dwordx4 %[va], %[m1], s64 offen lds\n\t"
      "s_add_u32 m0, %[lb], 34816\n\t"
      "s_mov_b32 s64, 2162688\n\t"
      "buffer_load_dwordx4 %[vb], %[m1], s64 offen lds\n\t"
      "s_add_u32 m0, %[lb], 35840\n\t"
      "s_mov_b32 s64, 2195456\n\t"
      "buffer_load_dwordx4 %[vb], %[m1], s64 offen lds\n\t"
      "s_waitcnt vmcnt(8) lgkmcnt(0)\n\t"
      "s_barrier\n\t"
      "s_add_u32 m0, %[lb], 49152\n\t"
      "s_mov_b32 s64, 524288\n\t"
      "buffer_load_dwordx4 %[va], %[m0], s64 offen lds\n\t"
      "s_add_u32 m0, %[lb], 50176\n\t"
      "s_mov_b32 s64, 557056\n\t"
      "buffer_load_dwordx4 %[va], %[m0], s64 offen lds\n\t"
      "s_add_u32 m0, %[lb], 51200\n\t"
      "s_mov_b32 s64, 589824\n\t"
      "buffer_load_dwordx4 %[vb], %[m0], s64 offen lds\n\t"
      "s_add_u32 m0, %[lb], 52224\n\t"
      "s_mov_b32 s64, 622592\n\t"
      "buffer_load_dwordx4 %[vb], %[m0], s64 offen lds\n\t"
      "ds_read_b64_tr_b16 a[0:1], %[t0] offset:0\n\t"
      "ds_read_b64_tr_b16 a[2:3], %[t0] offset:1024\n\t"
      "ds_read_b64_tr_b16 a[4:5], %[t0] offset:8192\n\t"
      "ds_read_b64_tr_b16 a[6:7], %[t0] offset:9216\n\t"
      "ds_read_b64_tr_b16 a[8:9], %[t1] offset:0\n\t"
      "ds_read_b64_tr_b16 a[10:11], %[t1] offset:1024\n\t"
      "ds_read_b64_tr_b16 a[12:13], %[t1] offset:8192\n\t"
      "ds_read_b64_tr_b16 a[14:15], %[t1] offset:9216\n\t"
      "s_waitcnt vmcnt(8) lgkmcnt(0)\n\t"
      "s_barrier\n\t"
      "s_add_u32 m0, %[lb], 0\n\t"
      "s_mov_b32 s64, 2621440\n\t"
      "buffer_load_dwordx4 %[va], %[m0], s64 offen lds\n\t"
      "s_add_u32 m0, %[lb], 1024\n\t"
      "s_mov_b32 s64, 2654208\n\t"
      "buffer_load_dwordx4 %[va], %[m0], s64 offen lds\n\t"
      "s_add_u32 m0, %[lb], 2048\n\t"
      "s_mov_b32 s64, 2686976\n\t"
      "buffer_load_dwordx4 %[vb], %[m0], s64 offen lds\n\t"
      "s_add_u32 m0, %[lb], 3072\n\t"
      "s_mov_b32 s64, 2719744\n\t"
      "buffer_load_dwordx4 %[vb], %[m0], s64 offen lds\n\t"
      "ds_read_b64_tr_b16 a[16:17], %[t0] offset:16384\n\t"
      "ds_read_b64_tr_b16 a[18:19], %[t0] offset:17408\n\t"
      "ds_read_b64_tr_b16 a[20:21], %[t0] offset:24576\n\t"
      "ds_read_b64_tr_b16 a[22:23], %[t0] offset:25600\n\t"
      "ds_read_b64_tr_b16 a[24:25], %[t1] offset:16384\n\t"
      "ds_read_b64_tr_b16 a[26:27], %[t1] offset:17408\n\t"
      "ds_read_b64_tr_b16 a[28:29], %[t1] offset:24576\n\t"
      "ds_read_b64_tr_b16 a[30:31], %[t1] offset:25600\n\t"
      "s_waitcnt vmcnt(8) lgkmcnt(0)\n\t"
      "s_barrier\n\t"
      "s_add_u32 m0, %[lb], 16384\n\t"
      "s_mov_b32 s64, 524288\n\t"
      "buffer_load_dwordx4 %[va], %[m1], s64 offen lds\n\t"
      "s_add_u32 m0, %[lb], 17408\n\t"
      "s_mov_b32 s64, 557056\n\t"
      "buffer_load_dwordx4 %[va], %[m1], s64 offen lds\n\t"
      "s_add_u32 m0, %[lb], 18432\n\t"
      "s_mov_b32 s64, 589824\n\t"
      "buffer_load_dwordx4 %[vb], %[m1], s64 offen lds\n\t"
      "s_add_u32 m0, %[lb], 19456\n\t"
      "s_mov_b32 s64, 622592\n\t"
      "buffer_load_dwordx4 %[vb], %[m1], s64 offen lds\n\t"
      "ds_read_b64_tr_b16 a[144:145], %[t0] offset:32768\n\t"
      "ds_read_b64_tr_b16 a[146:147], %[t0] offset:33792\n\t"
      "ds_read_b64_tr_b16 a[148:149], %[t0] offset:40960\n\t"
      "ds_read_b64_tr_b16 a[150:151], %[t0] offset:41984\n\t"
      "ds_read_b64_tr_b16 a[152:153], %[t1] offset:32768\n\t"
      "ds_read_b64_tr_b16 a[154:155], %[t1] offset:33792\n\t"
      "ds_read_b64_tr_b16 a[156:157], %[t1] offset:40960\n\t"
      "ds_read_b64_tr_b16 a[158:159], %[t1] offset:41984\n\t"
      "s_waitcnt vmcnt(8) lgkmcnt(0)\n\t"
      "s_barrier\n\t"
      "s_add_u32 m0, %[lb], 32768\n\t"
      "s_mov_b32 s64, 2621440\n\t"
      "buffer_load_dwordx4 %[va], %[m1], s64 offen lds\n\t"
      "s_add_u32 m0, %[lb], 33792\n\t"
      "s_mov_b32 s64, 2654208\n\t"
      "buffer_load_dwordx4 %[va], %[m1], s64 offen lds\n\t"
      "s_add_u32 m0, %[lb], 34816\n\t"
      "s_mov_b32 s64, 2686976\n\t"
      "buffer_load_dwordx4 %[vb], %[m1], s64 offen lds\n\t"
      "s_add_u32 m0, %[lb], 35840\n\t"
      "s_mov_b32 s64, 2719744\n\t"
      "buffer_load_dwordx4 %[vb], %[m1], s64 offen lds\n\t"
      "ds_read_b64_tr_b16 a[32:33], %[t0] offset:49152\n\t"
      "ds_read_b64_tr_b16 a[34:35], %[t0] offset:50176\n\t"
      "ds_read_b64_tr_b16 a[36:37], %[t0] offset:57344\n\t"
      "ds_read_b64_tr_b16 a[38:39], %[t0] offset:58368\n\t"
      "ds_read_b64_tr_b16 a[40:41], %[t1] offset:49152\n\t"
      "ds_read_b64_tr_b16 a[42:43], %[t1] offset:50176\n\t"
      "ds_read_b64_tr_b16 a[44:45], %[t1] offset:57344\n\t"
      "ds_read_b64_tr_b16 a[46:47], %[t1] offset:58368\n\t"
      "s_waitcnt vmcnt(8) lgkmcnt(0)\n\t"
      "s_barrier\n\t"
      "s_add_u32 m0, %[lb], 49152\n\t"
      "s_mov_b32 s64, 1048576\n\t"
      "buffer_load_dwordx4 %[va], %[m0], s64 offen lds\n\t"
      "s_add_u32 m0, %[lb], 50176\n\t"
      "s_mov_b32 s64, 1081344\n\t"
      "buffer_load_dwordx4 %[va], %[m0], s64 offen lds\n\t"
      "s_add_u32 m0, %[lb], 51200\n\t"
      "s_mov_b32 s64, 1114112\n\t"
      "buffer_load_dwordx4 %[vb], %[m0], s64 offen lds\n\t"
      "s_add_u32 m0, %[lb], 52224\n\t"
      "s_mov_b32 s64, 1146880\n\t"
      "buffer_load_dwordx4 %[vb], %[m0], s64 offen lds\n\t"
      "ds_read_b64_tr_b16 a[160:161], %[t0] offset:0\n\t"
      "ds_read_b64_tr_b16 a[162:163], %[t0] offset:1024\n\t"
      "ds_read_b64_tr_b16 a[164:165], %[t0] offset:8192\n\t"
      "ds_read_b64_tr_b16 a[166:167], %[t0] offset:9216\n\t"
      "ds_read_b64_tr_b16 a[168:169], %[t1] offset:0\n\t"
      "ds_read_b64_tr_b16 a[170:171], %[t1] offset:1024\n\t"
      "ds_read_b64_tr_b16 a[172:173], %[t1] offset:8192\n\t"
      "ds_read_b64_tr_b16 a[174:175], %[t1] offset:9216\n\t"
      "s_waitcnt vmcnt(8) lgkmcnt(0)\n\t"
      "s_barrier\n\t"
      "s_add_u32 m0, %[lb], 0\n\t"
      "s_mov_b32 s64, 3145728\n\t"
      "buffer_load_dwordx4 %[va], %[m0], s64 offen lds\n\t"
      "s_add_u32 m0, %[lb], 1024\n\t"
      "s_mov_b32 s64, 3178496\n\t"
      "buffer_load_dwordx4 %[va], %[m0], s64 offen lds\n\t"
      "s_add_u32 m0, %[lb], 2048\n\t"
      "s_mov_b32 s64, 3211264\n\t"
      "buffer_load_dwordx4 %[vb], %[m0], s64 offen lds\n\t"
      "s_add_u32 m0, %[lb], 3072\n\t"
      "s_mov_b32 s64, 3244032\n\t"
      "buffer_load_dwordx4 %[vb], %[m0], s64 offen lds\n\t"
      "ds_read_b64_tr_b16 a[48:49], %[t0] offset:16384\n\t"
      "ds_read_b64_tr_b16 a[50:51], %[t0] offset:17408\n\t"
      "ds_read_b64_tr_b16 a[52:53], %[t0] offset:24576\n\t"
      "ds_read_b64_tr_b16 a[54:55], %[t0] offset:25600\n\t"
      "ds_read_b64_tr_b16 a[56:57], %[t1] offset:16384\n\t"
      "ds_read_b64_tr_b16 a[58:59], %[t1] offset:17408\n\t"
      "ds_read_b64_tr_b16 a[60:61], %[t1] offset:24576\n\t"
      "ds_read_b64_tr_b16 a[62:63], %[t1] offset:25600\n\t"
      "s_waitcnt vmcnt(8) lgkmcnt(0)\n\t"
      "s_barrier\n\t"
      "s_add_u32 m0, %[lb], 16384\n\t"
      "s_mov_b32 s64, 1048576\n\t"
      "buffer_load_dwordx4 %[va], %[m1], s64 offen lds\n\t"
      "s_add_u32 m0, %[lb], 17408\n\t"
      "s_mov_b32 s64, 1081344\n\t"
      "buffer_load_dwordx4 %[va], %[m1], s64 offen lds\n\t"
      "s_add_u32 m0, %[lb], 18432\n\t"
      "s_mov_b32 s64, 1114112\n\t"
      "buffer_load_dwordx4 %[vb], %[m1], s64 offen lds\n\t"
      "s_add_u32 m0, %[lb], 19456\n\t"
      "s_mov_b32 s64, 1146880\n\t"
      "buffer_load_dwordx4 %[vb], %[m1], s64 offen lds\n\t"
      "ds_read_b64_tr_b16 a[176:177], %[t0] offset:32768\n\t"
      "ds_read_b64_tr_b16 a[178:179], %[t0] offset:33792\n\t"
      "ds_read_b64_tr_b16 a[180:181], %[t0] offset:40960\n\t"
      "ds_read_b64_tr_b16 a[182:183], %[t0] offset:41984\n\t"
      "ds_read_b64_tr_b16 a[184:185], %[t1] offset:32768\n\t"
      "ds_read_b64_tr_b16 a[186:187], %[t1] offset:33792\n\t"
      "ds_read_b64_tr_b16 a[188:189], %[t1] offset:40960\n\t"
      "ds_read_b64_tr_b16 a[190:191], %[t1] offset:41984\n\t"
      "s_waitcnt vmcnt(8) lgkmcnt(0)\n\t"
      "s_barrier\n\t"
      "s_add_u32 m0, %[lb], 32768\n\t"
      "s_mov_b32 s64, 3145728\n\t"
      "buffer_load_dwordx4 %[va], %[m1], s64 offen lds\n\t"
      "s_add_u32 m0, %[lb], 33792\n\t"
      "s_mov_b32 s64, 3178496\n\t"
      "buffer_load_dwordx4 %[va], %[m1], s64 offen lds\n\t"
      "s_add_u32 m0, %[lb], 34816\n\t"
      "s_mov_b32 s64, 3211264\n\t"
      "buffer_load_dwordx4 %[vb], %[m1], s64 offen lds\n\t"
      "s_add_u32 m0, %[lb], 35840\n\t"
      "s_mov_b32 s64, 3244032\n\t"
      "buffer_load_dwordx4 %[vb], %[m1], s64 offen lds\n\t"
      "ds_read_b64_tr_b16 a[64:65], %[t0] offset:49152\n\t"
      "ds_read_b64_tr_b16 a[66:67], %[t0] offset:50176\n\t"
      "ds_read_b64_tr_b16 a[68:69], %[t0] offset:57344\n\t"
      "ds_read_b64_tr_b16 a[70:71], %[t0] offset:58368\n\t"
      "ds_read_b64_tr_b16 a[72:73], %[t1] offset:49152\n\t"
      "ds_read_b64_tr_b16 a[74:75], %[t1] offset:50176\n\t"
      "ds_read_b64_tr_b16 a[76:77], %[t1] offset:57344\n\t"
      "ds_read_b64_tr_b16 a[78:79], %[t1] offset:58368\n\t"
      "s_waitcnt vmcnt(8) lgkmcnt(0)\n\t"
      "s_barrier\n\t"
      "s_add_u32 m0, %[lb], 49152\n\t"
      "s_mov_b32 s64, 1572864\n\t"
      "buffer_load_dwordx4 %[va], %[m0], s64 offen lds\n\t"
      "s_add_u32 m0, %[lb], 50176\n\t"
      "s_mov_b32 s64, 1605632\n\t"
      "buffer_load_dwordx4 %[va], %[m0], s64 offen lds\n\t"
      "s_add_u32 m0, %[lb], 51200\n\t"
      "s_mov_b32 s64, 1638400\n\t"
      "buffer_load_dwordx4 %[vb], %[m0], s64 offen lds\n\t"
      "s_add_u32 m0, %[lb], 52224\n\t"
      "s_mov_b32 s64, 1671168\n\t"
      "buffer_load_dwordx4 %[vb], %[m0], s64 offen lds\n\t"
      "ds_read_b64_tr_b16 a[192:193], %[t0] offset:0\n\t"
      "ds_read_b64_tr_b16 a[194:195], %[t0] offset:1024\n\t"
      "ds_read_b64_tr_b16 a[196:197], %[t0] offset:8192\n\t"
      "ds_read_b64_tr_b16 a[198:199], %[t0] offset:9216\n\t"
      "ds_read_b64_tr_b16 a[200:201], %[t1] offset:0\n\t"
      "ds_read_b64_tr_b16 a[202:203], %[t1] offset:1024\n\t"
      "ds_read_b64_tr_b16 a[204:205], %[t1] offset:8192\n\t"
      "ds_read_b64_tr_b16 a[206:207], %[t1] offset:9216\n\t"
      "s_waitcnt vmcnt(8) lgkmcnt(0)\n\t"
      "s_barrier\n\t"
      "s_add_u32 m0, %[lb], 0\n\t"
      "s_mov_b32 s64, 3670016\n\t"
      "buffer_load_dwordx4 %[va], %[m0], s64 offen lds\n\t"
      "s_add_u32 m0, %[lb], 1024\n\t"
      "s_mov_b32 s64, 3702784\n\t"
      "buffer_load_dwordx4 %[va], %[m0], s64 offen lds\n\t"
      "s_add_u32 m0, %[lb], 2048\n\t"
      "s_mov_b32 s64, 3735552\n\t"
      "buffer_load_dwordx4 %[vb], %[m0], s64 offen lds\n\t"
      "s_add_u32 m0, %[lb], 3072\n\t"
      "s_mov_b32 s64, 3768320\n\t"
      "buffer_load_dwordx4 %[vb], %[m0], s64 offen lds\n\t"
      "ds_read_b64_tr_b16 a[80:81], %[t0] offset:16384\n\t"
      "ds_read_b64_tr_b16 a[82:83], %[t0] offset:17408\n\t"
      "ds_read_b64_tr_b16 a[84:85], %[t0] offset:24576\n\t"
      "ds_read_b64_tr_b16 a[86:87], %[t0] offset:25600\n\t"
      "ds_read_b64_tr_b16 a[88:89], %[t1] offset:16384\n\t"
      "ds_read_b64_tr_b16 a[90:91], %[t1] offset:17408\n\t"
      "ds_read_b64_tr_b16 a[92:93], %[t1] offset:24576\n\t"
      "ds_read_b64_tr_b16 a[94:95], %[t1] offset:25600\n\t"
      "s_waitcnt vmcnt(8) lgkmcnt(0)\n\t"
      "s_barrier\n\t"
      "s_add_u32 m0, %[lb], 16384\n\t"
      "s_mov_b32 s64, 1572864\n\t"
      "buffer_load_dwordx4 %[va], %[m1], s64 offen lds\n\t"
      "s_add_u32 m0, %[lb], 17408\n\t"
      "s_mov_b32 s64, 1605632\n\t"
      "buffer_load_dwordx4 %[va], %[m1], s64 offen lds\n\t"
      "s_add_u32 m0, %[lb], 18432\n\t"
      "s_mov_b32 s64, 1638400\n\t"
      "buffer_load_dwordx4 %[vb], %[m1], s64 offen lds\n\t"
      "s_add_u32 m0, %[lb], 19456\n\t"
      "s_mov_b32 s64, 1671168\n\t"
      "buffer_load_dwordx4 %[vb], %[m1], s64 offen lds\n\t"
      "ds_read_b64_tr_b16 a[208:209], %[t0] offset:32768\n\t"
      "ds_read_b64_tr_b16 a[210:211], %[t0] offset:33792\n\t"
      "ds_read_b64_tr_b16 a[212:213], %[t0] offset:40960\n\t"
      "ds_read_b64_tr_b16 a[214:215], %[t0] offset:41984\n\t"
      "ds_read_b64_tr_b16 a[216:217], %[t1] offset:32768\n\t"
      "ds_read_b64_tr_b16 a[218:219], %[t1] offset:33792\n\t"
      "ds_read_b64_tr_b16 a[220:221], %[t1] offset:40960\n\t"
      "ds_read_b64_tr_b16 a[222:223], %[t1] offset:41984\n\t"
      "s_waitcnt vmcnt(8) lgkmcnt(0)\n\t"
      "s_barrier\n\t"
      "s_add_u32 m0, %[lb], 32768\n\t"
      "s_mov_b32 s64, 3670016\n\t"
      "buffer_load_dwordx4 %[va], %[m1], s64 offen lds\n\t"
      "s_add_u32 m0, %[lb], 33792\n\t"
      "s_mov_b32 s64, 3702784\n\t"
      "buffer_load_dwordx4 %[va], %[m1], s64 offen lds\n\t"
      "s_add_u32 m0, %[lb], 34816\n\t"
      "s_mov_b32 s64, 3735552\n\t"
      "buffer_load_dwordx4 %[vb], %[m1], s64 offen lds\n\t"
      "s_add_u32 m0, %[lb], 35840\n\t"
      "s_mov_b32 s64, 3768320\n\t"
      "buffer_load_dwordx4 %[vb], %[m1], s64 offen lds\n\t"
      "ds_read_b64_tr_b16 a[96:97], %[t0] offset:49152\n\t"
      "ds_read_b64_tr_b16 a[98:99], %[t0] offset:50176\n\t"
      "ds_read_b64_tr_b16 a[100:101], %[t0] offset:57344\n\t"
      "ds_read_b64_tr_b16 a[102:103], %[t0] offset:58368\n\t"
      "ds_read_b64_tr_b16 a[104:105], %[t1] offset:49152\n\t"
      "ds_read_b64_tr_b16 a[106:107], %[t1] offset:50176\n\t"
      "ds_read_b64_tr_b16 a[108:109], %[t1] offset:57344\n\t"
      "ds_read_b64_tr_b16 a[110:111], %[t1] offset:58368\n\t"
      "s_waitcnt vmcnt(8) lgkmcnt(0)\n\t"
      "s_barrier\n\t"
      "s_add_u32 m0, %[lb], 49152\n\t"
      "s_mov_b32 s64, 2097152\n\t"
      "buffer_load_dwordx4 %[va], %[m0], s64 offen lds\n\t"
      "s_add_u32 m0, %[lb], 50176\n\t"
      "s_mov_b32 s64, 2129920\n\t"
      "buffer_load_dwordx4 %[va], %[m0], s64 offen lds\n\t"
      "s_add_u32 m0, %[lb], 51200\n\t"
      "s_mov_b32 s64, 2162688\n\t"
      "buffer_load_dwordx4 %[vb], %[m0], s64 offen lds\n\t"
      "s_add_u32 m0, %[lb], 52224\n\t"
      "s_mov_b32 s64, 2195456\n\t"
      "buffer_load_dwordx4 %[vb], %[m0], s64 offen lds\n\t"
      "ds_read_b64_tr_b16 a[224:225], %[t0] offset:0\n\t"
      "ds_read_b64_tr_b16 a[226:227], %[t0] offset:1024\n\t"
      "ds_read_b64_tr_b16 a[228:229], %[t0] offset:8192\n\t"
      "ds_read_b64_tr_b16 a[230:231], %[t0] offset:9216\n\t"
      "ds_read_b64_tr_b16 a[232:233], %[t1] offset:0\n\t"
      "ds_read_b64_tr_b16 a[234:235], %[t1] offset:1024\n\t"
      "ds_read_b64_tr_b16 a[236:237], %[t1] offset:8192\n\t"
      "ds_read_b64_tr_b16 a[238:239], %[t1] offset:9216\n\t"
      "s_waitcnt vmcnt(8) lgkmcnt(0)\n\t"
      "s_barrier\n\t"
      "s_add_u32 m0, %[lb], 0\n\t"
      "s_mov_b32 s64, 4194304\n\t"
      "buffer_load_dwordx4 %[va], %[m0], s64 offen lds\n\t"
      "s_add_u32 m0, %[lb], 1024\n\t"
      "s_mov_b32 s64, 4227072\n\t"
      "buffer_load_dwordx4 %[va], %[m0], s64 offen lds\n\t"
      "s_add_u32 m0, %[lb], 2048\n\t"
      "s_mov_b32 s64, 4259840\n\t"
      "buffer_load_dwordx4 %[vb], %[m0], s64 offen lds\n\t"
      "s_add_u32 m0, %[lb], 3072\n\t"
      "s_mov_b32 s64, 4292608\n\t"
      "buffer_load_dwordx4 %[vb], %[m0], s64 offen lds\n\t"
      "ds_read_b64_tr_b16 a[112:113], %[t0] offset:16384\n\t"
      "ds_read_b64_tr_b16 a[114:115], %[t0] offset:17408\n\t"
      "ds_read_b64_tr_b16 a[116:117], %[t0] offset:24576\n\t"
      "ds_read_b64_tr_b16 a[118:119], %[t0] offset:25600\n\t"
      "ds_read_b64_tr_b16 a[120:121], %[t1] offset:16384\n\t"
      "ds_read_b64_tr_b16 a[122:123], %[t1] offset:17408\n\t"
      "ds_read_b64_tr_b16 a[124:125], %[t1] offset:24576\n\t"
      "ds_read_b64_tr_b16 a[126:127], %[t1] offset:25600\n\t"
      "s_waitcnt vmcnt(8) lgkmcnt(0)\n\t"
      "s_barrier\n\t"
      "s_add_u32 m0, %[lb], 16384\n\t"
      "s_mov_b32 s64, 4194304\n\t"
      "buffer_load_dwordx4 %[va], %[m1], s64 offen lds\n\t"
      "s_add_u32 m0, %[lb], 17408\n\t"
      "s_mov_b32 s64, 4227072\n\t"
      "buffer_load_dwordx4 %[va], %[m1], s64 offen lds\n\t"
      "s_add_u32 m0, %[lb], 18432\n\t"
      "s_mov_b32 s64, 4259840\n\t"
      "buffer_load_dwordx4 %[vb], %[m1], s64 offen lds\n\t"
      "s_add_u32 m0, %[lb], 19456\n\t"
      "s_mov_b32 s64, 4292608\n\t"
      "buffer_load_dwordx4 %[vb], %[m1], s64 offen lds\n\t"
      "ds_read_b64_tr_b16 a[240:241], %[t0] offset:32768\n\t"
      "ds_read_b64_tr_b16 a[242:243], %[t0] offset:33792\n\t"
      "ds_read_b64_tr_b16 a[244:245], %[t0] offset:40960\n\t"
      "ds_read_b64_tr_b16 a[246:247], %[t0] offset:41984\n\t"
      "ds_read_b64_tr_b16 a[248:249], %[t1] offset:32768\n\t"
      "ds_read_b64_tr_b16 a[250:251], %[t1] offset:33792\n\t"
      "ds_read_b64_tr_b16 a[252:253], %[t1] offset:40960\n\t"
      "ds_read_b64_tr_b16 a[254:255], %[t1] offset:41984\n\t"
      "s_waitcnt vmcnt(8) lgkmcnt(0)\n\t"
      "s_barrier\n\t"
      "ds_read_b64_tr_b16 a[128:129], %[t0] offset:49152\n\t"
      "ds_read_b64_tr_b16 a[130:131], %[t0] offset:50176\n\t"
      "ds_read_b64_tr_b16 a[132:133], %[t0] offset:57344\n\t"
      "ds_read_b64_tr_b16 a[134:135], %[t0] offset:58368\n\t"
      "ds_read_b64_tr_b16 a[136:137], %[t1] offset:49152\n\t"
      "ds_read_b64_tr_b16 a[138:139], %[t1] offset:50176\n\t"
      "ds_read_b64_tr_b16 a[140:141], %[t1] offset:57344\n\t"
      "ds_read_b64_tr_b16 a[142:143], %[t1] offset:58368\n\t"
      "s_waitcnt vmcnt(4) lgkmcnt(0)\n\t"
      "s_barrier\n\t"
      "ds_read_b64_tr_b16 v[224:225], %[t0] offset:0\n\t"
      "ds_read_b64_tr_b16 v[226:227], %[t0] offset:1024\n\t"
      "ds_read_b64_tr_b16 v[228:229], %[t0] offset:8192\n\t"
      "ds_read_b64_tr_b16 v[230:231], %[t0] offset:9216\n\t"
      "ds_read_b64_tr_b16 v[232:233], %[t1] offset:0\n\t"
      "ds_read_b64_tr_b16 v[234:235], %[t1] offset:1024\n\t"
      "ds_read_b64_tr_b16 v[236:237], %[t1] offset:8192\n\t"
      "ds_read_b64_tr_b16 v[238:239], %[t1] offset:9216\n\t"
      "s_waitcnt vmcnt(0) lgkmcnt(0)\n\t"
      "s_barrier\n\t"
      "ds_read_b64_tr_b16 v[240:241], %[t0] offset:16384\n\t"
      "ds_read_b64_tr_b16 v[242:243], %[t0] offset:17408\n\t"
      "ds_read_b64_tr_b16 v[244:245], %[t0] offset:24576\n\t"
      "ds_read_b64_tr_b16 v[246:247], %[t0] offset:25600\n\t"
      "ds_read_b64_tr_b16 v[248:249], %[t1] offset:16384\n\t"
      "ds_read_b64_tr_b16 v[250:251], %[t1] offset:17408\n\t"
      "ds_read_b64_tr_b16 v[252:253], %[t1] offset:24576\n\t"
      "ds_read_b64_tr_b16 v[254:255], %[t1] offset:25600\n\t"
      "s_waitcnt lgkmcnt(0)\n\t"
      "s_barrier\n\t"
      "s_mov_b32 m0, s65\n\t"
      : WOUT(W) : [m0] "s"(s0), [m1] "s"(s1), [va] "v"(va), [vb] "v"(vb), [t0] "v"(t0), [t1] "v"(t1), [lb] "s"(US(lb)) : "s64", "s65", "scc", "memory");
  else asm volatile(
      "s_mov_b32 s65, m0\n\t"
      "s_add_u32 m0, %[lb], 0\n\t"
      "s_mov_b32 s64, 0\n\t"
      "buffer_load_dwordx4 %[va], %[m0], s64 offen lds\n\t"
      "s_add_u32 m0, %[lb], 1024\n\t"
      "s_mov_b32 s64, 36864\n\t"
      "buffer_load_dwordx4 %[va], %[m0], s64 offen lds\n\t"
      "s_add_u32 m0, %[lb], 2048\n\t"
      "s_mov_b32 s64, 73728\n\t"
      "buffer_load_dwordx4 %[vb], %[m0], s64 offen lds\n\t"
      "s_add_u32 m0, %[lb], 3072\n\t"
      "s_mov_b32 s64, 110592\n\t"
      "buffer_load_dwordx4 %[vb], %[m0], s64 offen lds\n\t"
      "s_add_u32 m0, %[lb], 16384\n\t"
      "s_mov_b32 s64, 0\n\t"
      "buffer_load_dwordx4 %[va], %[m1], s64 offen lds\n\t"
      "s_add_u32 m0, %[lb], 17408\n\t"
      "s_mov_b32 s64, 36864\n\t"
      "buffer_load_dwordx4 %[va], %[m1], s64 offen lds\n\t"
      "s_add_u32 m0, %[lb], 18432\n\t"
      "s_mov_b32 s64, 73728\n\t"
      "buffer_load_dwordx4 %[vb], %[m1], s64 offen lds\n\t"
      "s_add_u32 m0, %[lb], 19456\n\t"
      "s_mov_b32 s64, 110592\n\t"
      "buffer_load_dwordx4 %[vb], %[m1], s64 offen lds\n\t"
      "s_add_u32 m0, %[lb], 32768\n\t"
      "s_mov_b32 s64, 2359296\n\t"
      "buffer_load_dwordx4 %[va], %[m1], s64 offen lds\n\t"
      "s_add_u32 m0, %[lb], 33792\n\t"
      "s_mov_b32 s64, 2396160\n\t"
      "buffer_load_dwordx4 %[va], %[m1], s64 offen lds\n\t"
      "s_add_u32 m0, %[lb], 34816\n\t"
      "s_mov_b32 s64, 2433024\n\t"
      "buffer_load_dwordx4 %[vb], %[m1], s64 offen lds\n\t"
      "s_add_u32 m0, %[lb], 35840\n\t"
      "s_mov_b32 s64, 2469888\n\t"
      "buffer_load_dwordx4 %[vb], %[m1], s64 offen lds\n\t"
      "s_waitcnt vmcnt(8) lgkmcnt(0)\n\t"
      "s_barrier\n\t"
      "s_add_u32 m0, %[lb], 49152\n\t"
      "s_mov_b32 s64, 589824\n\t"
      "buffer_load_dwordx4 %[va], %[m0], s64 offen lds\n\t"
      "s_add_u32 m0, %[lb], 50176\n\t"
      "s_mov_b32 s64, 626688\n\t"
      "buffer_load_dwordx4 %[va], %[m0], s64 offen lds\n\t"
      "s_add_u32 m0, %[lb], 51200\n\t"
      "s_mov_b32 s64, 663552\n\t"
      "buffer_load_dwordx4 %[vb], %[m0], s64 offen lds\n\t"
      "s_add_u32 m0, %[lb], 52224\n\t"
      "s_mov_b32 s64, 700416\n\t"
      "buffer_load_dwordx4 %[vb], %[m0], s64 offen lds\n\t"
      "ds_read_b64_tr_b16 a[0:1], %[t0] offset:0\n\t"
      "ds_read_b64_tr_b16 a[2:3], %[t0] offset:1024\n\t"
      "ds_read_b64_tr_b16 a[4:5], %[t0] offset:8192\n\t"
      "ds_read_b64_tr_b16 a[6:7], %[t0] offset:9216\n\t"
      "ds_read_b64_tr_b16 a[8:9], %[t1] offset:0\n\t"
      "ds_read_b64_tr_b16 a[10:11], %[t1] offset:1024\n\t"
      "ds_read_b64_tr_b16 a[12:13], %[t1] offset:8192\n\t"
      "ds_read_b64_tr_b16 a[14:15], %[t1] offset:9216\n\t"
      "s_waitcnt vmcnt(8) lgkmcnt(0)\n\t"
      "s_barrier\n\t"
      "s_add_u32 m0, %[lb], 0\n\t"
      "s_mov_b32 s64, 2949120\n\t"
      "buffer_load_dwordx4 %[va], %[m0], s64 offen lds\n\t"
      "s_add_u32 m0, %[lb], 1024\n\t"
      "s_mov_b32 s64, 2985984\n\t"
      "buffer_load_dwordx4 %[va], %[m0], s64 offen lds\n\t"
      "s_add_u32 m0, %[lb], 2048\n\t"
      "s_mov_b32 s64, 3022848\n\t"
      "buffer_load_dwordx4 %[vb], %[m0], s64 offen lds\n\t"
      "s_add_u32 m0, %[lb], 3072\n\t"
      "s_mov_b32 s64, 3059712\n\t"
      "buffer_load_dwordx4 %[vb], %[m0], s64 offen lds\n\t"
      "ds_read_b64_tr_b16 a[16:17], %[t0] offset:16384\n\t"
      "ds_read_b64_tr_b16 a[18:19], %[t0] offset:17408\n\t"
      "ds_read_b64_tr_b16 a[20:21], %[t0] offset:24576\n\t"
      "ds_read_b64_tr_b16 a[22:23], %[t0] offset:25600\n\t"
      "ds_read_b64_tr_b16 a[24:25], %[t1] offset:16384\n\t"
      "ds_read_b64_tr_b16 a[26:27], %[t1] offset:17408\n\t"
      "ds_read_b64_tr_b16 a[28:29], %[t1] offset:24576\n\t"
      "ds_read_b64_tr_b16 a[30:31], %[t1] offset:25600\n\t"
      "s_waitcnt vmcnt(8) lgkmcnt(0)\n\t"
      "s_barrier\n\t"
      "s_add_u32 m0, %[lb], 16384\n\t"
      "s_mov_b32 s64, 589824\n\t"
      "buffer_load_dwordx4 %[va], %[m1], s64 offen lds\n\t"
      "s_add_u32 m0, %[lb], 17408\n\t"
      "s_mov_b32 s64, 626688\n\t"
      "buffer_load_dwordx4 %[va], %[m1], s64 offen lds\n\t"
      "s_add_u32 m0, %[lb], 18432\n\t"
      "s_mov_b32 s64, 663552\n\t"
      "buffer_load_dwordx4 %[vb], %[m1], s64 offen lds\n\t"
      "s_add_u32 m0, %[lb], 19456\n\t"
      "s_mov_b32 s64, 700416\n\t"
      "buffer_load_dwordx4 %[vb], %[m1], s64 offen lds\n\t"
      "ds_read_b64_tr_b16 a[144:145], %[t0] offset:32768\n\t"
      "ds_read_b64_tr_b16 a[146:147], %[t0] offset:33792\n\t"
      "ds_read_b64_tr_b16 a[148:149], %[t0] offset:40960\n\t"
      "ds_read_b64_tr_b16 a[150:151], %[t0] offset:41984\n\t"
      "ds_read_b64_tr_b16 a[152:153], %[t1] offset:32768\n\t"
      "ds_read_b64_tr_b16 a[154:155], %[t1] offset:33792\n\t"
      "ds_read_b64_tr_b16 a[156:157], %[t1] offset:40960\n\t"
      "ds_read_b64_tr_b16 a[158:159], %[t1] offset:41984\n\t"
      "s_waitcnt vmcnt(8) lgkmcnt(0)\n\t"
      "s_barrier\n\t"
      "s_add_u32 m0, %[lb], 32768\n\t"
      "s_mov_b32 s64, 2949120\n\t"
      "buffer_load_dwordx4 %[va], %[m1], s64 offen lds\n\t"
      "s_add_u32 m0, %[lb], 33792\n\t"
      "s_mov_b32 s64, 2985984\n\t"
      "buffer_load_dwordx4 %[va], %[m1], s64 offen lds\n\t"
      "s_add_u32 m0, %[lb], 34816\n\t"
      "s_mov_b32 s64, 3022848\n\t"
      "buffer_load_dwordx4 %[vb], %[m1], s64 offen lds\n\t"
      "s_add_u32 m0, %[lb], 35840\n\t"
      "s_mov_b32 s64, 3059712\n\t"
      "buffer_load_dwordx4 %[vb], %[m1], s64 offen lds\n\t"
      "ds_read_b64_tr_b16 a[32:33], %[t0] offset:49152\n\t"
      "ds_read_b64_tr_b16 a[34:35], %[t0] offset:50176\n\t"
      "ds_read_b64_tr_b16 a[36:37], %[t0] offset:57344\n\t"
      "ds_read_b64_tr_b16 a[38:39], %[t0] offset:58368\n\t"
      "ds_read_b64_tr_b16 a[40:41], %[t1] offset:49152\n\t"
      "ds_read_b64_tr_b16 a[42:43], %[t1] offset:50176\n\t"
      "ds_read_b64_tr_b16 a[44:45], %[t1] offset:57344\n\t"
      "ds_read_b64_tr_b16 a[46:47], %[t1] offset:58368\n\t"
      "s_waitcnt vmcnt(8) lgkmcnt(0)\n\t"
      "s_barrier\n\t"
      "s_add_u32 m0, %[lb], 49152\n\t"
      "s_mov_b32 s64, 1179648\n\t"
      "buffer_load_dwordx4 %[va], %[m0], s64 offen lds\n\t"
      "s_add_u32 m0, %[lb], 50176\n\t"
      "s_mov_b32 s64, 1216512\n\t"
      "buffer_load_dwordx4 %[va], %[m0], s64 offen lds\n\t"
      "s_add_u32 m0, %[lb], 51200\n\t"
      "s_mov_b32 s64, 1253376\n\t"
      "buffer_load_dwordx4 %[vb], %[m0], s64 offen lds\n\t"
      "s_add_u32 m0, %[lb], 52224\n\t"
      "s_mov_b32 s64, 1290240\n\t"
      "buffer_load_dwordx4 %[vb], %[m0], s64 offen lds\n\t"
      "ds_read_b64_tr_b16 a[160:161], %[t0] offset:0\n\t"
      "ds_read_b64_tr_b16 a[162:163], %[t0] offset:1024\n\t"
      "ds_read_b64_tr_b16 a[164:165], %[t0] offset:8192\n\t"
      "ds_read_b64_tr_b16 a[166:167], %[t0] offset:9216\n\t"
      "ds_read_b64_tr_b16 a[168:169], %[t1] offset:0\n\t"
      "ds_read_b64_tr_b16 a[170:171], %[t1] offset:1024\n\t"
      "ds_read_b64_tr_b16 a[172:173], %[t1] offset:8192\n\t"
      "ds_read_b64_tr_b16 a[174:175], %[t1] offset:9216\n\t"
      "s_waitcnt vmcnt(8) lgkmcnt(0)\n\t"
      "s_barrier\n\t"
      "s_add_u32 m0, %[lb], 0\n\t"
      "s_mov_b32 s64, 3538944\n\t"
      "buffer_load_dwordx4 %[va], %[m0], s64 offen lds\n\t"
      "s_add_u32 m0, %[lb], 1024\n\t"
      "s_mov_b32 s64, 3575808\n\t"
      "buffer_load_dwordx4 %[va], %[m0], s64 offen lds\n\t"
      "s_add_u32 m0, %[lb], 2048\n\t"
      "s_mov_b32 s64, 3612672\n\t"
      "buffer_load_dwordx4 %[vb], %[m0], s64 offen lds\n\t"
      "s_add_u32 m0, %[lb], 3072\n\t"
      "s_mov_b32 s64, 3649536\n\t"
      "buffer_load_dwordx4 %[vb], %[m0], s64 offen lds\n\t"
      "ds_read_b64_tr_b16 a[48:49], %[t0] offset:16384\n\t"
      "ds_read_b64_tr_b16 a[50:51], %[t0] offset:17408\n\t"
      "ds_read_b64_tr_b16 a[52:53], %[t0] offset:24576\n\t"
      "ds_read_b64_tr_b16 a[54:55], %[t0] offset:25600\n\t"
      "ds_read_b64_tr_b16 a[56:57], %[t1] offset:16384\n\t"
      "ds_read_b64_tr_b16 a[58:59], %[t1] offset:17408\n\t"
      "ds_read_b64_tr_b16 a[60:61], %[t1] offset:24576\n\t"
      "ds_read_b64_tr_b16 a[62:63], %[t1] offset:25600\n\t"
      "s_waitcnt vmcnt(8) lgkmcnt(0)\n\t"
      "s_barrier\n\t"
      "s_add_u32 m0, %[lb], 16384\n\t"
      "s_mov_b32 s64, 1179648\n\t"
      "buffer_load_dwordx4 %[va], %[m1], s64 offen lds\n\t"
      "s_add_u32 m0, %[lb], 17408\n\t"
      "s_mov_b32 s64, 1216512\n\t"
      "buffer_load_dwordx4 %[va], %[m1], s64 offen lds\n\t"
      "s_add_u32 m0, %[lb], 18432\n\t"
      "s_mov_b32 s64, 1253376\n\t"
      "buffer_load_dwordx4 %[vb], %[m1], s64 offen lds\n\t"
      "s_add_u32 m0, %[lb], 19456\n\t"
      "s_mov_b32 s64, 1290240\n\t"
      "buffer_load_dwordx4 %[vb], %[m1], s64 offen lds\n\t"
      "ds_read_b64_tr_b16 a[176:177], %[t0] offset:32768\n\t"
      "ds_read_b64_tr_b16 a[178:179], %[t0] offset:33792\n\t"
      "ds_read_b64_tr_b16 a[180:181], %[t0] offset:40960\n\t"
      "ds_read_b64_tr_b16 a[182:183], %[t0] offset:41984\n\t"
      "ds_read_b64_tr_b16 a[184:185], %[t1] offset:32768\n\t"
      "ds_read_b64_tr_b16 a[186:187], %[t1] offset:33792\n\t"
      "ds_read_b64_tr_b16 a[188:189], %[t1] offset:40960\n\t"
      "ds_read_b64_tr_b16 a[190:191], %[t1] offset:41984\n\t"
      "s_waitcnt vmcnt(8) lgkmcnt(0)\n\t"
      "s_barrier\n\t"
      "s_add_u32 m0, %[lb], 32768\n\t"
      "s_mov_b32 s64, 3538944\n\t"
      "buffer_load_dwordx4 %[va], %[m1], s64 offen lds\n\t"
      "s_add_u32 m0, %[lb], 33792\n\t"
      "s_mov_b32 s64, 3575808\n\t"
      "buffer_load_dwordx4 %[va], %[m1], s64 offen lds\n\t"
      "s_add_u32 m0, %[lb], 34816\n\t"
      "s_mov_b32 s64, 3612672\n\t"
      "buffer_load_dwordx4 %[vb], %[m1], s64 offen lds\n\t"
      "s_add_u32 m0, %[lb], 35840\n\t"
      "s_mov_b32 s64, 3649536\n\t"
      "buffer_load_dwordx4 %[vb], %[m1], s64 offen lds\n\t"
      "ds_read_b64_tr_b16 a[64:65], %[t0] offset:49152\n\t"
      "ds_read_b64_tr_b16 a[66:67], %[t0] offset:50176\n\t"
      "ds_read_b64_tr_b16 a[68:69], %[t0] offset:57344\n\t"
      "ds_read_b64_tr_b16 a[70:71], %[t0] offset:58368\n\t"
      "ds_read_b64_tr_b16 a[72:73], %[t1] offset:49152\n\t"
      "ds_read_b64_tr_b16 a[74:75], %[t1] offset:50176\n\t"
      "ds_read_b64_tr_b16 a[76:77], %[t1] offset:57344\n\t"
      "ds_read_b64_tr_b16 a[78:79], %[t1] offset:58368\n\t"
      "s_waitcnt vmcnt(8) lgkmcnt(0)\n\t"
      "s_barrier\n\t"
      "s_add_u32 m0, %[lb], 49152\n\t"
      "s_mov_b32 s64, 1769472\n\t"
      "buffer_load_dwordx4 %[va], %[m0], s64 offen lds\n\t"
      "s_add_u32 m0, %[lb], 50176\n\t"
      "s_mov_b32 s64, 1806336\n\t"
      "buffer_load_dwordx4 %[va], %[m0], s64 offen lds\n\t"
      "s_add_u32 m0, %[lb], 51200\n\t"
      "s_mov_b32 s64, 1843200\n\t"
      "buffer_load_dwordx4 %[vb], %[m0], s64 offen lds\n\t"
      "s_add_u32 m0, %[lb], 52224\n\t"
      "s_mov_b32 s64, 1880064\n\t"
      "buffer_load_dwordx4 %[vb], %[m0], s64 offen lds\n\t"
      "ds_read_b64_tr_b16 a[192:193], %[t0] offset:0\n\t"
      "ds_read_b64_tr_b16 a[194:195], %[t0] offset:1024\n\t"
      "ds_read_b64_tr_b16 a[196:197], %[t0] offset:8192\n\t"
      "ds_read_b64_tr_b16 a[198:199], %[t0] offset:9216\n\t"
      "ds_read_b64_tr_b16 a[200:201], %[t1] offset:0\n\t"
      "ds_read_b64_tr_b16 a[202:203], %[t1] offset:1024\n\t"
      "ds_read_b64_tr_b16 a[204:205], %[t1] offset:8192\n\t"
      "ds_read_b64_tr_b16 a[206:207], %[t1] offset:9216\n\t"
      "s_waitcnt vmcnt(8) lgkmcnt(0)\n\t"
      "s_barrier\n\t"
      "s_add_u32 m0, %[lb], 0\n\t"
      "s_mov_b32 s64, 4128768\n\t"
      "buffer_load_dwordx4 %[va], %[m0], s64 offen lds\n\t"
      "s_add_u32 m0, %[lb], 1024\n\t"
      "s_mov_b32 s64, 4165632\n\t"
      "buffer_load_dwordx4 %[va], %[m0], s64 offen lds\n\t"
      "s_add_u32 m0, %[lb], 2048\n\t"
      "s_mov_b32 s64, 4202496\n\t"
      "buffer_load_dwordx4 %[vb], %[m0], s64 offen lds\n\t"
      "s_add_u32 m0, %[lb], 3072\n\t"
      "s_mov_b32 s64, 4239360\n\t"
      "buffer_load_dwordx4 %[vb], %[m0], s64 offen lds\n\t"
      "ds_read_b64_tr_b16 a[80:81], %[t0] offset:16384\n\t"
      "ds_read_b64_tr_b16 a[82:83], %[t0] offset:17408\n\t"
      "ds_read_b64_tr_b16 a[84:85], %[t0] offset:24576\n\t"
      "ds_read_b64_tr_b16 a[86:87], %[t0] offset:25600\n\t"
      "ds_read_b64_tr_b16 a[88:89], %[t1] offset:16384\n\t"
      "ds_read_b64_tr_b16 a[90:91], %[t1] offset:17408\n\t"
      "ds_read_b64_tr_b16 a[92:93], %[t1] offset:24576\n\t"
      "ds_read_b64_tr_b16 a[94:95], %[t1] offset:25600\n\t"
      "s_waitcnt vmcnt(8) lgkmcnt(0)\n\t"
      "s_barrier\n\t"
      "s_add_u32 m0, %[lb], 16384\n\t"
      "s_mov_b32 s64, 1769472\n\t"
      "buffer_load_dwordx4 %[va], %[m1], s64 offen lds\n\t"
      "s_add_u32 m0, %[lb], 17408\n\t"
      "s_mov_b32 s64, 1806336\n\t"
      "buffer_load_dwordx4 %[va], %[m1], s64 offen lds\n\t"
      "s_add_u32 m0, %[lb], 18432\n\t"
      "s_mov_b32 s64, 1843200\n\t"
      "buffer_load_dwordx4 %[vb], %[m1], s64 offen lds\n\t"
      "s_add_u32 m0, %[lb], 19456\n\t"
      "s_mov_b32 s64, 1880064\n\t"
      "buffer_load_dwordx4 %[vb], %[m1], s64 offen lds\n\t"
      "ds_read_b64_tr_b16 a[208:209], %[t0] offset:32768\n\t"
      "ds_read_b64_tr_b16 a[210:211], %[t0] offset:33792\n\t"
      "ds_read_b64_tr_b16 a[212:213], %[t0] offset:40960\n\t"
      "ds_read_b64_tr_b16 a[214:215], %[t0] offset:41984\n\t"
      "ds_read_b64_tr_b16 a[216:217], %[t1] offset:32768\n\t"
      "ds_read_b64_tr_b16 a[218:219], %[t1] offset:33792\n\t"
      "ds_read_b64_tr_b16 a[220:221], %[t1] offset:40960\n\t"
      "ds_read_b64_tr_b16 a[222:223], %[t1] offset:41984\n\t"
      "s_waitcnt vmcnt(8) lgkmcnt(0)\n\t"
      "s_barrier\n\t"
      "s_add_u32 m0, %[lb], 32768\n\t"
      "s_mov_b32 s64, 4128768\n\t"
      "buffer_load_dwordx4 %[va], %[m1], s64 offen lds\n\t"
      "s_add_u32 m0, %[lb], 33792\n\t"
      "s_mov_b32 s64, 4165632\n\t"
      "buffer_load_dwordx4 %[va], %[m1], s64 offen lds\n\t"
      "s_add_u32 m0, %[lb], 34816\n\t"
      "s_mov_b32 s64, 4202496\n\t"
      "buffer_load_dwordx4 %[vb], %[m1], s64 offen lds\n\t"
      "s_add_u32 m0, %[lb], 35840\n\t"
      "s_mov_b32 s64, 4239360\n\t"
      "buffer_load_dwordx4 %[vb], %[m1], s64 offen lds\n\t"
      "ds_read_b64_tr_b16 a[96:97], %[t0] offset:49152\n\t"
      "ds_read_b64_tr_b16 a[98:99], %[t0] offset:50176\n\t"
      "ds_read_b64_tr_b16 a[100:101], %[t0] offset:57344\n\t"
      "ds_read_b64_tr_b16 a[102:103], %[t0] offset:58368\n\t"
      "ds_read_b64_tr_b16 a[104:105], %[t1] offset:49152\n\t"
      "ds_read_b64_tr_b16 a[106:107], %[t1] offset:50176\n\t"
      "ds_read_b64_tr_b16 a[108:109], %[t1] offset:57344\n\t"
      "ds_read_b64_tr_b16 a[110:111], %[t1] offset:58368\n\t"
      "s_waitcnt vmcnt(8) lgkmcnt(0)\n\t"
      "s_barrier\n\t"
      "s_add_u32 m0, %[lb], 49152\n\t"
      "s_mov_b32 s64, 2359296\n\t"
      "buffer_load_dwordx4 %[va], %[m0], s64 offen lds\n\t"
      "s_add_u32 m0, %[lb], 50176\n\t"
      "s_mov_b32 s64, 2396160\n\t"
      "buffer_load_dwordx4 %[va], %[m0], s64 offen lds\n\t"
      "s_add_u32 m0, %[lb], 51200\n\t"
      "s_mov_b32 s64, 2433024\n\t"
      "buffer_load_dwordx4 %[vb], %[m0], s64 offen lds\n\t"
      "s_add_u32 m0, %[lb], 52224\n\t"
      "s_mov_b32 s64, 2469888\n\t"
      "buffer_load_dwordx4 %[vb], %[m0], s64 offen lds\n\t"
      "ds_read_b64_tr_b16 a[224:225], %[t0] offset:0\n\t"
      "ds_read_b64_tr_b16 a[226:227], %[t0] offset:1024\n\t"
      "ds_read_b64_tr_b16 a[228:229], %[t0] offset:8192\n\t"
      "ds_read_b64_tr_b16 a[230:231], %[t0] offset:9216\n\t"
      "ds_read_b64_tr_b16 a[232:233], %[t1] offset:0\n\t"
      "ds_read_b64_tr_b16 a[234:235], %[t1] offset:1024\n\t"
      "ds_read_b64_tr_b16 a[236:237], %[t1] offset:8192\n\t"
      "ds_read_b64_tr_b16 a[238:239], %[t1] offset:9216\n\t"
      "s_waitcnt vmcnt(8) lgkmcnt(0)\n\t"
      "s_barrier\n\t"
      "s_add_u32 m0, %[lb], 0\n\t"
      "s_mov_b32 s64, 4718592\n\t"
      "buffer_load_dwordx4 %[va], %[m0], s64 offen lds\n\t"
      "s_add_u32 m0, %[lb], 1024\n\t"
      "s_mov_b32 s64, 4755456\n\t"
      "buffer_load_dwordx4 %[va], %[m0], s64 offen lds\n\t"
      "s_add_u32 m0, %[lb], 2048\n\t"
      "s_mov_b32 s64, 4792320\n\t"
      "buffer_load_dwordx4 %[vb], %[m0], s64 offen lds\n\t"
      "s_add_u32 m0, %[lb], 3072\n\t"
      "s_mov_b32 s64, 4829184\n\t"
      "buffer_load_dwordx4 %[vb], %[m0], s64 offen lds\n\t"
      "ds_read_b64_tr_b16 a[112:113], %[t0] offset:16384\n\t"
      "ds_read_b64_tr_b16 a[114:115], %[t0] offset:17408\n\t"
      "ds_read_b64_tr_b16 a[116:117], %[t0] offset:24576\n\t"
      "ds_read_b64_tr_b16 a[118:119], %[t0] offset:25600\n\t"
      "ds_read_b64_tr_b16 a[120:121], %[t1] offset:16384\n\t"
      "ds_read_b64_tr_b16 a[122:123], %[t1] offset:17408\n\t"
      "ds_read_b64_tr_b16 a[124:125], %[t1] offset:24576\n\t"
      "ds_read_b64_tr_b16 a[126:127], %[t1] offset:25600\n\t"
      "s_waitcnt vmcnt(8) lgkmcnt(0)\n\t"
      "s_barrier\n\t"
      "s_add_u32 m0, %[lb], 16384\n\t"
      "s_mov_b32 s64, 4718592\n\t"
      "buffer_load_dwordx4 %[va], %[m1], s64 offen lds\n\t"
      "s_add_u32 m0, %[lb], 17408\n\t"
      "s_mov_b32 s64, 4755456\n\t"
      "buffer_load_dwordx4 %[va], %[m1], s64 offen lds\n\t"
      "s_add_u32 m0, %[lb], 18432\n\t"
      "s_mov_b32 s64, 4792320\n\t"
      "buffer_load_dwordx4 %[vb], %[m1], s64 offen lds\n\t"
      "s_add_u32 m0, %[lb], 19456\n\t"
      "s_mov_b32 s64, 4829184\n\t"
      "buffer_load_dwordx4 %[vb], %[m1], s64 offen lds\n\t"
      "ds_read_b64_tr_b16 a[240:241], %[t0] offset:32768\n\t"
      "ds_read_b64_tr_b16 a[242:243], %[t0] offset:33792\n\t"
      "ds_read_b64_tr_b16 a[244:245], %[t0] offset:40960\n\t"
      "ds_read_b64_tr_b16 a[246:247], %[t0] offset:41984\n\t"
      "ds_read_b64_tr_b16 a[248:249], %[t1] offset:32768\n\t"
      "ds_read_b64_tr_b16 a[250:251], %[t1] offset:33792\n\t"
      "ds_read_b64_tr_b16 a[252:253], %[t1] offset:40960\n\t"
      "ds_read_b64_tr_b16 a[254:255], %[t1] offset:41984\n\t"
      "s_waitcnt vmcnt(8) lgkmcnt(0)\n\t"
      "s_barrier\n\t"
      "ds_read_b64_tr_b16 a[128:129], %[t0] offset:49152\n\t"
      "ds_read_b64_tr_b16 a[130:131], %[t0] offset:50176\n\t"
      "ds_read_b64_tr_b16 a[132:133], %[t0] offset:57344\n\t"
      "ds_read_b64_tr_b16 a[134:135], %[t0] offset:58368\n\t"
      "ds_read_b64_tr_b16 a[136:137], %[t1] offset:49152\n\t"
      "ds_read_b64_tr_b16 a[138:139], %[t1] offset:50176\n\t"
      "ds_read_b64_tr_b16 a[140:141], %[t1] offset:57344\n\t"
      "ds_read_b64_tr_b16 a[142:143], %[t1] offset:58368\n\t"
      "s_waitcnt vmcnt(4) lgkmcnt(0)\n\t"
      "s_barrier\n\t"
      "ds_read_b64_tr_b16 v[224:225], %[t0] offset:0\n\t"
      "ds_read_b64_tr_b16 v[226:227], %[t0] offset:1024\n\t"
      "ds_read_b64_tr_b16 v[228:229], %[t0] offset:8192\n\t"
      "ds_read_b64_tr_b16 v[230:231], %[t0] offset:9216\n\t"
      "ds_read_b64_tr_b16 v[232:233], %[t1] offset:0\n\t"
      "ds_read_b64_tr_b16 v[234:235], %[t1] offset:1024\n\t"
      "ds_read_b64_tr_b16 v[236:237], %[t1] offset:8192\n\t"
      "ds_read_b64_tr_b16 v[238:239], %[t1] offset:9216\n\t"
      "s_waitcnt vmcnt(0) lgkmcnt(0)\n\t"
      "s_barrier\n\t"
      "ds_read_b64_tr_b16 v[240:241], %[t0] offset:16384\n\t"
      "ds_read_b64_tr_b16 v[242:243], %[t0] offset:17408\n\t"
      "ds_read_b64_tr_b16 v[244:245], %[t0] offset:24576\n\t"
      "ds_read_b64_tr_b16 v[246:247], %[t0] offset:25600\n\t"
      "ds_read_b64_tr_b16 v[248:249], %[t1] offset:16384\n\t"
      "ds_read_b64_tr_b16 v[250:251], %[t1] offset:17408\n\t"
      "ds_read_b64_tr_b16 v[252:253], %[t1] offset:24576\n\t"
      "ds_read_b64_tr_b16 v[254:255], %[t1] offset:25600\n\t"
      "s_waitcnt lgkmcnt(0)\n\t"
      "s_barrier\n\t"
      "s_mov_b32 m0, s65\n\t"
      : WOUT(W) : [m0] "s"(s0), [m1] "s"(s1), [va] "v"(va), [vb] "v"(vb), [t0] "v"(t0), [t1] "v"(t1), [lb] "s"(US(lb)) : "s64", "s65", "scc", "memory");
}
template <int PERM> DI void load_wvo(u32v (&W)[9], const Params& p, int h, int w, int lane) {
  const u4 s0 = mksrd(p.Wv + h * 128, 575 * 8192 + 256), s1 = mksrd(p.Wo + (size_t)h * 128 * 576, 128 * 1152);
  const u32 va = psrc(8192, false, 0, w, lane), vb = psrc(8192, false, 1, w, lane), t0 = ptile<0>(32 * w, 0, lane), t1 = ptile<0>(32 * w + 16, 0, lane), lb = PBASE + w * 4096;
  const u32 wa = psrc(1152, false, 0, w, lane), wb = psrc(1152, false, 1, w, lane), pa = psrc(1152, true, 0, w, lane), pb = psrc(1152, true, 1, w, lane);
  const u32 u0 = ptile<PERM>(16 * w, 0, lane), u1 = ptile<PERM>(64 + 16 * w, 0, lane), u2_ = ptile<PERM>(16 * w, 1, lane), u3 = ptile<PERM>(64 + 16 * w, 1, lane);
  if constexpr (PERM == 1) asm volatile(
      "s_mov_b32 s65, m0\n\t"
      "s_add_u32 m0, %[lb], 0\n\t"
      "s_mov_b32 s64, 0\n\t"
      "buffer_load_dwordx4 %[va], %[m0], s64 offen lds\n\t"
      "s_add_u32 m0, %[lb], 1024\n\t"
      "s_mov_b32 s64, 32768\n\t"
      "buffer_load_dwordx4 %[va], %[m0], s64 offen lds\n\t"
      "s_add_u32 m0, %[lb], 2048\n\t"
      "s_mov_b32 s64, 65536\n\t"
      "buffer_load_dwordx4 %[vb], %[m0], s64 offen lds\n\t"
      "s_add_u32 m0, %[lb], 3072\n\t"
      "s_mov_b32 s64, 98304\n\t"
      "buffer_load_dwordx4 %[vb], %[m0], s64 offen lds\n\t"
      "s_add_u32 m0, %[lb], 16384\n\t"
      "s_mov_b32 s64, 524288\n\t"
      "buffer_load_dwordx4 %[va], %[m0], s64 offen lds\n\t"
      "s_add_u32 m0, %[lb], 17408\n\t"
      "s_mov_b32 s64, 557056\n\t"
      "buffer_load_dwordx4 %[va], %[m0], s64 offen lds\n\t"
      "s_add_u32 m0, %[lb], 18432\n\t"
      "s_mov_b32 s64, 589824\n\t"
      "buffer_load_dwordx4 %[vb], %[m0], s64 offen lds\n\t"
      "s_add_u32 m0, %[lb], 19456\n\t"
      "s_mov_b32 s64, 622592\n\t"
      "buffer_load_dwordx4 %[vb], %[m0], s64 offen lds\n\t"
      "s_add_u32 m0, %[lb], 32768\n\t"
      "s_mov_b32 s64, 0\n\t"
      "buffer_load_dwordx4 %[wa], %[m1], s64 offen lds\n\t"
      "s_add_u32 m0, %[lb], 33792\n\t"
      "s_mov_b32 s64, 4608\n\t"
      "buffer_load_dwordx4 %[wa], %[m1], s64 offen lds\n\t"
      "s_add_u32 m0, %[lb], 34816\n\t"
      "s_mov_b32 s64, 9216\n\t"
      "buffer_load_dwordx4 %[wb], %[m1], s64 offen lds\n\t"
      "s_add_u32 m0, %[lb], 35840\n\t"
      "s_mov_b32 s64, 13824\n\t"
      "buffer_load_dwordx4 %[wb], %[m1], s64 offen lds\n\t"
      "s_waitcnt vmcnt(8) lgkmcnt(0)\n\t"
      "s_barrier\n\t"
      "s_add_u32 m0, %[lb], 49152\n\t"
      "s_mov_b32 s64, 1048576\n\t"
      "buffer_load_dwordx4 %[va], %[m0], s64 offen lds\n\t"
      "s_add_u32 m0, %[lb], 50176\n\t"
      "s_mov_b32 s64, 1081344\n\t"
      "buffer_load_dwordx4 %[va], %[m0], s64 offen lds\n\t"
      "s_add_u32 m0, %[lb], 51200\n\t"
      "s_mov_b32 s64, 1114112\n\t"
      "buffer_load_dwordx4 %[vb], %[m0], s64 offen lds\n\t"
      "s_add_u32 m0, %[lb], 52224\n\t"
      "s_mov_b32 s64, 1146880\n\t"
      "buffer_load_dwordx4 %[vb], %[m0], s64 offen lds\n\t"
      "ds_read_b64_tr_b16 a[0:1], %[t0] offset:0\n\t"
      "ds_read_b64_tr_b16 a[2:3], %[t0] offset:1024\n\t"
      "ds_read_b64_tr_b16 a[4:5], %[t0] offset:8192\n\t"
      "ds_read_b64_tr_b16 a[6:7], %[t0] offset:9216\n\t"
      "ds_read_b64_tr_b16 a[8:9], %[t1] offset:0\n\t"
      "ds_read_b64_tr_b16 a[10:11], %[t1] offset:1024\n\t"
      "ds_read_b64_tr_b16 a[12:13], %[t1] offset:8192\n\t"
      "ds_read_b64_tr_b16 a[14:15], %[t1] offset:9216\n\t"
      "s_waitcnt vmcnt(8) lgkmcnt(0)\n\t"
      "s_barrier\n\t"
      "s_add_u32 m0, %[lb], 0\n\t"
      "s_mov_b32 s64, 256\n\t"
      "buffer_load_dwordx4 %[wa], %[m1], s64 offen lds\n\t"
      "s_add_u32 m0, %[lb], 1024\n\t"
      "s_mov_b32 s64, 4864\n\t"
      "buffer_load_dwordx4 %[wa], %[m1], s64 offen lds\n\t"
      "s_add_u32 m0, %[lb], 2048\n\t"
      "s_mov_b32 s64, 9472\n\t"
      "buffer_load_dwordx4 %[wb], %[m1], s64 offen lds\n\t"
      "s_add_u32 m0, %[lb], 3072\n\t"
      "s_mov_b32 s64, 14080\n\t"
      "buffer_load_dwordx4 %[wb], %[m1], s64 offen lds\n\t"
      "ds_read_b64_tr_b16 a[16:17], %[t0] offset:16384\n\t"
      "ds_read_b64_tr_b16 a[18:19], %[t0] offset:17408\n\t"
      "ds_read_b64_tr_b16 a[20:21], %[t0] offset:24576\n\t"
      "ds_read_b64_tr_b16 a[22:23], %[t0] offset:25600\n\t"
      "ds_read_b64_tr_b16 a[24:25], %[t1] offset:16384\n\t"
      "ds_read_b64_tr_b16 a[26:27], %[t1] offset:17408\n\t"
      "ds_read_b64_tr_b16 a[28:29], %[t1] offset:24576\n\t"
      "ds_read_b64_tr_b16 a[30:31], %[t1] offset:25600\n\t"
      "s_waitcnt vmcnt(8) lgkmcnt(0)\n\t"
      "s_barrier\n\t"
      "s_add_u32 m0, %[lb], 16384\n\t"
      "s_mov_b32 s64, 1572864\n\t"
      "buffer_load_dwordx4 %[va], %[m0], s64 offen lds\n\t"
      "s_add_u32 m0, %[lb], 17408\n\t"
      "s_mov_b32 s64, 1605632\n\t"
      "buffer_load_dwordx4 %[va], %[m0], s64 offen lds\n\t"
      "s_add_u32 m0, %[lb], 18432\n\t"
      "s_mov_b32 s64, 1638400\n\t"
      "buffer_load_dwordx4 %[vb], %[m0], s64 offen lds\n\t"
      "s_add_u32 m0, %[lb], 19456\n\t"
      "s_mov_b32 s64, 1671168\n\t"
      "buffer_load_dwordx4 %[vb], %[m0], s64 offen lds\n\t"
      "ds_read_b64_tr_b16 a[144:145], %[u0] offset:32768\n\t"
      "ds_read_b64_tr_b16 a[146:147], %[u2] offset:32768\n\t"
      "ds_read_b64_tr_b16 a[148:149], %[u0] offset:40960\n\t"
      "ds_read_b64_tr_b16 a[150:151], %[u2] offset:40960\n\t"
      "ds_read_b64_tr_b16 a[160:161], %[u1] offset:32768\n\t"
      "ds_read_b64_tr_b16 a[162:163], %[u3] offset:32768\n\t"
      "ds_read_b64_tr_b16 a[164:165], %[u1] offset:40960\n\t"
      "ds_read_b64_tr_b16 a[166:167], %[u3] offset:40960\n\t"
      "s_waitcnt vmcnt(8) lgkmcnt(0)\n\t"
      "s_barrier\n\t"
      "s_add_u32 m0, %[lb], 32768\n\t"
      "s_mov_b32 s64, 512\n\t"
      "buffer_load_dwordx4 %[wa], %[m1], s64 offen lds\n\t"
      "s_add_u32 m0, %[lb], 33792\n\t"
      "s_mov_b32 s64, 5120\n\t"
      "buffer_load_dwordx4 %[wa], %[m1], s64 offen lds\n\t"
      "s_add_u32 m0, %[lb], 34816\n\t"
      "s_mov_b32 s64, 9728\n\t"
      "buffer_load_dwordx4 %[wb], %[m1], s64 offen lds\n\t"
      "s_add_u32 m0, %[lb], 35840\n\t"
      "s_mov_b32 s64, 14336\n\t"
      "buffer_load_dwordx4 %[wb], %[m1], s64 offen lds\n\t"
      "ds_read_b64_tr_b16 a[32:33], %[t0] offset:49152\n\t"
      "ds_read_b64_tr_b16 a[34:35], %[t0] offset:50176\n\t"
      "ds_read_b64_tr_b16 a[36:37], %[t0] offset:57344\n\t"
      "ds_read_b64_tr_b16 a[38:39], %[t0] offset:58368\n\t"
      "ds_read_b64_tr_b16 a[40:41], %[t1] offset:49152\n\t"
      "ds_read_b64_tr_b16 a[42:43], %[t1] offset:50176\n\t"
      "ds_read_b64_tr_b16 a[44:45], %[t1] offset:57344\n\t"
      "ds_read_b64_tr_b16 a[46:47], %[t1] offset:58368\n\t"
      "s_waitcnt vmcnt(8) lgkmcnt(0)\n\t"
      "s_barrier\n\t"
      "s_add_u32 m0, %[lb], 49152\n\t"
      "s_mov_b32 s64, 2097152\n\t"
      "buffer_load_dwordx4 %[va], %[m0], s64 offen lds\n\t"
      "s_add_u32 m0, %[lb], 50176\n\t"
      "s_mov_b32 s64, 2129920\n\t"
      "buffer_load_dwordx4 %[va], %[m0], s64 offen lds\n\t"
      "s_add_u32 m0, %[lb], 51200\n\t"
      "s_mov_b32 s64, 2162688\n\t"
      "buffer_load_dwordx4 %[vb], %[m0], s64 offen lds\n\t"
      "s_add_u32 m0, %[lb], 52224\n\t"
      "s_mov_b32 s64, 2195456\n\t"
      "buffer_load_dwordx4 %[vb], %[m0], s64 offen lds\n\t"
      "ds_read_b64_tr_b16 a[176:177], %[u0] offset:0\n\t"
      "ds_read_b64_tr_b16 a[178:179], %[u2] offset:0\n\t"
      "ds_read_b64_tr_b16 a[180:181], %[u0] offset:8192\n\t"
      "ds_read_b64_tr_b16 a[182:183], %[u2] offset:8192\n\t"
      "ds_read_b64_tr_b16 a[192:193], %[u1] offset:0\n\t"
      "ds_read_b64_tr_b16 a[194:195], %[u3] offset:0\n\t"
      "ds_read_b64_tr_b16 a[196:197], %[u1] offset:8192\n\t"
      "ds_read_b64_tr_b16 a[198:199], %[u3] offset:8192\n\t"
      "s_waitcnt vmcnt(8) lgkmcnt(0)\n\t"
      "s_barrier\n\t"
      "s_add_u32 m0, %[lb], 0\n\t"
      "s_mov_b32 s64, 768\n\t"
      "buffer_load_dwordx4 %[wa], %[m1], s64 offen lds\n\t"
      "s_add_u32 m0, %[lb], 1024\n\t"
      "s_mov_b32 s64, 5376\n\t"
      "buffer_load_dwordx4 %[wa], %[m1], s64 offen lds\n\t"
      "s_add_u32 m0, %[lb], 2048\n\t"
      "s_mov_b32 s64, 9984\n\t"
      "buffer_load_dwordx4 %[wb], %[m1], s64 offen lds\n\t"
      "s_add_u32 m0, %[lb], 3072\n\t"
      "s_mov_b32 s64, 14592\n\t"
      "buffer_load_dwordx4 %[wb], %[m1], s64 offen lds\n\t"
      "ds_read_b64_tr_b16 a[48:49], %[t0] offset:16384\n\t"
      "ds_read_b64_tr_b16 a[50:51], %[t0] offset:17408\n\t"
      "ds_read_b64_tr_b16 a[52:53], %[t0] offset:24576\n\t"
      "ds_read_b64_tr_b16 a[54:55], %[t0] offset:25600\n\t"
      "ds_read_b64_tr_b16 a[56:57], %[t1] offset:16384\n\t"
      "ds_read_b64_tr_b16 a[58:59], %[t1] offset:17408\n\t"
      "ds_read_b64_tr_b16 a[60:61], %[t1] offset:24576\n\t"
      "ds_read_b64_tr_b16 a[62:63], %[t1] offset:25600\n\t"
      "s_waitcnt vmcnt(8) lgkmcnt(0)\n\t"
      "s_barrier\n\t"
      "s_add_u32 m0, %[lb], 16384\n\t"
      "s_mov_b32 s64, 2621440\n\t"
      "buffer_load_dwordx4 %[va], %[m0], s64 offen lds\n\t"
      "s_add_u32 m0, %[lb], 17408\n\t"
      "s_mov_b32 s64, 2654208\n\t"
      "buffer_load_dwordx4 %[va], %[m0], s64 offen lds\n\t"
      "s_add_u32 m0, %[lb], 18432\n\t"
      "s_mov_b32 s64, 2686976\n\t"
      "buffer_load_dwordx4 %[vb], %[m0], s64 offen lds\n\t"
      "s_add_u32 m0, %[lb], 19456\n\t"
      "s_mov_b32 s64, 2719744\n\t"
      "buffer_load_dwordx4 %[vb], %[m0], s64 offen lds\n\t"
      "ds_read_b64_tr_b16 a[208:209], %[u0] offset:32768\n\t"
      "ds_read_b64_tr_b16 a[210:211], %[u2] offset:32768\n\t"
      "ds_read_b64_tr_b16 a[212:213], %[u0] offset:40960\n\t"
      "ds_read_b64_tr_b16 a[214:215], %[u2] offset:40960\n\t"
      "ds_read_b64_tr_b16 a[224:225], %[u1] offset:32768\n\t"
      "ds_read_b64_tr_b16 a[226:227], %[u3] offset:32768\n\t"
      "ds_read_b64_tr_b16 a[228:229], %[u1] offset:40960\n\t"
      "ds_read_b64_tr_b16 a[230:231], %[u3] offset:40960\n\t"
      "s_waitcnt vmcnt(8) lgkmcnt(0)\n\t"
      "s_barrier\n\t"
      "s_add_u32 m0, %[lb], 32768\n\t"
      "s_mov_b32 s64, 73728\n\t"
      "buffer_load_dwordx4 %[wa], %[m1], s64 offen lds\n\t"
      "s_add_u32 m0, %[lb], 33792\n\t"
      "s_mov_b32 s64, 78336\n\t"
      "buffer_load_dwordx4 %[wa], %[m1], s64 offen lds\n\t"
      "s_add_u32 m0, %[lb], 34816\n\t"
      "s_mov_b32 s64, 82944\n\t"
      "buffer_load_dwordx4 %[wb], %[m1], s64 offen lds\n\t"
      "s_add_u32 m0, %[lb], 35840\n\t"
      "s_mov_b32 s64, 87552\n\t"
      "buffer_load_dwordx4 %[wb], %[m1], s64 offen lds\n\t"
      "ds_read_b64_tr_b16 a[64:65], %[t0] offset:49152\n\t"
      "ds_read_b64_tr_b16 a[66:67], %[t0] offset:50176\n\t"
      "ds_read_b64_tr_b16 a[68:69], %[t0] offset:57344\n\t"
      "ds_read_b64_tr_b16 a[70:71], %[t0] offset:58368\n\t"
      "ds_read_b64_tr_b16 a[72:73], %[t1] offset:49152\n\t"
      "ds_read_b64_tr_b16 a[74:75], %[t1] offset:50176\n\t"
      "ds_read_b64_tr_b16 a[76:77], %[t1] offset:57344\n\t"
      "ds_read_b64_tr_b16 a[78:79], %[t1] offset:58368\n\t"
      "s_waitcnt vmcnt(8) lgkmcnt(0)\n\t"
      "s_barrier\n\t"
      "s_add_u32 m0, %[lb], 49152\n\t"
      "s_mov_b32 s64, 3145728\n\t"
      "buffer_load_dwordx4 %[va], %[m0], s64 offen lds\n\t"
      "s_add_u32 m0, %[lb], 50176\n\t"
      "s_mov_b32 s64, 3178496\n\t"
      "buffer_load_dwordx4 %[va], %[m0], s64 offen lds\n\t"
      "s_add_u32 m0, %[lb], 51200\n\t"
      "s_mov_b32 s64, 3211264\n\t"
      "buffer_load_dwordx4 %[vb], %[m0], s64 offen lds\n\t"
      "s_add_u32 m0, %[lb], 52224\n\t"
      "s_mov_b32 s64, 3244032\n\t"
      "buffer_load_dwordx4 %[vb], %[m0], s64 offen lds\n\t"
      "ds_read_b64_tr_b16 a[240:241], %[u0] offset:0\n\t"
      "ds_read_b64_tr_b16 a[242:243], %[u2] offset:0\n\t"
      "ds_read_b64_tr_b16 a[244:245], %[u0] offset:8192\n\t"
      "ds_read_b64_tr_b16 a[246:247], %[u2] offset:8192\n\t"
      "ds_read_b64_tr_b16 v[224:225], %[u1] offset:0\n\t"
      "ds_read_b64_tr_b16 v[226:227], %[u3] offset:0\n\t"
      "ds_read_b64_tr_b16 v[228:229], %[u1] offset:8192\n\t"
      "ds_read_b64_tr_b16 v[230:231], %[u3] offset:8192\n\t"
      "s_waitcnt vmcnt(8) lgkmcnt(0)\n\t"
      "s_barrier\n\t"
      "s_add_u32 m0, %[lb], 0\n\t"
      "s_mov_b32 s64, 73984\n\t"
      "buffer_load_dwordx4 %[wa], %[m1], s64 offen lds\n\t"
      "s_add_u32 m0, %[lb], 1024\n\t"
      "s_mov_b32 s64, 78592\n\t"
      "buffer_load_dwordx4 %[wa], %[m1], s64 offen lds\n\t"
      "s_add_u32 m0, %[lb], 2048\n\t"
      "s_mov_b32 s64, 83200\n\t"
      "buffer_load_dwordx4 %[wb], %[m1], s64 offen lds\n\t"
      "s_add_u32 m0, %[lb], 3072\n\t"
      "s_mov_b32 s64, 87808\n\t"
      "buffer_load_dwordx4 %[wb], %[m1], s64 offen lds\n\t"
      "ds_read_b64_tr_b16 a[80:81], %[t0] offset:16384\n\t"
      "ds_read_b64_tr_b16 a[82:83], %[t0] offset:17408\n\t"
      "ds_read_b64_tr_b16 a[84:85], %[t0] offset:24576\n\t"
      "ds_read_b64_tr_b16 a[86:87], %[t0] offset:25600\n\t"
      "ds_read_b64_tr_b16 a[88:89], %[t1] offset:16384\n\t"
      "ds_read_b64_tr_b16 a[90:91], %[t1] offset:17408\n\t"
      "ds_read_b64_tr_b16 a[92:93], %[t1] offset:24576\n\t"
      "ds_read_b64_tr_b16 a[94:95], %[t1] offset:25600\n\t"
      "s_waitcnt vmcnt(8) lgkmcnt(0)\n\t"
      "s_barrier\n\t"
      "s_add_u32 m0, %[lb], 16384\n\t"
      "s_mov_b32 s64, 3670016\n\t"
      "buffer_load_dwordx4 %[va], %[m0], s64 offen lds\n\t"
      "s_add_u32 m0, %[lb], 17408\n\t"
      "s_mov_b32 s64, 3702784\n\t"
      "buffer_load_dwordx4 %[va], %[m0], s64 offen lds\n\t"
      "s_add_u32 m0, %[lb], 18432\n\t"
      "s_mov_b32 s64, 3735552\n\t"
      "buffer_load_dwordx4 %[vb], %[m0], s64 offen lds\n\t"
      "s_add_u32 m0, %[lb], 19456\n\t"
      "s_mov_b32 s64, 3768320\n\t"
      "buffer_load_dwordx4 %[vb], %[m0], s64 offen lds\n\t"
      "ds_read_b64_tr_b16 a[152:153], %[u0] offset:32768\n\t"
      "ds_read_b64_tr_b16 a[154:155], %[u2] offset:32768\n\t"
      "ds_read_b64_tr_b16 a[156:157], %[u0] offset:40960\n\t"
      "ds_read_b64_tr_b16 a[158:159], %[u2] offset:40960\n\t"
      "ds_read_b64_tr_b16 a[168:169], %[u1] offset:32768\n\t"
      "ds_read_b64_tr_b16 a[170:171], %[u3] offset:32768\n\t"
      "ds_read_b64_tr_b16 a[172:173], %[u1] offset:40960\n\t"
      "ds_read_b64_tr_b16 a[174:175], %[u3] offset:40960\n\t"
      "s_waitcnt vmcnt(8) lgkmcnt(0)\n\t"
      "s_barrier\n\t"
      "s_add_u32 m0, %[lb], 32768\n\t"
      "s_mov_b32 s64, 74240\n\t"
      "buffer_load_dwordx4 %[wa], %[m1], s64 offen lds\n\t"
      "s_add_u32 m0, %[lb], 33792\n\t"
      "s_mov_b32 s64, 78848\n\t"
      "buffer_load_dwordx4 %[wa], %[m1], s64 offen lds\n\t"
      "s_add_u32 m0, %[lb], 34816\n\t"
      "s_mov_b32 s64, 83456\n\t"
      "buffer_load_dwordx4 %[wb], %[m1], s64 offen lds\n\t"
      "s_add_u32 m0, %[lb], 35840\n\t"
      "s_mov_b32 s64, 88064\n\t"
      "buffer_load_dwordx4 %[wb], %[m1], s64 offen lds\n\t"
      "ds_read_b64_tr_b16 a[96:97], %[t0] offset:49152\n\t"
      "ds_read_b64_tr_b16 a[98:99], %[t0] offset:50176\n\t"
      "ds_read_b64_tr_b16 a[100:101], %[t0] offset:57344\n\t"
      "ds_read_b64_tr_b16 a[102:103], %[t0] offset:58368\n\t"
      "ds_read_b64_tr_b16 a[104:105], %[t1] offset:49152\n\t"
      "ds_read_b64_tr_b16 a[106:107], %[t1] offset:50176\n\t"
      "ds_read_b64_tr_b16 a[108:109], %[t1] offset:57344\n\t"
      "ds_read_b64_tr_b16 a[110:111], %[t1] offset:58368\n\t"
      "s_waitcnt vmcnt(8) lgkmcnt(0)\n\t"
      "s_barrier\n\t"
      "s_add_u32 m0, %[lb], 49152\n\t"
      "s_mov_b32 s64, 4194304\n\t"
      "buffer_load_dwordx4 %[va], %[m0], s64 offen lds\n\t"
      "s_add_u32 m0, %[lb], 50176\n\t"
      "s_mov_b32 s64, 4227072\n\t"
      "buffer_load_dwordx4 %[va], %[m0], s64 offen lds\n\t"
      "s_add_u32 m0, %[lb], 51200\n\t"
      "s_mov_b32 s64, 4259840\n\t"
      "buffer_load_dwordx4 %[vb], %[m0], s64 offen lds\n\t"
      "s_add_u32 m0, %[lb], 52224\n\t"
      "s_mov_b32 s64, 4292608\n\t"
      "buffer_load_dwordx4 %[vb], %[m0], s64 offen lds\n\t"
      "ds_read_b64_tr_b16 a[184:185], %[u0] offset:0\n\t"
      "ds_read_b64_tr_b16 a[186:187], %[u2] offset:0\n\t"
      "ds_read_b64_tr_b16 a[188:189], %[u0] offset:8192\n\t"
      "ds_read_b64_tr_b16 a[190:191], %[u2] offset:8192\n\t"
      "ds_read_b64_tr_b16 a[200:201], %[u1] offset:0\n\t"
      "ds_read_b64_tr_b16 a[202:203], %[u3] offset:0\n\t"
      "ds_read_b64_tr_b16 a[204:205], %[u1] offset:8192\n\t"
      "ds_read_b64_tr_b16 a[206:207], %[u3] offset:8192\n\t"
      "s_waitcnt vmcnt(8) lgkmcnt(0)\n\t"
      "s_barrier\n\t"
      "s_add_u32 m0, %[lb], 0\n\t"
      "s_mov_b32 s64, 74496\n\t"
      "buffer_load_dwordx4 %[wa], %[m1], s64 offen lds\n\t"
      "s_add_u32 m0, %[lb], 1024\n\t"
      "s_mov_b32 s64, 79104\n\t"
      "buffer_load_dwordx4 %[wa], %[m1], s64 offen lds\n\t"
      "s_add_u32 m0, %[lb], 2048\n\t"
      "s_mov_b32 s64, 83712\n\t"
      "buffer_load_dwordx4 %[wb], %[m1], s64 offen lds\n\t"
      "s_add_u32 m0, %[lb], 3072\n\t"
      "s_mov_b32 s64, 88320\n\t"
      "buffer_load_dwordx4 %[wb], %[m1], s64 offen lds\n\t"
      "ds_read_b64_tr_b16 a[112:113], %[t0] offset:16384\n\t"
      "ds_read_b64_tr_b16 a[114:115], %[t0] offset:17408\n\t"
      "ds_read_b64_tr_b16 a[116:117], %[t0] offset:24576\n\t"
      "ds_read_b64_tr_b16 a[118:119], %[t0] offset:25600\n\t"
      "ds_read_b64_tr_b16 a[120:121], %[t1] offset:16384\n\t"
      "ds_read_b64_tr_b16 a[122:123], %[t1] offset:17408\n\t"
      "ds_read_b64_tr_b16 a[124:125], %[t1] offset:24576\n\t"
      "ds_read_b64_tr_b16 a[126:127], %[t1] offset:25600\n\t"
      "s_waitcnt vmcnt(8) lgkmcnt(0)\n\t"
      "s_barrier\n\t"
      "s_add_u32 m0, %[lb], 16384\n\t"
      "s_mov_b32 s64, 1024\n\t"
      "buffer_load_dwordx4 %[pa], %[m1], s64 offen lds\n\t"
      "s_add_u32 m0, %[lb], 17408\n\t"
      "s_mov_b32 s64, 5632\n\t"
      "buffer_load_dwordx4 %[pa], %[m1], s64 offen lds\n\t"
      "s_add_u32 m0, %[lb], 18432\n\t"
      "s_mov_b32 s64, 10240\n\t"
      "buffer_load_dwordx4 %[pb], %[m1], s64 offen lds\n\t"
      "s_add_u32 m0, %[lb], 19456\n\t"
      "s_mov_b32 s64, 14848\n\t"
      "buffer_load_dwordx4 %[pb], %[m1], s64 offen lds\n\t"
      "ds_read_b64_tr_b16 a[216:217], %[u0] offset:32768\n\t"
      "ds_read_b64_tr_b16 a[218:219], %[u2] offset:32768\n\t"
      "ds_read_b64_tr_b16 a[220:221], %[u0] offset:40960\n\t"
      "ds_read_b64_tr_b16 a[222:223], %[u2] offset:40960\n\t"
      "ds_read_b64_tr_b16 a[232:233], %[u1] offset:32768\n\t"
      "ds_read_b64_tr_b16 a[234:235], %[u3] offset:32768\n\t"
      "ds_read_b64_tr_b16 a[236:237], %[u1] offset:40960\n\t"
      "ds_read_b64_tr_b16 a[238:239], %[u3] offset:40960\n\t"
      "s_waitcnt vmcnt(8) lgkmcnt(0)\n\t"
      "s_barrier\n\t"
      "ds_read_b64_tr_b16 a[128:129], %[t0] offset:49152\n\t"
      "ds_read_b64_tr_b16 a[130:131], %[t0] offset:50176\n\t"
      "ds_read_b64_tr_b16 a[132:133], %[t0] offset:57344\n\t"
      "ds_read_b64_tr_b16 a[134:135], %[t0] offset:58368\n\t"
      "ds_read_b64_tr_b16 a[136:137], %[t1] offset:49152\n\t"
      "ds_read_b64_tr_b16 a[138:139], %[t1] offset:50176\n\t"
      "ds_read_b64_tr_b16 a[140:141], %[t1] offset:57344\n\t"
      "ds_read_b64_tr_b16 a[142:143], %[t1] offset:58368\n\t"
      "s_waitcnt vmcnt(4) lgkmcnt(0)\n\t"
      "s_barrier\n\t"
      "ds_read_b64_tr_b16 a[248:249], %[u0] offset:0\n\t"
      "ds_read_b64_tr_b16 a[250:251], %[u2] offset:0\n\t"
      "ds_read_b64_tr_b16 a[252:253], %[u0] offset:8192\n\t"
      "ds_read_b64_tr_b16 a[254:255], %[u2] offset:8192\n\t"
      "ds_read_b64_tr_b16 v[232:233], %[u1] offset:0\n\t"
      "ds_read_b64_tr_b16 v[234:235], %[u3] offset:0\n\t"
      "ds_read_b64_tr_b16 v[236:237], %[u1] offset:8192\n\t"
      "ds_read_b64_tr_b16 v[238:239], %[u3] offset:8192\n\t"
      "s_waitcnt vmcnt(0) lgkmcnt(0)\n\t"
      "s_barrier\n\t"
      "ds_read_b64_tr_b16 v[240:241], %[u0] offset:16384\n\t"
      "ds_read_b64_tr_b16 v[242:243], %[u2] offset:16384\n\t"
      "ds_read_b64_tr_b16 v[244:245], %[u0] offset:24576\n\t"
      "ds_read_b64_tr_b16 v[246:247], %[u2] offset:24576\n\t"
      "ds_read_b64_tr_b16 v[248:249], %[u1] offset:16384\n\t"
      "ds_read_b64_tr_b16 v[250:251], %[u3] offset:16384\n\t"
      "ds_read_b64_tr_b16 v[252:253], %[u1] offset:24576\n\t"
      "ds_read_b64_tr_b16 v[254:255], %[u3] offset:24576\n\t"
      "s_waitcnt lgkmcnt(0)\n\t"
      "s_barrier\n\t"
      "s_mov_b32 m0, s65\n\t"
      : WOUT(W) : [m0] "s"(s0), [m1] "s"(s1), [va] "v"(va), [vb] "v"(vb), [t0] "v"(t0), [t1] "v"(t1), [lb] "s"(US(lb)), [wa] "v"(wa), [wb] "v"(wb), [pa] "v"(pa), [pb] "v"(pb),
        [u0] "v"(u0), [u1] "v"(u1), [u2] "v"(u2_), [u3] "v"(u3) : "s64", "s65", "scc", "memory");
  else asm volatile(
      "s_mov_b32 s65, m0\n\t"
      "s_add_u32 m0, %[lb], 0\n\t"
      "s_mov_b32 s64, 0\n\t"
      "buffer_load_dwordx4 %[va], %[m0], s64 offen lds\n\t"
      "s_add_u32 m0, %[lb], 1024\n\t"
      "s_mov_b32 s64, 32768\n\t"
      "buffer_load_dwordx4 %[va], %[m0], s64 offen lds\n\t"
      "s_add_u32 m0, %[lb], 2048\n\t"
      "s_mov_b32 s64, 65536\n\t"
      "buffer_load_dwordx4 %[vb], %[m0], s64 offen lds\n\t"
      "s_add_u32 m0, %[lb], 3072\n\t"
      "s_mov_b32 s64, 98304\n\t"
      "buffer_load_dwordx4 %[vb], %[m0], s64 offen lds\n\t"
      "s_add_u32 m0, %[lb], 16384\n\t"
      "s_mov_b32 s64, 524288\n\t"
      "buffer_load_dwordx4 %[va], %[m0], s64 offen lds\n\t"
      "s_add_u32 m0, %[lb], 17408\n\t"
      "s_mov_b32 s64, 557056\n\t"
      "buffer_load_dwordx4 %[va], %[m0], s64 offen lds\n\t"
      "s_add_u32 m0, %[lb], 18432\n\t"
      "s_mov_b32 s64, 589824\n\t"
      "buffer_load_dwordx4 %[vb], %[m0], s64 offen lds\n\t"
      "s_add_u32 m0, %[lb], 19456\n\t"
      "s_mov_b32 s64, 622592\n\t"
      "buffer_load_dwordx4 %[vb], %[m0], s64 offen lds\n\t"
      "s_add_u32 m0, %[lb], 32768\n\t"
      "s_mov_b32 s64, 0\n\t"
      "buffer_load_dwordx4 %[wa], %[m1], s64 offen lds\n\t"
      "s_add_u32 m0, %[lb], 33792\n\t"
      "s_mov_b32 s64, 4608\n\t"
      "buffer_load_dwordx4 %[wa], %[m1], s64 offen lds\n\t"
      "s_add_u32 m0, %[lb], 34816\n\t"
      "s_mov_b32 s64, 9216\n\t"
      "buffer_load_dwordx4 %[wb], %[m1], s64 offen lds\n\t"
      "s_add_u32 m0, %[lb], 35840\n\t"
      "s_mov_b32 s64, 13824\n\t"
      "buffer_load_dwordx4 %[wb], %[m1], s64 offen lds\n\t"
      "s_waitcnt vmcnt(8) lgkmcnt(0)\n\t"
      "s_barrier\n\t"
      "s_add_u32 m0, %[lb], 49152\n\t"
      "s_mov_b32 s64, 1048576\n\t"
      "buffer_load_dwordx4 %[va], %[m0], s64 offen lds\n\t"
      "s_add_u32 m0, %[lb], 50176\n\t"
      "s_mov_b32 s64, 1081344\n\t"
      "buffer_load_dwordx4 %[va], %[m0], s64 offen lds\n\t"
      "s_add_u32 m0, %[lb], 51200\n\t"
      "s_mov_b32 s64, 1114112\n\t"
      "buffer_load_dwordx4 %[vb], %[m0], s64 offen lds\n\t"
      "s_add_u32 m0, %[lb], 52224\n\t"
      "s_mov_b32 s64, 1146880\n\t"
      "buffer_load_dwordx4 %[vb], %[m0], s64 offen lds\n\t"
      "ds_read_b64_tr_b16 a[0:1], %[t0] offset:0\n\t"
      "ds_read_b64_tr_b16 a[2:3], %[t0] offset:1024\n\t"
      "ds_read_b64_tr_b16 a[4:5], %[t0] offset:8192\n\t"
      "ds_read_b64_tr_b16 a[6:7], %[t0] offset:9216\n\t"
      "ds_read_b64_tr_b16 a[8:9], %[t1] offset:0\n\t"
      "ds_read_b64_tr_b16 a[10:11], %[t1] offset:1024\n\t"
      "ds_read_b64_tr_b16 a[12:13], %[t1] offset:8192\n\t"
      "ds_read_b64_tr_b16 a[14:15], %[t1] offset:9216\n\t"
      "s_waitcnt vmcnt(8) lgkmcnt(0)\n\t"
      "s_barrier\n\t"
      "s_add_u32 m0, %[lb], 0\n\t"
      "s_mov_b32 s64, 256\n\t"
      "buffer_load_dwordx4 %[wa], %[m1], s64 offen lds\n\t"
      "s_add_u32 m0, %[lb], 1024\n\t"
      "s_mov_b32 s64, 4864\n\t"
      "buffer_load_dwordx4 %[wa], %[m1], s64 offen lds\n\t"
      "s_add_u32 m0, %[lb], 2048\n\t"
      "s_mov_b32 s64, 9472\n\t"
      "buffer_load_dwordx4 %[wb], %[m1], s64 offen lds\n\t"
      "s_add_u32 m0, %[lb], 3072\n\t"
      "s_mov_b32 s64, 14080\n\t"
      "buffer_load_dwordx4 %[wb], %[m1], s64 offen lds\n\t"
      "ds_read_b64_tr_b16 a[16:17], %[t0] offset:16384\n\t"
      "ds_read_b64_tr_b16 a[18:19], %[t0] offset:17408\n\t"
      "ds_read_b64_tr_b16 a[20:21], %[t0] offset:24576\n\t"
      "ds_read_b64_tr_b16 a[22:23], %[t0] offset:25600\n\t"
      "ds_read_b64_tr_b16 a[24:25], %[t1] offset:16384\n\t"
      "ds_read_b64_tr_b16 a[26:27], %[t1] offset:17408\n\t"
      "ds_read_b64_tr_b16 a[28:29], %[t1] offset:24576\n\t"
      "ds_read_b64_tr_b16 a[30:31], %[t1] offset:25600\n\t"
      "s_waitcnt vmcnt(8) lgkmcnt(0)\n\t"
      "s_barrier\n\t"
      "s_add_u32 m0, %[lb], 16384\n\t"
      "s_mov_b32 s64, 1572864\n\t"
      "buffer_load_dwordx4 %[va], %[m0], s64 offen lds\n\t"
      "s_add_u32 m0, %[lb], 17408\n\t"
      "s_mov_b32 s64, 1605632\n\t"
      "buffer_load_dwordx4 %[va], %[m0], s64 offen lds\n\t"
      "s_add_u32 m0, %[lb], 18432\n\t"
      "s_mov_b32 s64, 1638400\n\t"
      "buffer_load_dwordx4 %[vb], %[m0], s64 offen lds\n\t"
      "s_add_u32 m0, %[lb], 19456\n\t"
      "s_mov_b32 s64, 1671168\n\t"
      "buffer_load_dwordx4 %[vb], %[m0], s64 offen lds\n\t"
      "ds_read_b64_tr_b16 a[144:145], %[u0] offset:32768\n\t"
      "ds_read_b64_tr_b16 a[146:147], %[u0] offset:36864\n\t"
      "ds_read_b64_tr_b16 a[148:149], %[u0] offset:40960\n\t"
      "ds_read_b64_tr_b16 a[150:151], %[u0] offset:45056\n\t"
      "ds_read_b64_tr_b16 a[160:161], %[u1] offset:32768\n\t"
      "ds_read_b64_tr_b16 a[162:163], %[u1] offset:36864\n\t"
      "ds_read_b64_tr_b16 a[164:165], %[u1] offset:40960\n\t"
      "ds_read_b64_tr_b16 a[166:167], %[u1] offset:45056\n\t"
      "s_waitcnt vmcnt(8) lgkmcnt(0)\n\t"
      "s_barrier\n\t"
      "s_add_u32 m0, %[lb], 32768\n\t"
      "s_mov_b32 s64, 512\n\t"
      "buffer_load_dwordx4 %[wa], %[m1], s64 offen lds\n\t"
      "s_add_u32 m0, %[lb], 33792\n\t"
      "s_mov_b32 s64, 5120\n\t"
      "buffer_load_dwordx4 %[wa], %[m1], s64 offen lds\n\t"
      "s_add_u32 m0, %[lb], 34816\n\t"
      "s_mov_b32 s64, 9728\n\t"
      "buffer_load_dwordx4 %[wb], %[m1], s64 offen lds\n\t"
      "s_add_u32 m0, %[lb], 35840\n\t"
      "s_mov_b32 s64, 14336\n\t"
      "buffer_load_dwordx4 %[wb], %[m1], s64 offen lds\n\t"
      "ds_read_b64_tr_b16 a[32:33], %[t0] offset:49152\n\t"
      "ds_read_b64_tr_b16 a[34:35], %[t0] offset:50176\n\t"
      "ds_read_b64_tr_b16 a[36:37], %[t0] offset:57344\n\t"
      "ds_read_b64_tr_b16 a[38:39], %[t0] offset:58368\n\t"
      "ds_read_b64_tr_b16 a[40:41], %[t1] offset:49152\n\t"
      "ds_read_b64_tr_b16 a[42:43], %[t1] offset:50176\n\t"
      "ds_read_b64_tr_b16 a[44:45], %[t1] offset:57344\n\t"
      "ds_read_b64_tr_b16 a[46:47], %[t1] offset:58368\n\t"
      "s_waitcnt vmcnt(8) lgkmcnt(0)\n\t"
      "s_barrier\n\t"
      "s_add_u32 m0, %[lb], 49152\n\t"
      "s_mov_b32 s64, 2097152\n\t"
      "buffer_load_dwordx4 %[va], %[m0], s64 offen lds\n\t"
      "s_add_u32 m0, %[lb], 50176\n\t"
      "s_mov_b32 s64, 2129920\n\t"
      "buffer_load_dwordx4 %[va], %[m0], s64 offen lds\n\t"
      "s_add_u32 m0, %[lb], 51200\n\t"
      "s_mov_b32 s64, 2162688\n\t"
      "buffer_load_dwordx4 %[vb], %[m0], s64 offen lds\n\t"
      "s_add_u32 m0, %[lb], 52224\n\t"
      "s_mov_b32 s64, 2195456\n\t"
      "buffer_load_dwordx4 %[vb], %[m0], s64 offen lds\n\t"
      "ds_read_b64_tr_b16 a[176:177], %[u0] offset:0\n\t"
      "ds_read_b64_tr_b16 a[178:179], %[u0] offset:4096\n\t"
      "ds_read_b64_tr_b16 a[180:181], %[u0] offset:8192\n\t"
      "ds_read_b64_tr_b16 a[182:183], %[u0] offset:12288\n\t"
      "ds_read_b64_tr_b16 a[192:193], %[u1] offset:0\n\t"
      "ds_read_b64_tr_b16 a[194:195], %[u1] offset:4096\n\t"
      "ds_read_b64_tr_b16 a[196:197], %[u1] offset:8192\n\t"
      "ds_read_b64_tr_b16 a[198:199], %[u1] offset:12288\n\t"
      "s_waitcnt vmcnt(8) lgkmcnt(0)\n\t"
      "s_barrier\n\t"
      "s_add_u32 m0, %[lb], 0\n\t"
      "s_mov_b32 s64, 768\n\t"
      "buffer_load_dwordx4 %[wa], %[m1], s64 offen lds\n\t"
      "s_add_u32 m0, %[lb], 1024\n\t"
      "s_mov_b32 s64, 5376\n\t"
      "buffer_load_dwordx4 %[wa], %[m1], s64 offen lds\n\t"
      "s_add_u32 m0, %[lb], 2048\n\t"
      "s_mov_b32 s64, 9984\n\t"
      "buffer_load_dwordx4 %[wb], %[m1], s64 offen lds\n\t"
      "s_add_u32 m0, %[lb], 3072\n\t"
      "s_mov_b32 s64, 14592\n\t"
      "buffer_load_dwordx4 %[wb], %[m1], s64 offen lds\n\t"
      "ds_read_b64_tr_b16 a[48:49], %[t0] offset:16384\n\t"
      "ds_read_b64_tr_b16 a[50:51], %[t0] offset:17408\n\t"
      "ds_read_b64_tr_b16 a[52:53], %[t0] offset:24576\n\t"
      "ds_read_b64_tr_b16 a[54:55], %[t0] offset:25600\n\t"
      "ds_read_b64_tr_b16 a[56:57], %[t1] offset:16384\n\t"
      "ds_read_b64_tr_b16 a[58:59], %[t1] offset:17408\n\t"
      "ds_read_b64_tr_b16 a[60:61], %[t1] offset:24576\n\t"
      "ds_read_b64_tr_b16 a[62:63], %[t1] offset:25600\n\t"
      "s_waitcnt vmcnt(8) lgkmcnt(0)\n\t"
      "s_barrier\n\t"
      "s_add_u32 m0, %[lb], 16384\n\t"
      "s_mov_b32 s64, 2621440\n\t"
      "buffer_load_dwordx4 %[va], %[m0], s64 offen lds\n\t"
      "s_add_u32 m0, %[lb], 17408\n\t"
      "s_mov_b32 s64, 2654208\n\t"
      "buffer_load_dwordx4 %[va], %[m0], s64 offen lds\n\t"
      "s_add_u32 m0, %[lb], 18432\n\t"
      "s_mov_b32 s64, 2686976\n\t"
      "buffer_load_dwordx4 %[vb], %[m0], s64 offen lds\n\t"
      "s_add_u32 m0, %[lb], 19456\n\t"
      "s_mov_b32 s64, 2719744\n\t"
      "buffer_load_dwordx4 %[vb], %[m0], s64 offen lds\n\t"
      "ds_read_b64_tr_b16 a[208:209], %[u0] offset:32768\n\t"
      "ds_read_b64_tr_b16 a[210:211], %[u0] offset:36864\n\t"
      "ds_read_b64_tr_b16 a[212:213], %[u0] offset:40960\n\t"
      "ds_read_b64_tr_b16 a[214:215], %[u0] offset:45056\n\t"
      "ds_read_b64_tr_b16 a[224:225], %[u1] offset:32768\n\t"
      "ds_read_b64_tr_b16 a[226:227], %[u1] offset:36864\n\t"
      "ds_read_b64_tr_b16 a[228:229], %[u1] offset:40960\n\t"
      "ds_read_b64_tr_b16 a[230:231], %[u1] offset:45056\n\t"
      "s_waitcnt vmcnt(8) lgkmcnt(0)\n\t"
      "s_barrier\n\t"
      "s_add_u32 m0, %[lb], 32768\n\t"
      "s_mov_b32 s64, 73728\n\t"
      "buffer_load_dwordx4 %[wa], %[m1], s64 offen lds\n\t"
      "s_add_u32 m0, %[lb], 33792\n\t"
      "s_mov_b32 s64, 78336\n\t"
      "buffer_load_dwordx4 %[wa], %[m1], s64 offen lds\n\t"
      "s_add_u32 m0, %[lb], 34816\n\t"
      "s_mov_b32 s64, 82944\n\t"
      "buffer_load_dwordx4 %[wb], %[m1], s64 offen lds\n\t"
      "s_add_u32 m0, %[lb], 35840\n\t"
      "s_mov_b32 s64, 87552\n\t"
      "buffer_load_dwordx4 %[wb], %[m1], s64 offen lds\n\t"
      "ds_read_b64_tr_b16 a[64:65], %[t0] offset:49152\n\t"
      "ds_read_b64_tr_b16 a[66:67], %[t0] offset:50176\n\t"
      "ds_read_b64_tr_b16 a[68:69], %[t0] offset:57344\n\t"
      "ds_read_b64_tr_b16 a[70:71], %[t0] offset:58368\n\t"
      "ds_read_b64_tr_b16 a[72:73], %[t1] offset:49152\n\t"
      "ds_read_b64_tr_b16 a[74:75], %[t1] offset:50176\n\t"
      "ds_read_b64_tr_b16 a[76:77], %[t1] offset:57344\n\t"
      "ds_read_b64_tr_b16 a[78:79], %[t1] offset:58368\n\t"
      "s_waitcnt vmcnt(8) lgkmcnt(0)\n\t"
      "s_barrier\n\t"
      "s_add_u32 m0, %[lb], 49152\n\t"
      "s_mov_b32 s64, 3145728\n\t"
      "buffer_load_dwordx4 %[va], %[m0], s64 offen lds\n\t"
      "s_add_u32 m0, %[lb], 50176\n\t"
      "s_mov_b32 s64, 3178496\n\t"
      "buffer_load_dwordx4 %[va], %[m0], s64 offen lds\n\t"
      "s_add_u32 m0, %[lb], 51200\n\t"
      "s_mov_b32 s64, 3211264\n\t"
      "buffer_load_dwordx4 %[vb], %[m0], s64 offen lds\n\t"
      "s_add_u32 m0, %[lb], 52224\n\t"
      "s_mov_b32 s64, 3244032\n\t"
      "buffer_load_dwordx4 %[vb], %[m0], s64 offen lds\n\t"
      "ds_read_b64_tr_b16 a[240:241], %[u0] offset:0\n\t"
      "ds_read_b64_tr_b16 a[242:243], %[u0] offset:4096\n\t"
      "ds_read_b64_tr_b16 a[244:245], %[u0] offset:8192\n\t"
      "ds_read_b64_tr_b16 a[246:247], %[u0] offset:12288\n\t"
      "ds_read_b64_tr_b16 v[224:225], %[u1] offset:0\n\t"
      "ds_read_b64_tr_b16 v[226:227], %[u1] offset:4096\n\t"
      "ds_read_b64_tr_b16 v[228:229], %[u1] offset:8192\n\t"
      "ds_read_b64_tr_b16 v[230:231], %[u1] offset:12288\n\t"
      "s_waitcnt vmcnt(8) lgkmcnt(0)\n\t"
      "s_barrier\n\t"
      "s_add_u32 m0, %[lb], 0\n\t"
      "s_mov_b32 s64, 73984\n\t"
      "buffer_load_dwordx4 %[wa], %[m1], s64 offen lds\n\t"
      "s_add_u32 m0, %[lb], 1024\n\t"
      "s_mov_b32 s64, 78592\n\t"
      "buffer_load_dwordx4 %[wa], %[m1], s64 offen lds\n\t"
      "s_add_u32 m0, %[lb], 2048\n\t"
      "s_mov_b32 s64, 83200\n\t"
      "buffer_load_dwordx4 %[wb], %[m1], s64 offen lds\n\t"
      "s_add_u32 m0, %[lb], 3072\n\t"
      "s_mov_b32 s64, 87808\n\t"
      "buffer_load_dwordx4 %[wb], %[m1], s64 offen lds\n\t"
      "ds_read_b64_tr_b16 a[80:81], %[t0] offset:16384\n\t"
      "ds_read_b64_tr_b16 a[82:83], %[t0] offset:17408\n\t"
      "ds_read_b64_tr_b16 a[84:85], %[t0] offset:24576\n\t"
      "ds_read_b64_tr_b16 a[86:87], %[t0] offset:25600\n\t"
      "ds_read_b64_tr_b16 a[88:89], %[t1] offset:16384\n\t"
      "ds_read_b64_tr_b16 a[90:91], %[t1] offset:17408\n\t"
      "ds_read_b64_tr_b16 a[92:93], %[t1] offset:24576\n\t"
      "ds_read_b64_tr_b16 a[94:95], %[t1] offset:25600\n\t"
      "s_waitcnt vmcnt(8) lgkmcnt(0)\n\t"
      "s_barrier\n\t"
      "s_add_u32 m0, %[lb], 16384\n\t"
      "s_mov_b32 s64, 3670016\n\t"
      "buffer_load_dwordx4 %[va], %[m0], s64 offen lds\n\t"
      "s_add_u32 m0, %[lb], 17408\n\t"
      "s_mov_b32 s64, 3702784\n\t"
      "buffer_load_dwordx4 %[va], %[m0], s64 offen lds\n\t"
      "s_add_u32 m0, %[lb], 18432\n\t"
      "s_mov_b32 s64, 3735552\n\t"
      "buffer_load_dwordx4 %[vb], %[m0], s64 offen lds\n\t"
      "s_add_u32 m0, %[lb], 19456\n\t"
      "s_mov_b32 s64, 3768320\n\t"
      "buffer_load_dwordx4 %[vb], %[m0], s64 offen lds\n\t"
      "ds_read_b64_tr_b16 a[152:153], %[u0] offset:32768\n\t"
      "ds_read_b64_tr_b16 a[154:155], %[u0] offset:36864\n\t"
      "ds_read_b64_tr_b16 a[156:157], %[u0] offset:40960\n\t"
      "ds_read_b64_tr_b16 a[158:159], %[u0] offset:45056\n\t"
      "ds_read_b64_tr_b16 a[168:169], %[u1] offset:32768\n\t"
      "ds_read_b64_tr_b16 a[170:171], %[u1] offset:36864\n\t"
      "ds_read_b64_tr_b16 a[172:173], %[u1] offset:40960\n\t"
      "ds_read_b64_tr_b16 a[174:175], %[u1] offset:45056\n\t"
      "s_waitcnt vmcnt(8) lgkmcnt(0)\n\t"
      "s_barrier\n\t"
      "s_add_u32 m0, %[lb], 32768\n\t"
      "s_mov_b32 s64, 74240\n\t"
      "buffer_load_dwordx4 %[wa], %[m1], s64 offen lds\n\t"
      "s_add_u32 m0, %[lb], 33792\n\t"
      "s_mov_b32 s64, 78848\n\t"
      "buffer_load_dwordx4 %[wa], %[m1], s64 offen lds\n\t"
      "s_add_u32 m0, %[lb], 34816\n\t"
      "s_mov_b32 s64, 83456\n\t"
      "buffer_load_dwordx4 %[wb], %[m1], s64 offen lds\n\t"
      "s_add_u32 m0, %[lb], 35840\n\t"
      "s_mov_b32 s64, 88064\n\t"
      "buffer_load_dwordx4 %[wb], %[m1], s64 offen lds\n\t"
      "ds_read_b64_tr_b16 a[96:97], %[t0] offset:49152\n\t"
      "ds_read_b64_tr_b16 a[98:99], %[t0] offset:50176\n\t"
      "ds_read_b64_tr_b16 a[100:101], %[t0] offset:57344\n\t"
      "ds_read_b64_tr_b16 a[102:103], %[t0] offset:58368\n\t"
      "ds_read_b64_tr_b16 a[104:105], %[t1] offset:49152\n\t"
      "ds_read_b64_tr_b16 a[106:107], %[t1] offset:50176\n\t"
      "ds_read_b64_tr_b16 a[108:109], %[t1] offset:57344\n\t"
      "ds_read_b64_tr_b16 a[110:111], %[t1] offset:58368\n\t"
      "s_waitcnt vmcnt(8) lgkmcnt(0)\n\t"
      "s_barrier\n\t"
      "s_add_u32 m0, %[lb], 49152\n\t"
      "s_mov_b32 s64, 4194304\n\t"
      "buffer_load_dwordx4 %[va], %[m0], s64 offen lds\n\t"
      "s_add_u32 m0, %[lb], 50176\n\t"
      "s_mov_b32 s64, 4227072\n\t"
      "buffer_load_dwordx4 %[va], %[m0], s64 offen lds\n\t"
      "s_add_u32 m0, %[lb], 51200\n\t"
      "s_mov_b32 s64, 4259840\n\t"
      "buffer_load_dwordx4 %[vb], %[m0], s64 offen lds\n\t"
      "s_add_u32 m0, %[lb], 52224\n\t"
      "s_mov_b32 s64, 4292608\n\t"
      "buffer_load_dwordx4 %[vb], %[m0], s64 offen lds\n\t"
      "ds_read_b64_tr_b16 a[184:185], %[u0] offset:0\n\t"
      "ds_read_b64_tr_b16 a[186:187], %[u0] offset:4096\n\t"
      "ds_read_b64_tr_b16 a[188:189], %[u0] offset:8192\n\t"
      "ds_read_b64_tr_b16 a[190:191], %[u0] offset:12288\n\t"
      "ds_read_b64_tr_b16 a[200:201], %[u1] offset:0\n\t"
      "ds_read_b64_tr_b16 a[202:203], %[u1] offset:4096\n\t"
      "ds_read_b64_tr_b16 a[204:205], %[u1] offset:8192\n\t"
      "ds_read_b64_tr_b16 a[206:207], %[u1] offset:12288\n\t"
      "s_waitcnt vmcnt(8) lgkmcnt(0)\n\t"
      "s_barrier\n\t"
      "s_add_u32 m0, %[lb], 0\n\t"
      "s_mov_b32 s64, 74496\n\t"
      "buffer_load_dwordx4 %[wa], %[m1], s64 offen lds\n\t"
      "s_add_u32 m0, %[lb], 1024\n\t"
      "s_mov_b32 s64, 79104\n\t"
      "buffer_load_dwordx4 %[wa], %[m1], s64 offen lds\n\t"
      "s_add_u32 m0, %[lb], 2048\n\t"
      "s_mov_b32 s64, 83712\n\t"
      "buffer_load_dwordx4 %[wb], %[m1], s64 offen lds\n\t"
      "s_add_u32 m0, %[lb], 3072\n\t"
      "s_mov_b32 s64, 88320\n\t"
      "buffer_load_dwordx4 %[wb], %[m1], s64 offen lds\n\t"
      "ds_read_b64_tr_b16 a[112:113], %[t0] offset:16384\n\t"
      "ds_read_b64_tr_b16 a[114:115], %[t0] offset:17408\n\t"
      "ds_read_b64_tr_b16 a[116:117], %[t0] offset:24576\n\t"
      "ds_read_b64_tr_b16 a[118:119], %[t0] offset:25600\n\t"
      "ds_read_b64_tr_b16 a[120:121], %[t1] offset:16384\n\t"
      "ds_read_b64_tr_b16 a[122:123], %[t1] offset:17408\n\t"
      "ds_read_b64_tr_b16 a[124:125], %[t1] offset:24576\n\t"
      "ds_read_b64_tr_b16 a[126:127], %[t1] offset:25600\n\t"
      "s_waitcnt vmcnt(8) lgkmcnt(0)\n\t"
      "s_barrier\n\t"
      "s_add_u32 m0, %[lb], 16384\n\t"
      "s_mov_b32 s64, 1024\n\t"
      "buffer_load_dwordx4 %[pa], %[m1], s64 offen lds\n\t"
      "s_add_u32 m0, %[lb], 17408\n\t"
      "s_mov_b32 s64, 5632\n\t"
      "buffer_load_dwordx4 %[pa], %[m1], s64 offen lds\n\t"
      "s_add_u32 m0, %[lb], 18432\n\t"
      "s_mov_b32 s64, 10240\n\t"
      "buffer_load_dwordx4 %[pb], %[m1], s64 offen lds\n\t"
      "s_add_u32 m0, %[lb], 19456\n\t"
      "s_mov_b32 s64, 14848\n\t"
      "buffer_load_dwordx4 %[pb], %[m1], s64 offen lds\n\t"
      "ds_read_b64_tr_b16 a[216:217], %[u0] offset:32768\n\t"
      "ds_read_b64_tr_b16 a[218:219], %[u0] offset:36864\n\t"
      "ds_read_b64_tr_b16 a[220:221], %[u0] offset:40960\n\t"
      "ds_read_b64_tr_b16 a[222:223], %[u0] offset:45056\n\t"
      "ds_read_b64_tr_b16 a[232:233], %[u1] offset:32768\n\t"
      "ds_read_b64_tr_b16 a[234:235], %[u1] offset:36864\n\t"
      "ds_read_b64_tr_b16 a[236:237], %[u1] offset:40960\n\t"
      "ds_read_b64_tr_b16 a[238:239], %[u1] offset:45056\n\t"
      "s_waitcnt vmcnt(8) lgkmcnt(0)\n\t"
      "s_barrier\n\t"
      "ds_read_b64_tr_b16 a[128:129], %[t0] offset:49152\n\t"
      "ds_read_b64_tr_b16 a[130:131], %[t0] offset:50176\n\t"
      "ds_read_b64_tr_b16 a[132:133], %[t0] offset:57344\n\t"
      "ds_read_b64_tr_b16 a[134:135], %[t0] offset:58368\n\t"
      "ds_read_b64_tr_b16 a[136:137], %[t1] offset:49152\n\t"
      "ds_read_b64_tr_b16 a[138:139], %[t1] offset:50176\n\t"
      "ds_read_b64_tr_b16 a[140:141], %[t1] offset:57344\n\t"
      "ds_read_b64_tr_b16 a[142:143], %[t1] offset:58368\n\t"
      "s_waitcnt vmcnt(4) lgkmcnt(0)\n\t"
      "s_barrier\n\t"
      "ds_read_b64_tr_b16 a[248:249], %[u0] offset:0\n\t"
      "ds_read_b64_tr_b16 a[250:251], %[u0] offset:4096\n\t"
      "ds_read_b64_tr_b16 a[252:253], %[u0] offset:8192\n\t"
      "ds_read_b64_tr_b16 a[254:255], %[u0] offset:12288\n\t"
      "ds_read_b64_tr_b16 v[232:233], %[u1] offset:0\n\t"
      "ds_read_b64_tr_b16 v[234:235], %[u1] offset:4096\n\t"
      "ds_read_b64_tr_b16 v[236:237], %[u1] offset:8192\n\t"
      "ds_read_b64_tr_b16 v[238:239], %[u1] offset:12288\n\t"
      "s_waitcnt vmcnt(0) lgkmcnt(0)\n\t"
      "s_barrier\n\t"
      "ds_read_b64_tr_b16 v[240:241], %[u0] offset:16384\n\t"
      "ds_read_b64_tr_b16 v[242:243], %[u0] offset:20480\n\t"
      "ds_read_b64_tr_b16 v[244:245], %[u0] offset:24576\n\t"
      "ds_read_b64_tr_b16 v[246:247], %[u0] offset:28672\n\t"
      "ds_read_b64_tr_b16 v[248:249], %[u1] offset:16384\n\t"
      "ds_read_b64_tr_b16 v[250:251], %[u1] offset:20480\n\t"
      "ds_read_b64_tr_b16 v[252:253], %[u1] offset:24576\n\t"
      "ds_read_b64_tr_b16 v[254:255], %[u1] offset:28672\n\t"
      "s_waitcnt lgkmcnt(0)\n\t"
      "s_barrier\n\t"
      "s_mov_b32 m0, s65\n\t"
      : WOUT(W) : [m0] "s"(s0), [m1] "s"(s1), [va] "v"(va), [vb] "v"(vb), [t0] "v"(t0), [t1] "v"(t1), [lb] "s"(US(lb)), [wa] "v"(wa), [wb] "v"(wb), [pa] "v"(pa), [pb] "v"(pb),
        [u0] "v"(u0), [u1] "v"(u1) : "s64", "s65", "scc", "memory");
}
DI int wmax(int v);
DI bool wait_flags(__amdgpu_buffer_rsrc_t rs, u32 off, int need, int& seen) {
  if (seen >= need) return true;
  for (int n = 0; n < (SPIN_CAP << 2); ++n) {
    asm volatile("" ::: "memory");
    const int v = (int)__builtin_amdgcn_raw_buffer_load_b32(rs, off, 0, 16);
    seen = -wmax(-v);
    if (seen >= need) { asm volatile("" ::: "memory"); return true; }
    __builtin_amdgcn_s_sleep(2);
  }
  return false;
}
DI void role_S(const Params& p, int h, u2 e) {
  const int tid = threadIdx.x, lane = tid & 63, w = __builtin_amdgcn_readfirstlane(tid >> 6);
  const int c = lane & 15, g = lane >> 4;
  u32v W[9];
  load_w2x<8192>(W, p.Wq + h * 128, p.Wk + h * 128, w, lane);
  const u32 par = get_par(p, mk_tag(p, e), 1);
  const __amdgpu_buffer_rsrc_t rs = mkrsrc(p.ws, WS_SPAN);
  const u4 srd = mksrd(p.ws, WS_SPAN);
  const int idx = (c & 1) ? 0xEEEE : 0x4444;
  const u32 l16 = lane * 16, gl0 = l16 + w * 2048, l16c = 16384u + l16, ma = 147456u + l16;
  const u32 wa = 156672u + (32 * w + c) * 40 + g * 4;
  const int q4 = (lane >> 2) & 3, p4 = lane & 3;
  const u32 ra = 156672u + (4 * g + q4) * 40 + (p4 & 1) * 8;
  const int rr = 2 * (c & 7) + (c >> 3);
  const u32 px_off = w ? h * 2048 + (w - 1) * 1024 + l16 : 32768u + (u32)(h >> 4) * 16384u + (u32)(h & 15) * 512u + ((g >> 1) * 16 + rr) * 16 + (g & 1) * 8;
  const u32 pm_off = g >= 2 ? 0xFFFFFF00u : w ? 65536u + (w - 1) * 8192 + h * 256 + c * 16 + g * 8 : 20480u + (h >> 1) * 256 + rr * 16 + (h & 1) * 8 + g * 4;
  const float ninit = g < 2 ? 0.f : -INFINITY;
  const f4 ni = {ninit, ninit, ninit, ninit};
  const unsigned long long b8mask = 0xFF00FF00FF00FF00ull;
  const int cx = blockIdx.x & 3, cy = cx == 2 ? 0 : cx + 1;
  const u32 ca = cx == 0 ? 0u : cx == 1 ? 65536u : 114688u, cb = cx * 1024u, da = cy == 0 ? 0u : cy == 1 ? 65536u : 114688u, db = cy * 1024u;
  u32* stamp = (u32*)(p.ws + OFF_CTL) + CTL_STAMP;
  u32 t0 = 0, t1 = 0, t2 = 0, te, du, flag;
  LOOPSTART
  if (w == 0) {
    for (int s = -1; s < NSEG; ++s) {
      const u32 mb = mp_base(max(s, 0), par), pb = (s < 0 || s == NSEG - 1) ? 0xF0000000u : mb;
      asm volatile(
      "v_mov_b64 v[164:165], 0\n\t"
      "v_mov_b64 v[166:167], 0\n\t"
      "v_mov_b64 v[168:169], 0\n\t"
      "v_mov_b64 v[170:171], 0\n\t"
      "v_mov_b64 v[172:173], 0\n\t"
      "v_mov_b64 v[174:175], 0\n\t"
      "v_mov_b64 v[176:177], 0\n\t"
      "v_mov_b64 v[178:179], 0\n\t"
      "s_mov_b32 %[flag], 0\n\t"
      "s_barrier\n\t"
      "buffer_load_dwordx4 v[128:131], %[o0], %[srd], %[ma0] offen sc1\n\t"
      "buffer_load_dwordx4 v[132:135], %[o0], %[srd], %[ma0] offen offset:1024 sc1\n\t"
      "s_memtime s[92:93]\n\t"
      "ds_read_b128 v[192:195], %[l16] offset:0\n\t"
      "ds_read_b128 v[196:199], %[l16] offset:1024\n\t"
      "ds_read_b128 v[200:203], %[l16] offset:2048\n\t"
      "ds_read_b128 v[204:207], %[l16] offset:3072\n\t"
      "ds_read_b128 v[208:211], %[l16] offset:4096\n\t"
      "ds_read_b128 v[212:215], %[l16] offset:5120\n\t"
      "ds_read_b128 v[216:219], %[l16] offset:6144\n\t"
      "ds_read_b128 v[220:223], %[l16] offset:7168\n\t"
      "s_waitcnt vmcnt(1)\n\t"
      "ds_write_b128 %[ma], v[128:131] offset:0\n\t"
      "v_smfmac_f32_16x16x64_bf16 v[164:167], v[128:131], a[0:7], %[idx]\n\t"
      "s_waitcnt vmcnt(0)\n\t"
      "ds_write_b128 %[ma], v[132:135] offset:1024\n\t"
      "v_max_u32 v180, v128, v129\n\t"
      "v_smfmac_f32_16x16x64_bf16 v[168:171], v[128:131], a[8:15], %[idx]\n\t"
      "v_max3_u32 v180, v180, v130, v131\n\t"
      "v_max3_u32 v180, v180, v132, v133\n\t"
      "v_smfmac_f32_16x16x64_bf16 v[172:175], v[128:131], a[16:23], %[idx]\n\t"
      "v_max3_u32 v180, v180, v134, v135\n\t"
      "v_cmp_eq_u32 vcc, -1, v180\n\t"
      "v_smfmac_f32_16x16x64_bf16 v[176:179], v[128:131], a[24:31], %[idx]\n\t"
      "s_cbranch_vccnz 20f\n\t"
      "s_waitcnt lgkmcnt(0)\n\t"
      "21:\n\t"
      "s_barrier\n\t"
      "ds_read_b128 v[136:139], %[ma] offset:2048\n\t"
      "ds_read_b128 v[140:143], %[ma] offset:3072\n\t"
      "v_smfmac_f32_16x16x64_bf16 v[164:167], v[132:135], a[32:39], %[idx]\n\t"
      "ds_read_b128 v[144:147], %[ma] offset:4096\n\t"
      "ds_read_b128 v[148:151], %[ma] offset:5120\n\t"
      "v_smfmac_f32_16x16x64_bf16 v[168:171], v[132:135], a[40:47], %[idx]\n\t"
      "ds_read_b128 v[152:155], %[ma] offset:6144\n\t"
      "ds_read_b128 v[156:159], %[ma] offset:7168\n\t"
      "v_smfmac_f32_16x16x64_bf16 v[172:175], v[132:135], a[48:55], %[idx]\n\t"
      "ds_read_b128 v[160:163], %[ma] offset:8192\n\t"
      "v_smfmac_f32_16x16x64_bf16 v[176:179], v[132:135], a[56:63], %[idx]\n\t"
      "s_waitcnt lgkmcnt(6)\n\t"
      "v_smfmac_f32_16x16x64_bf16 v[164:167], v[136:139], a[64:71], %[idx]\n\t"
      "v_smfmac_f32_16x16x64_bf16 v[168:171], v[136:139], a[72:79], %[idx]\n\t"
      "s_waitcnt lgkmcnt(5)\n\t"
      "v_smfmac_f32_16x16x64_bf16 v[164:167], v[140:143], a[96:103], %[idx]\n\t"
      "v_smfmac_f32_16x16x64_bf16 v[168:171], v[140:143], a[104:111], %[idx]\n\t"
      "s_waitcnt lgkmcnt(4)\n\t"
      "v_smfmac_f32_16x16x64_bf16 v[164:167], v[144:147], a[128:135], %[idx]\n\t"
      "v_smfmac_f32_16x16x64_bf16 v[168:171], v[144:147], a[136:143], %[idx]\n\t"
      "s_waitcnt lgkmcnt(3)\n\t"
      "v_smfmac_f32_16x16x64_bf16 v[164:167], v[148:151], a[160:167], %[idx]\n\t"
      "v_smfmac_f32_16x16x64_bf16 v[168:171], v[148:151], a[168:175], %[idx]\n\t"
      "s_waitcnt lgkmcnt(2)\n\t"
      "v_smfmac_f32_16x16x64_bf16 v[164:167], v[152:155], a[192:199], %[idx]\n\t"
      "v_smfmac_f32_16x16x64_bf16 v[168:171], v[152:155], a[200:207], %[idx]\n\t"
      "s_waitcnt lgkmcnt(1)\n\t"
      "v_smfmac_f32_16x16x64_bf16 v[164:167], v[156:159], a[224:231], %[idx]\n\t"
      "v_smfmac_f32_16x16x64_bf16 v[168:171], v[156:159], a[232:239], %[idx]\n\t"
      "s_waitcnt lgkmcnt(0)\n\t"
      "v_smfmac_f32_16x16x64_bf16 v[164:167], v[160:163], v[224:231], %[idx]\n\t"
      "v_smfmac_f32_16x16x64_bf16 v[168:171], v[160:163], v[232:239], %[idx]\n\t"
      "v_smfmac_f32_16x16x64_bf16 v[172:175], v[136:139], a[80:87], %[idx]\n\t"
      "s_nop 2\n\t"
      "v_smfmac_f32_16x16x64_bf16 v[176:179], v[136:139], a[88:95], %[idx]\n\t"
      "s_nop 1\n\t"
      "v_add_f32 v164, v164, v165\n\t"
      "v_smfmac_f32_16x16x64_bf16 v[172:175], v[140:143], a[112:119], %[idx]\n\t"
      "v_add_f32 v166, v166, v167\n\t"
      "v_add_f32 v168, v168, v169\n\t"
      "v_smfmac_f32_16x16x64_bf16 v[176:179], v[140:143], a[120:127], %[idx]\n\t"
      "v_add_f32 v170, v170, v171\n\t"
      "v_cvt_pk_bf16_f32 v165, v164, v166\n\t"
      "v_smfmac_f32_16x16x64_bf16 v[172:175], v[144:147], a[144:151], %[idx]\n\t"
      "v_cvt_pk_bf16_f32 v169, v168, v170\n\t"
      "ds_write2_b32 %[wa], v165, v169 offset0:0 offset1:160\n\t"
      "v_smfmac_f32_16x16x64_bf16 v[176:179], v[144:147], a[152:159], %[idx]\n\t"
      "v_smfmac_f32_16x16x64_bf16 v[172:175], v[148:151], a[176:183], %[idx]\n\t"
      "v_smfmac_f32_16x16x64_bf16 v[176:179], v[148:151], a[184:191], %[idx]\n\t"
      "v_smfmac_f32_16x16x64_bf16 v[172:175], v[152:155], a[208:215], %[idx]\n\t"
      "s_waitcnt lgkmcnt(0)\n\t"
      "s_barrier\n\t"
      "v_smfmac_f32_16x16x64_bf16 v[176:179], v[152:155], a[216:223], %[idx]\n\t"
      "ds_read_b64_tr_b16 v[112:113], %[ra] offset:0\n\t"
      "ds_read_b64_tr_b16 v[114:115], %[ra] offset:640\n\t"
      "v_smfmac_f32_16x16x64_bf16 v[172:175], v[156:159], a[240:247], %[idx]\n\t"
      "ds_read_b64_tr_b16 v[116:117], %[ra] offset:1280\n\t"
      "ds_read_b64_tr_b16 v[118:119], %[ra] offset:1920\n\t"
      "v_smfmac_f32_16x16x64_bf16 v[176:179], v[156:159], a[248:255], %[idx]\n\t"
      "ds_read_b64_tr_b16 v[120:121], %[ra] offset:2560\n\t"
      "ds_read_b64_tr_b16 v[122:123], %[ra] offset:3200\n\t"
      "v_smfmac_f32_16x16x64_bf16 v[172:175], v[160:163], v[240:247], %[idx]\n\t"
      "ds_read_b64_tr_b16 v[124:125], %[ra] offset:3840\n\t"
      "ds_read_b64_tr_b16 v[126:127], %[ra] offset:4480\n\t"
      "v_smfmac_f32_16x16x64_bf16 v[176:179], v[160:163], v[248:255], %[idx]\n\t"
      "s_nop 6\n\t"
      "v_add_f32 v172, v172, v173\n\t"
      "v_add_f32 v174, v174, v175\n\t"
      "v_add_f32 v176, v176, v177\n\t"
      "v_add_f32 v178, v178, v179\n\t"
      "v_cvt_pk_bf16_f32 v173, v172, v174\n\t"
      "v_cvt_pk_bf16_f32 v177, v176, v178\n\t"
      "ds_write2_b32 %[wa], v173, v177 offset0:4 offset1:164\n\t"
      "s_waitcnt lgkmcnt(1)\n\t"
      "v_mfma_f32_16x16x32_bf16 v[144:147], v[192:195], v[112:115], 0\n\t"
      "v_mfma_f32_16x16x32_bf16 v[148:151], v[208:211], v[112:115], 0\n\t"
      "v_mfma_f32_16x16x32_bf16 v[144:147], v[196:199], v[116:119], v[144:147]\n\t"
      "v_mfma_f32_16x16x32_bf16 v[148:151], v[212:215], v[116:119], v[148:151]\n\t"
      "s_waitcnt lgkmcnt(0)\n\t"
      "s_barrier\n\t"
      "ds_read_b64_tr_b16 v[128:129], %[ra] offset:16\n\t"
      "ds_read_b64_tr_b16 v[130:131], %[ra] offset:656\n\t"
      "v_mfma_f32_16x16x32_bf16 v[144:147], v[200:203], v[120:123], v[144:147]\n\t"
      "ds_read_b64_tr_b16 v[132:133], %[ra] offset:1296\n\t"
      "ds_read_b64_tr_b16 v[134:135], %[ra] offset:1936\n\t"
      "v_mfma_f32_16x16x32_bf16 v[148:151], v[216:219], v[120:123], v[148:151]\n\t"
      "ds_read_b64_tr_b16 v[136:137], %[ra] offset:2576\n\t"
      "ds_read_b64_tr_b16 v[138:139], %[ra] offset:3216\n\t"
      "v_mfma_f32_16x16x32_bf16 v[144:147], v[204:207], v[124:127], v[144:147]\n\t"
      "ds_read_b64_tr_b16 v[140:141], %[ra] offset:3856\n\t"
      "ds_read_b64_tr_b16 v[142:143], %[ra] offset:4496\n\t"
      "v_mfma_f32_16x16x32_bf16 v[148:151], v[220:223], v[124:127], v[148:151]\n\t"
      "s_nop 0\n\t"
      "s_waitcnt lgkmcnt(6)\n\t"
      "v_mfma_f32_16x16x32_bf16 v[152:155], v[128:131], v[112:115], %[ni]\n\t"
      "s_waitcnt lgkmcnt(4)\n\t"
      "v_mfma_f32_16x16x32_bf16 v[152:155], v[132:135], v[116:119], v[152:155]\n\t"
      "s_waitcnt lgkmcnt(2)\n\t"
      "v_mfma_f32_16x16x32_bf16 v[152:155], v[136:139], v[120:123], v[152:155]\n\t"
      "s_waitcnt lgkmcnt(0)\n\t"
      "v_mfma_f32_16x16x32_bf16 v[152:155], v[140:143], v[124:127], v[152:155]\n\t"
      "v_cndmask_b32 v176, v144, v146, %[b8]\n\t"
      "v_cndmask_b32 v177, v145, v147, %[b8]\n\t"
      "v_cndmask_b32 v178, v148, v150, %[b8]\n\t"
      "v_cndmask_b32 v179, v149, v151, %[b8]\n\t"
      "v_max3_f32 v182, v176, v177, v178\n\t"
      "v_max_f32 v182, v182, v179\n\t"
      "s_nop 1\n\t"
      "v_cndmask_b32 v180, v152, v154, %[b8]\n\t"
      "v_cndmask_b32 v181, v153, v155, %[b8]\n\t"
      "v_max3_f32 v182, v182, v180, v181\n\t"
      "s_nop 1\n\t"
      "v_max_f32_dpp v184, v182, v182 row_ror:8 row_mask:0xf bank_mask:0xf\n\t"
      "v_max_f32_dpp v185, v182, v182 row_ror:8 row_mask:0xf bank_mask:0xf\n\t"
      "s_nop 1\n\t"
      "v_permlane16_swap_b32 v184, v185\n\t"
      "v_max_f32 v186, v184, v185\n\t"
      "v_max_f32 v187, v184, v185\n\t"
      "s_nop 1\n\t"
      "v_permlane32_swap_b32 v186, v187\n\t"
      "v_max_f32 v186, v186, v187\n\t"
      "v_mul_f32 v186, 0xbe0293ee, v186\n\t"
      "v_fmamk_f32 v176, v176, 0x3e0293ee, v186\n\t"
      "v_fmamk_f32 v177, v177, 0x3e0293ee, v186\n\t"
      "v_fmamk_f32 v178, v178, 0x3e0293ee, v186\n\t"
      "v_fmamk_f32 v179, v179, 0x3e0293ee, v186\n\t"
      "v_fmamk_f32 v180, v180, 0x3e0293ee, v186\n\t"
      "v_fmamk_f32 v181, v181, 0x3e0293ee, v186\n\t"
      "v_exp_f32 v176, v176\n\t"
      "v_exp_f32 v177, v177\n\t"
      "v_exp_f32 v178, v178\n\t"
      "v_add_f32 v182, v176, v177\n\t"
      "v_exp_f32 v179, v179\n\t"
      "v_add_f32 v182, v182, v178\n\t"
      "v_exp_f32 v180, v180\n\t"
      "v_add_f32 v182, v182, v179\n\t"
      "v_exp_f32 v181, v181\n\t"
      "v_add_f32 v182, v182, v180\n\t"
      "v_add_f32 v182, v182, v181\n\t"
      "s_nop 1\n\t"
      "v_add_f32_dpp v184, v182, v182 row_ror:8 row_mask:0xf bank_mask:0xf\n\t"
      "v_add_f32_dpp v185, v182, v182 row_ror:8 row_mask:0xf bank_mask:0xf\n\t"
      "s_nop 1\n\t"
      "v_permlane16_swap_b32 v184, v185\n\t"
      "v_add_f32 v186, v184, v185\n\t"
      "v_add_f32 v187, v184, v185\n\t"
      "s_nop 1\n\t"
      "v_permlane32_swap_b32 v186, v187\n\t"
      "v_add_f32 v186, v186, v187\n\t"
      "v_rcp_f32 v186, v186\n\t"
      "s_nop 0\n\t"
      "v_mul_f32 v176, v176, v186\n\t"
      "v_mul_f32 v177, v177, v186\n\t"
      "v_mul_f32 v178, v178, v186\n\t"
      "v_mul_f32 v179, v179, v186\n\t"
      "v_cvt_pk_bf16_f32 v176, |v176|, |v177|\n\t"
      "v_cvt_pk_bf16_f32 v177, |v178|, |v179|\n\t"
      "buffer_store_dwordx2 v[176:177], %[px], %[srd], %[pb] offen sc1\n\t"
      "v_mul_f32 v180, v180, v186\n\t"
      "v_mul_f32 v181, v181, v186\n\t"
      "v_cvt_pk_bf16_f32 v180, |v180|, |v181|\n\t"
      "buffer_store_dword v180, %[pm], %[srd], %[pb] offen sc1\n\t"
      "s_memtime s[94:95]\n\t"
      "s_memrealtime s[64:65]\n\t"
      "s_nop 1\n\t"
      "s_memrealtime s[66:67]\n\t"
      "s_nop 1\n\t"
      "s_memrealtime s[68:69]\n\t"
      "s_nop 1\n\t"
      "s_memrealtime s[70:71]\n\t"
      "s_nop 1\n\t"
      "s_memrealtime s[72:73]\n\t"
      "s_nop 1\n\t"
      "s_memrealtime s[74:75]\n\t"
      "s_nop 1\n\t"
      "s_memrealtime s[76:77]\n\t"
      "s_nop 1\n\t"
      "s_memrealtime s[78:79]\n\t"
      "s_waitcnt lgkmcnt(0)\n\t"
      "s_add_u32 s64, s64, s66\n\t"
      "s_add_u32 s68, s68, s70\n\t"
      "s_add_u32 s72, s72, s74\n\t"
      "s_add_u32 s76, s76, s78\n\t"
      "s_add_u32 s64, s64, s68\n\t"
      "s_add_u32 s72, s72, s76\n\t"
      "s_add_u32 %[te], s64, s72\n\t"
      "s_sub_u32 %[du], s94, s92\n\t"
      "s_branch 29f\n\t"
      "20:\n\t"
      "s_mov_b32 s66, 0x20000\n\t"
      "s_mov_b32 %[flag], 1\n\t"
      "s_add_u32 s64, %[mb], %[da]\n\t"
      "s_add_u32 s65, %[mb], %[db]\n\t"
      "22:\n\t"
      "buffer_load_dwordx4 v[128:131], %[o0], %[srd], s64 offen sc1\n\t"
      "buffer_load_dwordx4 v[132:135], %[o0], %[srd], s64 offen offset:1024 sc1\n\t"
      "s_waitcnt vmcnt(0)\n\t"
      "v_max_u32 v160, v128, v129\n\t"
      "v_max3_u32 v160, v160, v130, v131\n\t"
      "v_max3_u32 v160, v160, v132, v133\n\t"
      "v_max3_u32 v160, v160, v134, v135\n\t"
      "v_cmp_eq_u32 vcc, -1, v160\n\t"
      "s_cbranch_vccz 23f\n\t"
      "s_sleep 1\n\t"
      "s_sub_u32 s66, s66, 1\n\t"
      "s_cbranch_scc0 22b\n\t"
      "s_endpgm\n\t"
      "23:\n\t"
      "ds_write_b128 %[ma], v[128:131] offset:0\n\t"
      "ds_write_b128 %[ma], v[132:135] offset:1024\n\t"
      "v_mov_b64 v[164:165], 0\n\t"
      "v_mov_b64 v[166:167], 0\n\t"
      "v_mov_b64 v[168:169], 0\n\t"
      "v_mov_b64 v[170:171], 0\n\t"
      "v_mov_b64 v[172:173], 0\n\t"
      "v_mov_b64 v[174:175], 0\n\t"
      "v_mov_b64 v[176:177], 0\n\t"
      "v_mov_b64 v[178:179], 0\n\t"
      "s_nop 1\n\t"
      "v_smfmac_f32_16x16x64_bf16 v[164:167], v[128:131], a[0:7], %[idx]\n\t"
      "v_smfmac_f32_16x16x64_bf16 v[168:171], v[128:131], a[8:15], %[idx]\n\t"
      "v_smfmac_f32_16x16x64_bf16 v[172:175], v[128:131], a[16:23], %[idx]\n\t"
      "v_smfmac_f32_16x16x64_bf16 v[176:179], v[128:131], a[24:31], %[idx]\n\t"
      "s_waitcnt lgkmcnt(0)\n\t"
      "s_branch 21b\n\t"
      "29:\n\t"
      : [flag] "=&s"(flag) , [te] "=&s"(te), [du] "=&s"(du)
      : [o0] "v"(gl0), [l16] "v"(l16), [srd] "s"(srd), [mb] "s"(US(mb)), [ma0] "s"(US(mb + ca)), [mb8] "s"(US(mb + cb)), [da] "s"(US(da)), [db] "s"(US(db)), [pb] "s"(US(pb)), [idx] "v"(idx), [ma] "v"(ma), [wa] "v"(wa), [ra] "v"(ra),
        [ni] "v"(ni), [b8] "s"(b8mask), [px] "v"(px_off), [pm] "v"(pm_off), WPIN(W)
      : "v64", "v65", "v66", "v67", "v68", "v69", "v70", "v71", "v72", "v73", "v74", "v75", "v76", "v77", "v78", "v79", "v80", "v81", "v82", "v83", "v84", "v85", "v86", "v87", "v88", "v89", "v90", "v91", "v92", "v93", "v94", "v95", "v112", "v113", "v114", "v115", "v116", "v117", "v118", "v119", "v120", "v121", "v122", "v123", "v124", "v125", "v126", "v127", "v128", "v129", "v130", "v131", "v132", "v133", "v134", "v135", "v136", "v137", "v138", "v139", "v140", "v141", "v142", "v143", "v144", "v145", "v146", "v147", "v148", "v149", "v150", "v151", "v152", "v153", "v154", "v155", "v156", "v157", "v158", "v159", "v160", "v161", "v162", "v163", "v164", "v165", "v166", "v167", "v168", "v169", "v170", "v171", "v172", "v173", "v174", "v175", "v176", "v177", "v178", "v179", "v180", "v181", "v182", "v183", "v184", "v185", "v186", "v187", "v192", "v193", "v194", "v195", "v196", "v197", "v198", "v199", "v200", "v201", "v202", "v203", "v204", "v205", "v206", "v207", "v208", "v209", "v210", "v211", "v212", "v213", "v214", "v215", "v216", "v217", "v218", "v219", "v220", "v221", "v222", "v223", "s64", "s65", "s66", "s67", "s68", "s69", "s70", "s71", "s72", "s73", "s74", "s75", "s76", "s77", "s78", "s79", "s80", "s81", "s82", "s83", "s84", "s85", "s86", "s87", "s88", "s89", "s90", "s91", "s92", "s93", "s94", "s95", "vcc", "scc", "memory");
      put_stamp(rs, pb, h, te, du, flag != 0, lane);
    }
  } else if (w == 3) {
    u32 tgt = rtc() * 8u - 2000u, prov = 0;
    bool have = false;
    for (int s = -1; s < NSEG; ++s) {
      u32 mb = mp_base(max(s, 0), par);
      asm volatile("" : "+s"(mb));
      if (s > 0) tgt = next_tgt(rs, srd, mp_base(s - 1, par), mb, tgt, prov, have, lane);
      asm volatile(
      "v_mov_b64 v[164:165], 0\n\t"
      "v_mov_b64 v[166:167], 0\n\t"
      "v_mov_b64 v[168:169], 0\n\t"
      "v_mov_b64 v[170:171], 0\n\t"
      "v_mov_b64 v[172:173], 0\n\t"
      "v_mov_b64 v[174:175], 0\n\t"
      "v_mov_b64 v[176:177], 0\n\t"
      "v_mov_b64 v[178:179], 0\n\t"
      "s_mov_b32 %[flag], 0\n\t"
      "1:\n\t"
      "s_memrealtime s[64:65]\n\t"
      "s_waitcnt lgkmcnt(0)\n\t"
      "s_lshl_b32 s64, s64, 3\n\t"
      "s_sub_i32 s64, %[tgt], s64\n\t"
      "s_cmp_gt_i32 s64, 175\n\t"
      "s_cbranch_scc1 1b\n\t"
      "s_cmp_lt_i32 s64, 48\n\t"
      "s_cbranch_scc1 2f\n\t"
      "s_sub_i32 s64, s64, 64\n\t"
      "s_cmp_lt_i32 s64, 51\n\t"
      "s_cbranch_scc1 4f\n\t"
      "s_memrealtime s[64:65]\n\t"
      "s_nop 1\n\t"
      "s_memrealtime s[66:67]\n\t"
      "s_nop 1\n\t"
      "s_memrealtime s[68:69]\n\t"
      "s_nop 1\n\t"
      "s_memrealtime s[70:71]\n\t"
      "s_nop 1\n\t"
      "s_memrealtime s[72:73]\n\t"
      "s_nop 1\n\t"
      "s_memrealtime s[74:75]\n\t"
      "s_nop 1\n\t"
      "s_memrealtime s[76:77]\n\t"
      "s_nop 1\n\t"
      "s_memrealtime s[78:79]\n\t"
      "s_waitcnt lgkmcnt(0)\n\t"
      "s_add_u32 s64, s64, s66\n\t"
      "s_add_u32 s68, s68, s70\n\t"
      "s_add_u32 s72, s72, s74\n\t"
      "s_add_u32 s76, s76, s78\n\t"
      "s_add_u32 s64, s64, s68\n\t"
      "s_add_u32 s72, s72, s76\n\t"
      "s_add_u32 s64, s64, s72\n\t"
      "s_sub_i32 s64, %[tgt], s64\n\t"
      "s_sub_i32 s64, s64, 75\n\t"
      "4:\n\t"
      "s_mul_i32 s64, s64, 3\n\t"
      "s_and_b32 s64, s64, -4\n\t"
      "s_max_i32 s64, s64, 0\n\t"
      "s_min_i32 s64, s64, 288\n\t"
      "s_getpc_b64 s[66:67]\n\t"
      "5:\n\t"
      "s_sub_i32 s64, 2f-5b, s64\n\t"
      "s_add_u32 s66, s66, s64\n\t"
      "s_addc_u32 s67, s67, 0\n\t"
      "s_setpc_b64 s[66:67]\n\t"
      "s_nop 0\n\t"
      "s_nop 0\n\t"
      "s_nop 0\n\t"
      "s_nop 0\n\t"
      "s_nop 0\n\t"
      "s_nop 0\n\t"
      "s_nop 0\n\t"
      "s_nop 0\n\t"
      "s_nop 0\n\t"
      "s_nop 0\n\t"
      "s_nop 0\n\t"
      "s_nop 0\n\t"
      "s_nop 0\n\t"
      "s_nop 0\n\t"
      "s_nop 0\n\t"
      "s_nop 0\n\t"
      "s_nop 0\n\t"
      "s_nop 0\n\t"
      "s_nop 0\n\t"
      "s_nop 0\n\t"
      "s_nop 0\n\t"
      "s_nop 0\n\t"
      "s_nop 0\n\t"
      "s_nop 0\n\t"
      "s_nop 0\n\t"
      "s_nop 0\n\t"
      "s_nop 0\n\t"
      "s_nop 0\n\t"
      "s_nop 0\n\t"
      "s_nop 0\n\t"
      "s_nop 0\n\t"
      "s_nop 0\n\t"
      "s_nop 0\n\t"
      "s_nop 0\n\t"
      "s_nop 0\n\t"
      "s_nop 0\n\t"
      "s_nop 0\n\t"
      "s_nop 0\n\t"
      "s_nop 0\n\t"
      "s_nop 0\n\t"
      "s_nop 0\n\t"
      "s_nop 0\n\t"
      "s_nop 0\n\t"
      "s_nop 0\n\t"
      "s_nop 0\n\t"
      "s_nop 0\n\t"
      "s_nop 0\n\t"
      "s_nop 0\n\t"
      "s_nop 0\n\t"
      "s_nop 0\n\t"
      "s_nop 0\n\t"
      "s_nop 0\n\t"
      "s_nop 0\n\t"
      "s_nop 0\n\t"
      "s_nop 0\n\t"
      "s_nop 0\n\t"
      "s_nop 0\n\t"
      "s_nop 0\n\t"
      "s_nop 0\n\t"
      "s_nop 0\n\t"
      "s_nop 0\n\t"
      "s_nop 0\n\t"
      "s_nop 0\n\t"
      "s_nop 0\n\t"
      "s_nop 0\n\t"
      "s_nop 0\n\t"
      "s_nop 0\n\t"
      "s_nop 0\n\t"
      "s_nop 0\n\t"
      "s_nop 0\n\t"
      "s_nop 0\n\t"
      "s_nop 0\n\t"
      "2:\n\t"
      "s_barrier\n\t"
      "buffer_load_dwordx4 v[128:131], %[o0], %[srd], %[ma0] offen sc1\n\t"
      "buffer_load_dwordx4 v[132:135], %[o0], %[srd], %[ma0] offen offset:1024 sc1\n\t"
      "buffer_load_dwordx4 v[136:139], %[o2], %[srd], %[mb8] offen sc1\n\t"
      "s_waitcnt vmcnt(2)\n\t"
      "ds_write_b128 %[ma], v[128:131] offset:6144\n\t"
      "v_smfmac_f32_16x16x64_bf16 v[164:167], v[128:131], a[192:199], %[idx]\n\t"
      "s_waitcnt vmcnt(1)\n\t"
      "ds_write_b128 %[ma], v[132:135] offset:7168\n\t"
      "v_max_u32 v180, v128, v129\n\t"
      "v_smfmac_f32_16x16x64_bf16 v[168:171], v[128:131], a[200:207], %[idx]\n\t"
      "s_waitcnt vmcnt(0)\n\t"
      "ds_write_b128 %[ma], v[136:139] offset:8192\n\t"
      "v_max3_u32 v180, v180, v130, v131\n\t"
      "v_max3_u32 v180, v180, v132, v133\n\t"
      "v_smfmac_f32_16x16x64_bf16 v[172:175], v[128:131], a[208:215], %[idx]\n\t"
      "v_max3_u32 v180, v180, v134, v135\n\t"
      "v_max3_u32 v180, v180, v136, v137\n\t"
      "v_max3_u32 v180, v180, v138, v139\n\t"
      "v_cmp_eq_u32 vcc, -1, v180\n\t"
      "v_smfmac_f32_16x16x64_bf16 v[176:179], v[128:131], a[216:223], %[idx]\n\t"
      "s_cbranch_vccnz 20f\n\t"
      "s_waitcnt lgkmcnt(0)\n\t"
      "21:\n\t"
      "s_barrier\n\t"
      "ds_read_b128 v[140:143], %[ma] offset:4096\n\t"
      "ds_read_b128 v[144:147], %[ma] offset:5120\n\t"
      "v_smfmac_f32_16x16x64_bf16 v[164:167], v[132:135], a[224:231], %[idx]\n\t"
      "ds_read_b128 v[148:151], %[ma] offset:2048\n\t"
      "ds_read_b128 v[152:155], %[ma] offset:3072\n\t"
      "v_smfmac_f32_16x16x64_bf16 v[168:171], v[132:135], a[232:239], %[idx]\n\t"
      "ds_read_b128 v[156:159], %[ma] offset:0\n\t"
      "ds_read_b128 v[160:163], %[ma] offset:1024\n\t"
      "v_smfmac_f32_16x16x64_bf16 v[172:175], v[132:135], a[240:247], %[idx]\n\t"
      "v_smfmac_f32_16x16x64_bf16 v[176:179], v[132:135], a[248:255], %[idx]\n\t"
      "v_smfmac_f32_16x16x64_bf16 v[164:167], v[136:139], v[224:231], %[idx]\n\t"
      "v_smfmac_f32_16x16x64_bf16 v[168:171], v[136:139], v[232:239], %[idx]\n\t"
      "s_waitcnt lgkmcnt(5)\n\t"
      "v_smfmac_f32_16x16x64_bf16 v[164:167], v[140:143], a[128:135], %[idx]\n\t"
      "v_smfmac_f32_16x16x64_bf16 v[168:171], v[140:143], a[136:143], %[idx]\n\t"
      "s_waitcnt lgkmcnt(4)\n\t"
      "v_smfmac_f32_16x16x64_bf16 v[164:167], v[144:147], a[160:167], %[idx]\n\t"
      "v_smfmac_f32_16x16x64_bf16 v[168:171], v[144:147], a[168:175], %[idx]\n\t"
      "s_waitcnt lgkmcnt(3)\n\t"
      "v_smfmac_f32_16x16x64_bf16 v[164:167], v[148:151], a[64:71], %[idx]\n\t"
      "v_smfmac_f32_16x16x64_bf16 v[168:171], v[148:151], a[72:79], %[idx]\n\t"
      "s_waitcnt lgkmcnt(2)\n\t"
      "v_smfmac_f32_16x16x64_bf16 v[164:167], v[152:155], a[96:103], %[idx]\n\t"
      "v_smfmac_f32_16x16x64_bf16 v[168:171], v[152:155], a[104:111], %[idx]\n\t"
      "s_waitcnt lgkmcnt(1)\n\t"
      "v_smfmac_f32_16x16x64_bf16 v[164:167], v[156:159], a[0:7], %[idx]\n\t"
      "v_smfmac_f32_16x16x64_bf16 v[168:171], v[156:159], a[8:15], %[idx]\n\t"
      "s_waitcnt lgkmcnt(0)\n\t"
      "v_smfmac_f32_16x16x64_bf16 v[164:167], v[160:163], a[32:39], %[idx]\n\t"
      "v_smfmac_f32_16x16x64_bf16 v[168:171], v[160:163], a[40:47], %[idx]\n\t"
      "v_smfmac_f32_16x16x64_bf16 v[172:175], v[136:139], v[240:247], %[idx]\n\t"
      "s_nop 2\n\t"
      "v_smfmac_f32_16x16x64_bf16 v[176:179], v[136:139], v[248:255], %[idx]\n\t"
      "s_nop 1\n\t"
      "v_add_f32 v164, v164, v165\n\t"
      "v_smfmac_f32_16x16x64_bf16 v[172:175], v[140:143], a[144:151], %[idx]\n\t"
      "v_add_f32 v166, v166, v167\n\t"
      "v_add_f32 v168, v168, v169\n\t"
      "v_smfmac_f32_16x16x64_bf16 v[176:179], v[140:143], a[152:159], %[idx]\n\t"
      "v_add_f32 v170, v170, v171\n\t"
      "v_cvt_pk_bf16_f32 v165, v164, v166\n\t"
      "v_smfmac_f32_16x16x64_bf16 v[172:175], v[144:147], a[176:183], %[idx]\n\t"
      "v_cvt_pk_bf16_f32 v169, v168, v170\n\t"
      "ds_write2_b32 %[wa], v165, v169 offset0:0 offset1:160\n\t"
      "v_smfmac_f32_16x16x64_bf16 v[176:179], v[144:147], a[184:191], %[idx]\n\t"
      "v_smfmac_f32_16x16x64_bf16 v[172:175], v[148:151], a[80:87], %[idx]\n\t"
      "v_smfmac_f32_16x16x64_bf16 v[176:179], v[148:151], a[88:95], %[idx]\n\t"
      "v_smfmac_f32_16x16x64_bf16 v[172:175], v[152:155], a[112:119], %[idx]\n\t"
      "s_waitcnt lgkmcnt(0)\n\t"
      "s_barrier\n\t"
      "v_smfmac_f32_16x16x64_bf16 v[176:179], v[152:155], a[120:127], %[idx]\n\t"
      "v_smfmac_f32_16x16x64_bf16 v[172:175], v[156:159], a[16:23], %[idx]\n\t"
      "v_smfmac_f32_16x16x64_bf16 v[176:179], v[156:159], a[24:31], %[idx]\n\t"
      "v_smfmac_f32_16x16x64_bf16 v[172:175], v[160:163], a[48:55], %[idx]\n\t"
      "v_smfmac_f32_16x16x64_bf16 v[176:179], v[160:163], a[56:63], %[idx]\n\t"
      "s_nop 6\n\t"
      "v_add_f32 v172, v172, v173\n\t"
      "v_add_f32 v174, v174, v175\n\t"
      "v_add_f32 v176, v176, v177\n\t"
      "v_add_f32 v178, v178, v179\n\t"
      "v_cvt_pk_bf16_f32 v173, v172, v174\n\t"
      "v_cvt_pk_bf16_f32 v177, v176, v178\n\t"
      "ds_write2_b32 %[wa], v173, v177 offset0:4 offset1:164\n\t"
      "s_waitcnt lgkmcnt(0)\n\t"
      "s_barrier\n\t"
      "s_branch 29f\n\t"
      "20:\n\t"
      "s_mov_b32 s66, 0x20000\n\t"
      "s_mov_b32 %[flag], 1\n\t"
      "s_add_u32 s64, %[mb], %[da]\n\t"
      "s_add_u32 s65, %[mb], %[db]\n\t"
      "22:\n\t"
      "buffer_load_dwordx4 v[128:131], %[o0], %[srd], s64 offen sc1\n\t"
      "buffer_load_dwordx4 v[132:135], %[o0], %[srd], s64 offen offset:1024 sc1\n\t"
      "buffer_load_dwordx4 v[136:139], %[o2], %[srd], s65 offen sc1\n\t"
      "s_waitcnt vmcnt(0)\n\t"
      "v_max_u32 v160, v128, v129\n\t"
      "v_max3_u32 v160, v160, v130, v131\n\t"
      "v_max3_u32 v160, v160, v132, v133\n\t"
      "v_max3_u32 v160, v160, v134, v135\n\t"
      "v_max3_u32 v160, v160, v136, v137\n\t"
      "v_max3_u32 v160, v160, v138, v139\n\t"
      "v_cmp_eq_u32 vcc, -1, v160\n\t"
      "s_cbranch_vccz 23f\n\t"
      "s_sleep 1\n\t"
      "s_sub_u32 s66, s66, 1\n\t"
      "s_cbranch_scc0 22b\n\t"
      "s_endpgm\n\t"
      "23:\n\t"
      "ds_write_b128 %[ma], v[128:131] offset:6144\n\t"
      "ds_write_b128 %[ma], v[132:135] offset:7168\n\t"
      "ds_write_b128 %[ma], v[136:139] offset:8192\n\t"
      "v_mov_b64 v[164:165], 0\n\t"
      "v_mov_b64 v[166:167], 0\n\t"
      "v_mov_b64 v[168:169], 0\n\t"
      "v_mov_b64 v[170:171], 0\n\t"
      "v_mov_b64 v[172:173], 0\n\t"
      "v_mov_b64 v[174:175], 0\n\t"
      "v_mov_b64 v[176:177], 0\n\t"
      "v_mov_b64 v[178:179], 0\n\t"
      "s_nop 1\n\t"
      "v_smfmac_f32_16x16x64_bf16 v[164:167], v[128:131], a[192:199], %[idx]\n\t"
      "v_smfmac_f32_16x16x64_bf16 v[168:171], v[128:131], a[200:207], %[idx]\n\t"
      "v_smfmac_f32_16x16x64_bf16 v[172:175], v[128:131], a[208:215], %[idx]\n\t"
      "v_smfmac_f32_16x16x64_bf16 v[176:179], v[128:131], a[216:223], %[idx]\n\t"
      "s_waitcnt lgkmcnt(0)\n\t"
      "s_branch 21b\n\t"
      "29:\n\t"
      : [flag] "=&s"(flag)
      : [tgt] "s"(US(tgt)), [o0] "v"(gl0), [o2] "v"(l16c), [srd] "s"(srd), [mb] "s"(US(mb)), [ma0] "s"(US(mb + ca)), [mb8] "s"(US(mb + cb)), [da] "s"(US(da)), [db] "s"(US(db)), [idx] "v"(idx), [ma] "v"(ma), [wa] "v"(wa), WPIN(W)
      : "v64", "v65", "v66", "v67", "v68", "v69", "v70", "v71", "v72", "v73", "v74", "v75", "v76", "v77", "v78", "v79", "v80", "v81", "v82", "v83", "v84", "v85", "v86", "v87", "v88", "v89", "v90", "v91", "v92", "v93", "v94", "v95", "v112", "v113", "v114", "v115", "v116", "v117", "v118", "v119", "v120", "v121", "v122", "v123", "v124", "v125", "v126", "v127", "v128", "v129", "v130", "v131", "v132", "v133", "v134", "v135", "v136", "v137", "v138", "v139", "v140", "v141", "v142", "v143", "v144", "v145", "v146", "v147", "v148", "v149", "v150", "v151", "v152", "v153", "v154", "v155", "v156", "v157", "v158", "v159", "v160", "v161", "v162", "v163", "v164", "v165", "v166", "v167", "v168", "v169", "v170", "v171", "v172", "v173", "v174", "v175", "v176", "v177", "v178", "v179", "v180", "v181", "v182", "v183", "v184", "v185", "v186", "v187", "v192", "v193", "v194", "v195", "v196", "v197", "v198", "v199", "v200", "v201", "v202", "v203", "v204", "v205", "v206", "v207", "v208", "v209", "v210", "v211", "v212", "v213", "v214", "v215", "v216", "v217", "v218", "v219", "v220", "v221", "v222", "v223", "s64", "s65", "s66", "s67", "s68", "s69", "s70", "s71", "s72", "s73", "s74", "s75", "s76", "s77", "s78", "s79", "s80", "s81", "s82", "s83", "s84", "s85", "s86", "s87", "s88", "s89", "s90", "s91", "s92", "s93", "s94", "s95", "vcc", "scc", "memory");
    }
  } else {
    u16v qf = {};
    int seen = 0;
    const u32 fl = fnear(h, par);
    for (int s = -1; s < NSEG; ++s) {
      const u32 mb = mp_base(max(s, 0), par);
      const bool pre = seen >= s + 2 || s + 1 >= NSEG;
      const u32 kq = kq_base(s + 1, par) + (u32)h * 32768u, kb = (pre && s + 1 < NSEG) ? kq : 0xF0000000u;
      const u32 xb = s >= 0 ? OFF_PX + (u32)s * 81920u : 0xF0000000u, xn = xb + 5u * 81920u;
      if (w == 1) asm volatile(
      "v_mov_b64 v[164:165], 0\n\t"
      "v_mov_b64 v[166:167], 0\n\t"
      "v_mov_b64 v[168:169], 0\n\t"
      "v_mov_b64 v[170:171], 0\n\t"
      "v_mov_b64 v[172:173], 0\n\t"
      "v_mov_b64 v[174:175], 0\n\t"
      "v_mov_b64 v[176:177], 0\n\t"
      "v_mov_b64 v[178:179], 0\n\t"
      "s_mov_b32 %[flag], 0\n\t"
      "s_add_u32 s86, %[kb], 0x0\n\t"
      "s_add_u32 s87, %[kb], 0x4000\n\t"
      "s_barrier\n\t"
      "buffer_load_dwordx4 v[128:131], %[o0], %[srd], %[ma0] offen sc1\n\t"
      "buffer_load_dwordx4 v[132:135], %[o0], %[srd], %[ma0] offen offset:1024 sc1\n\t"
      "ds_read_b128 v[192:195], %[l16] offset:0\n\t"
      "ds_read_b128 v[196:199], %[l16] offset:1024\n\t"
      "ds_read_b128 v[200:203], %[l16] offset:2048\n\t"
      "ds_read_b128 v[204:207], %[l16] offset:3072\n\t"
      "ds_read_b128 v[208:211], %[l16] offset:4096\n\t"
      "ds_read_b128 v[212:215], %[l16] offset:5120\n\t"
      "ds_read_b128 v[216:219], %[l16] offset:6144\n\t"
      "ds_read_b128 v[220:223], %[l16] offset:7168\n\t"
      "s_nop 15\n\t"
      "v_mov_b64 v[112:113], -1\n\t"
      "v_mov_b64 v[114:115], -1\n\t"
      "buffer_load_dwordx4 v[64:67], %[l16], %[srd], s86 offen sc1\n\t"
      "buffer_load_dwordx4 v[68:71], %[l16], %[srd], s86 offen offset:1024 sc1\n\t"
      "buffer_load_dwordx4 v[72:75], %[l16], %[srd], s86 offen offset:2048 sc1\n\t"
      "buffer_load_dwordx4 v[76:79], %[l16], %[srd], s86 offen offset:3072 sc1\n\t"
      "buffer_load_dwordx4 v[80:83], %[l16], %[srd], s87 offen sc1\n\t"
      "buffer_load_dwordx4 v[84:87], %[l16], %[srd], s87 offen offset:1024 sc1\n\t"
      "buffer_load_dwordx4 v[88:91], %[l16], %[srd], s87 offen offset:2048 sc1\n\t"
      "buffer_load_dwordx4 v[92:95], %[l16], %[srd], s87 offen offset:3072 sc1\n\t"
      "buffer_store_dwordx4 v[112:115], %[px], %[srd], %[xn] offen sc1\n\t"
      "buffer_store_dwordx2 v[112:113], %[pm], %[srd], %[xn] offen sc1\n\t"
      "s_waitcnt vmcnt(11)\n\t"
      "ds_write_b128 %[ma], v[128:131] offset:2048\n\t"
      "v_smfmac_f32_16x16x64_bf16 v[164:167], v[128:131], a[64:71], %[idx]\n\t"
      "s_waitcnt vmcnt(10)\n\t"
      "ds_write_b128 %[ma], v[132:135] offset:3072\n\t"
      "v_max_u32 v180, v128, v129\n\t"
      "v_smfmac_f32_16x16x64_bf16 v[168:171], v[128:131], a[72:79], %[idx]\n\t"
      "v_max3_u32 v180, v180, v130, v131\n\t"
      "v_max3_u32 v180, v180, v132, v133\n\t"
      "v_smfmac_f32_16x16x64_bf16 v[172:175], v[128:131], a[80:87], %[idx]\n\t"
      "v_max3_u32 v180, v180, v134, v135\n\t"
      "v_cmp_eq_u32 vcc, -1, v180\n\t"
      "v_smfmac_f32_16x16x64_bf16 v[176:179], v[128:131], a[88:95], %[idx]\n\t"
      "s_cbranch_vccnz 20f\n\t"
      "s_waitcnt lgkmcnt(0)\n\t"
      "21:\n\t"
      "s_barrier\n\t"
      "ds_read_b128 v[136:139], %[ma] offset:0\n\t"
      "ds_read_b128 v[140:143], %[ma] offset:1024\n\t"
      "v_smfmac_f32_16x16x64_bf16 v[164:167], v[132:135], a[96:103], %[idx]\n\t"
      "ds_read_b128 v[144:147], %[ma] offset:6144\n\t"
      "ds_read_b128 v[148:151], %[ma] offset:7168\n\t"
      "v_smfmac_f32_16x16x64_bf16 v[168:171], v[132:135], a[104:111], %[idx]\n\t"
      "ds_read_b128 v[152:155], %[ma] offset:4096\n\t"
      "ds_read_b128 v[156:159], %[ma] offset:5120\n\t"
      "v_smfmac_f32_16x16x64_bf16 v[172:175], v[132:135], a[112:119], %[idx]\n\t"
      "ds_read_b128 v[160:163], %[ma] offset:8192\n\t"
      "v_smfmac_f32_16x16x64_bf16 v[176:179], v[132:135], a[120:127], %[idx]\n\t"
      "s_waitcnt lgkmcnt(6)\n\t"
      "v_smfmac_f32_16x16x64_bf16 v[164:167], v[136:139], a[0:7], %[idx]\n\t"
      "v_smfmac_f32_16x16x64_bf16 v[168:171], v[136:139], a[8:15], %[idx]\n\t"
      "s_waitcnt lgkmcnt(5)\n\t"
      "v_smfmac_f32_16x16x64_bf16 v[164:167], v[140:143], a[32:39], %[idx]\n\t"
      "v_smfmac_f32_16x16x64_bf16 v[168:171], v[140:143], a[40:47], %[idx]\n\t"
      "s_waitcnt lgkmcnt(4)\n\t"
      "v_smfmac_f32_16x16x64_bf16 v[164:167], v[144:147], a[192:199], %[idx]\n\t"
      "v_smfmac_f32_16x16x64_bf16 v[168:171], v[144:147], a[200:207], %[idx]\n\t"
      "s_waitcnt lgkmcnt(3)\n\t"
      "v_smfmac_f32_16x16x64_bf16 v[164:167], v[148:151], a[224:231], %[idx]\n\t"
      "v_smfmac_f32_16x16x64_bf16 v[168:171], v[148:151], a[232:239], %[idx]\n\t"
      "s_waitcnt lgkmcnt(2)\n\t"
      "v_smfmac_f32_16x16x64_bf16 v[164:167], v[152:155], a[128:135], %[idx]\n\t"
      "v_smfmac_f32_16x16x64_bf16 v[168:171], v[152:155], a[136:143], %[idx]\n\t"
      "s_waitcnt lgkmcnt(1)\n\t"
      "v_smfmac_f32_16x16x64_bf16 v[164:167], v[156:159], a[160:167], %[idx]\n\t"
      "v_smfmac_f32_16x16x64_bf16 v[168:171], v[156:159], a[168:175], %[idx]\n\t"
      "s_waitcnt lgkmcnt(0)\n\t"
      "v_smfmac_f32_16x16x64_bf16 v[164:167], v[160:163], v[224:231], %[idx]\n\t"
      "v_smfmac_f32_16x16x64_bf16 v[168:171], v[160:163], v[232:239], %[idx]\n\t"
      "v_smfmac_f32_16x16x64_bf16 v[172:175], v[136:139], a[16:23], %[idx]\n\t"
      "s_nop 2\n\t"
      "v_smfmac_f32_16x16x64_bf16 v[176:179], v[136:139], a[24:31], %[idx]\n\t"
      "s_nop 1\n\t"
      "v_add_f32 v164, v164, v165\n\t"
      "v_smfmac_f32_16x16x64_bf16 v[172:175], v[140:143], a[48:55], %[idx]\n\t"
      "v_add_f32 v166, v166, v167\n\t"
      "v_add_f32 v168, v168, v169\n\t"
      "v_smfmac_f32_16x16x64_bf16 v[176:179], v[140:143], a[56:63], %[idx]\n\t"
      "v_add_f32 v170, v170, v171\n\t"
      "v_cvt_pk_bf16_f32 v165, v164, v166\n\t"
      "v_smfmac_f32_16x16x64_bf16 v[172:175], v[144:147], a[208:215], %[idx]\n\t"
      "v_cvt_pk_bf16_f32 v169, v168, v170\n\t"
      "ds_write2_b32 %[wa], v165, v169 offset0:0 offset1:160\n\t"
      "v_smfmac_f32_16x16x64_bf16 v[176:179], v[144:147], a[216:223], %[idx]\n\t"
      "v_smfmac_f32_16x16x64_bf16 v[172:175], v[148:151], a[240:247], %[idx]\n\t"
      "v_smfmac_f32_16x16x64_bf16 v[176:179], v[148:151], a[248:255], %[idx]\n\t"
      "v_smfmac_f32_16x16x64_bf16 v[172:175], v[152:155], a[144:151], %[idx]\n\t"
      "s_waitcnt lgkmcnt(0)\n\t"
      "s_barrier\n\t"
      "v_smfmac_f32_16x16x64_bf16 v[176:179], v[152:155], a[152:159], %[idx]\n\t"
      "v_smfmac_f32_16x16x64_bf16 v[172:175], v[156:159], a[176:183], %[idx]\n\t"
      "v_smfmac_f32_16x16x64_bf16 v[176:179], v[156:159], a[184:191], %[idx]\n\t"
      "v_smfmac_f32_16x16x64_bf16 v[172:175], v[160:163], v[240:247], %[idx]\n\t"
      "v_smfmac_f32_16x16x64_bf16 v[176:179], v[160:163], v[248:255], %[idx]\n\t"
      "s_nop 6\n\t"
      "v_add_f32 v172, v172, v173\n\t"
      "v_add_f32 v174, v174, v175\n\t"
      "v_add_f32 v176, v176, v177\n\t"
      "v_add_f32 v178, v178, v179\n\t"
      "v_cvt_pk_bf16_f32 v173, v172, v174\n\t"
      "v_cvt_pk_bf16_f32 v177, v176, v178\n\t"
      "ds_write2_b32 %[wa], v173, v177 offset0:4 offset1:164\n\t"
      "s_waitcnt lgkmcnt(0)\n\t"
      "s_barrier\n\t"
      "buffer_load_dword v116, off, %[srd], %[fl] sc1\n\t"
      "ds_read_b64_tr_b16 v[128:129], %[ra] offset:16\n\t"
      "ds_read_b64_tr_b16 v[130:131], %[ra] offset:656\n\t"
      "ds_read_b64_tr_b16 v[132:133], %[ra] offset:1296\n\t"
      "ds_read_b64_tr_b16 v[134:135], %[ra] offset:1936\n\t"
      "ds_read_b64_tr_b16 v[136:137], %[ra] offset:2576\n\t"
      "ds_read_b64_tr_b16 v[138:139], %[ra] offset:3216\n\t"
      "ds_read_b64_tr_b16 v[140:141], %[ra] offset:3856\n\t"
      "ds_read_b64_tr_b16 v[142:143], %[ra] offset:4496\n\t"
      "v_mfma_f32_16x16x32_bf16 v[148:151], v[192:195], v[96:99], 0\n\t"
      "v_mfma_f32_16x16x32_bf16 v[148:151], v[196:199], v[100:103], v[148:151]\n\t"
      "v_mfma_f32_16x16x32_bf16 v[148:151], v[200:203], v[104:107], v[148:151]\n\t"
      "v_mfma_f32_16x16x32_bf16 v[148:151], v[204:207], v[108:111], v[148:151]\n\t"
      "s_waitcnt lgkmcnt(0)\n\t"
      "v_mfma_f32_16x16x32_bf16 v[144:147], v[128:131], v[96:99], %[ni]\n\t"
      "v_mfma_f32_16x16x32_bf16 v[144:147], v[132:135], v[100:103], v[144:147]\n\t"
      "v_mfma_f32_16x16x32_bf16 v[144:147], v[136:139], v[104:107], v[144:147]\n\t"
      "v_mfma_f32_16x16x32_bf16 v[144:147], v[140:143], v[108:111], v[144:147]\n\t"
      "s_mov_b32 s64, 0xf0000\n\t"
      "s_mov_b32 s65, 0xfff00ff\n\t"
      "s_mov_b32 s66, 0x1f0001\n\t"
      "s_mov_b32 s67, 0x1fff01ff\n\t"
      "s_mov_b32 s68, 0x3f0003\n\t"
      "s_mov_b32 s69, 0x3fff03ff\n\t"
      "s_mov_b32 s70, 0x7f0007\n\t"
      "s_mov_b32 s71, 0x7fff07ff\n\t"
      "v_mov_b32 v156, 0xd0289660\n\t"
      "v_cndmask_b32 v148, v148, v156, s[64:65]\n\t"
      "v_cndmask_b32 v149, v149, v156, s[66:67]\n\t"
      "v_cndmask_b32 v150, v150, v156, s[68:69]\n\t"
      "v_cndmask_b32 v151, v151, v156, s[70:71]\n\t"
      "v_mov_b32 v152, v156\n\t"
      "v_mov_b32 v153, v156\n\t"
      "v_mov_b32 v154, v156\n\t"
      "v_mov_b32 v155, v156\n\t"
      "v_max_f32 v157, v144, v145\n\t"
      "v_max3_f32 v157, v157, v146, v147\n\t"
      "v_max3_f32 v157, v157, v148, v149\n\t"
      "v_max3_f32 v157, v157, v150, v151\n\t"
      "v_max3_f32 v157, v157, v152, v153\n\t"
      "v_max3_f32 v157, v157, v154, v155\n\t"
      "v_mov_b32 v158, v157\n\t"
      "s_nop 1\n\t"
      "v_permlane16_swap_b32 v157, v158\n\t"
      "v_max_f32 v157, v157, v158\n\t"
      "v_mov_b32 v158, v157\n\t"
      "s_nop 1\n\t"
      "v_permlane32_swap_b32 v157, v158\n\t"
      "v_max_f32 v157, v157, v158\n\t"
      "v_mul_f32 v157, 0xbe0293ee, v157\n\t"
      "v_fmamk_f32 v144, v144, 0x3e0293ee, v157\n\t"
      "v_fmamk_f32 v145, v145, 0x3e0293ee, v157\n\t"
      "v_fmamk_f32 v146, v146, 0x3e0293ee, v157\n\t"
      "v_fmamk_f32 v147, v147, 0x3e0293ee, v157\n\t"
      "v_fmamk_f32 v148, v148, 0x3e0293ee, v157\n\t"
      "v_fmamk_f32 v149, v149, 0x3e0293ee, v157\n\t"
      "v_fmamk_f32 v150, v150, 0x3e0293ee, v157\n\t"
      "v_fmamk_f32 v151, v151, 0x3e0293ee, v157\n\t"
      "v_fmamk_f32 v152, v152, 0x3e0293ee, v157\n\t"
      "v_fmamk_f32 v153, v153, 0x3e0293ee, v157\n\t"
      "v_fmamk_f32 v154, v154, 0x3e0293ee, v157\n\t"
      "v_fmamk_f32 v155, v155, 0x3e0293ee, v157\n\t"
      "v_exp_f32 v144, v144\n\t"
      "v_exp_f32 v145, v145\n\t"
      "v_exp_f32 v146, v146\n\t"
      "v_exp_f32 v147, v147\n\t"
      "v_exp_f32 v148, v148\n\t"
      "v_exp_f32 v149, v149\n\t"
      "v_exp_f32 v150, v150\n\t"
      "v_exp_f32 v151, v151\n\t"
      "v_exp_f32 v152, v152\n\t"
      "v_exp_f32 v153, v153\n\t"
      "v_exp_f32 v154, v154\n\t"
      "v_exp_f32 v155, v155\n\t"
      "v_add_f32 v157, v144, v145\n\t"
      "v_add_f32 v157, v157, v146\n\t"
      "v_add_f32 v157, v157, v147\n\t"
      "v_add_f32 v157, v157, v148\n\t"
      "v_add_f32 v157, v157, v149\n\t"
      "v_add_f32 v157, v157, v150\n\t"
      "v_add_f32 v157, v157, v151\n\t"
      "v_add_f32 v157, v157, v152\n\t"
      "v_add_f32 v157, v157, v153\n\t"
      "v_add_f32 v157, v157, v154\n\t"
      "v_add_f32 v157, v157, v155\n\t"
      "v_mov_b32 v158, v157\n\t"
      "s_nop 1\n\t"
      "v_permlane16_swap_b32 v157, v158\n\t"
      "v_add_f32 v157, v157, v158\n\t"
      "v_mov_b32 v158, v157\n\t"
      "s_nop 1\n\t"
      "v_permlane32_swap_b32 v157, v158\n\t"
      "v_add_f32 v157, v157, v158\n\t"
      "v_rcp_f32 v157, v157\n\t"
      "s_nop 0\n\t"
      "v_mul_f32 v144, v144, v157\n\t"
      "v_mul_f32 v145, v145, v157\n\t"
      "v_mul_f32 v146, v146, v157\n\t"
      "v_mul_f32 v147, v147, v157\n\t"
      "v_mul_f32 v148, v148, v157\n\t"
      "v_mul_f32 v149, v149, v157\n\t"
      "v_mul_f32 v150, v150, v157\n\t"
      "v_mul_f32 v151, v151, v157\n\t"
      "v_mul_f32 v152, v152, v157\n\t"
      "v_mul_f32 v153, v153, v157\n\t"
      "v_mul_f32 v154, v154, v157\n\t"
      "v_mul_f32 v155, v155, v157\n\t"
      "v_cvt_pk_bf16_f32 v160, |v148|, |v149|\n\t"
      "v_cvt_pk_bf16_f32 v161, |v150|, |v151|\n\t"
      "v_cvt_pk_bf16_f32 v162, |v152|, |v153|\n\t"
      "v_cvt_pk_bf16_f32 v163, |v154|, |v155|\n\t"
      "v_cvt_pk_bf16_f32 v158, |v144|, |v145|\n\t"
      "v_cvt_pk_bf16_f32 v159, |v146|, |v147|\n\t"
      "buffer_store_dwordx4 v[160:163], %[px], %[srd], %[xb] offen sc1\n\t"
      "buffer_store_dwordx2 v[158:159], %[pm], %[srd], %[xb] offen sc1\n\t"
      "s_waitcnt vmcnt(2)\n\t"
      "ds_write_b128 %[l16], v[64:67] offset:0\n\t"
      "ds_write_b128 %[l16], v[68:71] offset:1024\n\t"
      "ds_write_b128 %[l16], v[72:75] offset:2048\n\t"
      "ds_write_b128 %[l16], v[76:79] offset:3072\n\t"
      "v_mov_b64 v[96:97], v[80:81]\n\t"
      "v_mov_b64 v[98:99], v[82:83]\n\t"
      "v_mov_b64 v[100:101], v[84:85]\n\t"
      "v_mov_b64 v[102:103], v[86:87]\n\t"
      "v_mov_b64 v[104:105], v[88:89]\n\t"
      "v_mov_b64 v[106:107], v[90:91]\n\t"
      "v_mov_b64 v[108:109], v[92:93]\n\t"
      "v_mov_b64 v[110:111], v[94:95]\n\t"
      "v_readfirstlane_b32 %[te], v116\n\t"
      "s_waitcnt lgkmcnt(0)\n\t"
      "s_branch 29f\n\t"
      "20:\n\t"
      "s_mov_b32 s66, 0x20000\n\t"
      "s_mov_b32 %[flag], 1\n\t"
      "s_add_u32 s64, %[mb], %[da]\n\t"
      "s_add_u32 s65, %[mb], %[db]\n\t"
      "22:\n\t"
      "buffer_load_dwordx4 v[128:131], %[o0], %[srd], s64 offen sc1\n\t"
      "buffer_load_dwordx4 v[132:135], %[o0], %[srd], s64 offen offset:1024 sc1\n\t"
      "s_waitcnt vmcnt(0)\n\t"
      "v_max_u32 v160, v128, v129\n\t"
      "v_max3_u32 v160, v160, v130, v131\n\t"
      "v_max3_u32 v160, v160, v132, v133\n\t"
      "v_max3_u32 v160, v160, v134, v135\n\t"
      "v_cmp_eq_u32 vcc, -1, v160\n\t"
      "s_cbranch_vccz 23f\n\t"
      "s_sleep 1\n\t"
      "s_sub_u32 s66, s66, 1\n\t"
      "s_cbranch_scc0 22b\n\t"
      "s_endpgm\n\t"
      "23:\n\t"
      "ds_write_b128 %[ma], v[128:131] offset:2048\n\t"
      "ds_write_b128 %[ma], v[132:135] offset:3072\n\t"
      "v_mov_b64 v[164:165], 0\n\t"
      "v_mov_b64 v[166:167], 0\n\t"
      "v_mov_b64 v[168:169], 0\n\t"
      "v_mov_b64 v[170:171], 0\n\t"
      "v_mov_b64 v[172:173], 0\n\t"
      "v_mov_b64 v[174:175], 0\n\t"
      "v_mov_b64 v[176:177], 0\n\t"
      "v_mov_b64 v[178:179], 0\n\t"
      "s_nop 1\n\t"
      "v_smfmac_f32_16x16x64_bf16 v[164:167], v[128:131], a[64:71], %[idx]\n\t"
      "v_smfmac_f32_16x16x64_bf16 v[168:171], v[128:131], a[72:79], %[idx]\n\t"
      "v_smfmac_f32_16x16x64_bf16 v[172:175], v[128:131], a[80:87], %[idx]\n\t"
      "v_smfmac_f32_16x16x64_bf16 v[176:179], v[128:131], a[88:95], %[idx]\n\t"
      "s_waitcnt lgkmcnt(0)\n\t"
      "s_branch 21b\n\t"
      "29:\n\t"
      : [flag] "=&s"(flag), [te] "=&s"(te) , "+{v[96:111]}"(qf)
      : [o0] "v"(gl0), [l16] "v"(l16), [kb] "s"(US(kb)), [fl] "s"(US(fl)), [xb] "s"(US(xb)), [xn] "s"(US(xn)), [srd] "s"(srd), [mb] "s"(US(mb)), [ma0] "s"(US(mb + ca)), [mb8] "s"(US(mb + cb)), [da] "s"(US(da)), [db] "s"(US(db)), [idx] "v"(idx), [ma] "v"(ma), [wa] "v"(wa), [ra] "v"(ra),
        [ni] "v"(ni), [px] "v"(px_off), [pm] "v"(pm_off), WPIN(W)
      : "v64", "v65", "v66", "v67", "v68", "v69", "v70", "v71", "v72", "v73", "v74", "v75", "v76", "v77", "v78", "v79", "v80", "v81", "v82", "v83", "v84", "v85", "v86", "v87", "v88", "v89", "v90", "v91", "v92", "v93", "v94", "v95", "v112", "v113", "v114", "v115", "v116", "v117", "v118", "v119", "v120", "v121", "v122", "v123", "v124", "v125", "v126", "v127", "v128", "v129", "v130", "v131", "v132", "v133", "v134", "v135", "v136", "v137", "v138", "v139", "v140", "v141", "v142", "v143", "v144", "v145", "v146", "v147", "v148", "v149", "v150", "v151", "v152", "v153", "v154", "v155", "v156", "v157", "v158", "v159", "v160", "v161", "v162", "v163", "v164", "v165", "v166", "v167", "v168", "v169", "v170", "v171", "v172", "v173", "v174", "v175", "v176", "v177", "v178", "v179", "v180", "v181", "v182", "v183", "v184", "v185", "v186", "v187", "v192", "v193", "v194", "v195", "v196", "v197", "v198", "v199", "v200", "v201", "v202", "v203", "v204", "v205", "v206", "v207", "v208", "v209", "v210", "v211", "v212", "v213", "v214", "v215", "v216", "v217", "v218", "v219", "v220", "v221", "v222", "v223", "s64", "s65", "s66", "s67", "s68", "s69", "s70", "s71", "s72", "s73", "s74", "s75", "s76", "s77", "s78", "s79", "s80", "s81", "s82", "s83", "s84", "s85", "s86", "s87", "s88", "s89", "s90", "s91", "s92", "s93", "s94", "s95", "vcc", "scc", "memory");
      else asm volatile(
      "v_mov_b64 v[164:165], 0\n\t"
      "v_mov_b64 v[166:167], 0\n\t"
      "v_mov_b64 v[168:169], 0\n\t"
      "v_mov_b64 v[170:171], 0\n\t"
      "v_mov_b64 v[172:173], 0\n\t"
      "v_mov_b64 v[174:175], 0\n\t"
      "v_mov_b64 v[176:177], 0\n\t"
      "v_mov_b64 v[178:179], 0\n\t"
      "s_mov_b32 %[flag], 0\n\t"
      "s_add_u32 s86, %[kb], 0x1000\n\t"
      "s_add_u32 s87, %[kb], 0x5000\n\t"
      "s_barrier\n\t"
      "buffer_load_dwordx4 v[128:131], %[o0], %[srd], %[ma0] offen sc1\n\t"
      "buffer_load_dwordx4 v[132:135], %[o0], %[srd], %[ma0] offen offset:1024 sc1\n\t"
      "ds_read_b128 v[192:195], %[l16] offset:0\n\t"
      "ds_read_b128 v[196:199], %[l16] offset:1024\n\t"
      "ds_read_b128 v[200:203], %[l16] offset:2048\n\t"
      "ds_read_b128 v[204:207], %[l16] offset:3072\n\t"
      "ds_read_b128 v[208:211], %[l16] offset:4096\n\t"
      "ds_read_b128 v[212:215], %[l16] offset:5120\n\t"
      "ds_read_b128 v[216:219], %[l16] offset:6144\n\t"
      "ds_read_b128 v[220:223], %[l16] offset:7168\n\t"
      "s_nop 15\n\t"
      "v_mov_b64 v[112:113], -1\n\t"
      "v_mov_b64 v[114:115], -1\n\t"
      "buffer_load_dwordx4 v[64:67], %[l16], %[srd], s86 offen sc1\n\t"
      "buffer_load_dwordx4 v[68:71], %[l16], %[srd], s86 offen offset:1024 sc1\n\t"
      "buffer_load_dwordx4 v[72:75], %[l16], %[srd], s86 offen offset:2048 sc1\n\t"
      "buffer_load_dwordx4 v[76:79], %[l16], %[srd], s86 offen offset:3072 sc1\n\t"
      "buffer_load_dwordx4 v[80:83], %[l16], %[srd], s87 offen sc1\n\t"
      "buffer_load_dwordx4 v[84:87], %[l16], %[srd], s87 offen offset:1024 sc1\n\t"
      "buffer_load_dwordx4 v[88:91], %[l16], %[srd], s87 offen offset:2048 sc1\n\t"
      "buffer_load_dwordx4 v[92:95], %[l16], %[srd], s87 offen offset:3072 sc1\n\t"
      "buffer_store_dwordx4 v[112:115], %[px], %[srd], %[xn] offen sc1\n\t"
      "buffer_store_dwordx2 v[112:113], %[pm], %[srd], %[xn] offen sc1\n\t"
      "s_waitcnt vmcnt(11)\n\t"
      "ds_write_b128 %[ma], v[128:131] offset:4096\n\t"
      "v_smfmac_f32_16x16x64_bf16 v[164:167], v[128:131], a[128:135], %[idx]\n\t"
      "s_waitcnt vmcnt(10)\n\t"
      "ds_write_b128 %[ma], v[132:135] offset:5120\n\t"
      "v_max_u32 v180, v128, v129\n\t"
      "v_smfmac_f32_16x16x64_bf16 v[168:171], v[128:131], a[136:143], %[idx]\n\t"
      "v_max3_u32 v180, v180, v130, v131\n\t"
      "v_max3_u32 v180, v180, v132, v133\n\t"
      "v_smfmac_f32_16x16x64_bf16 v[172:175], v[128:131], a[144:151], %[idx]\n\t"
      "v_max3_u32 v180, v180, v134, v135\n\t"
      "v_cmp_eq_u32 vcc, -1, v180\n\t"
      "v_smfmac_f32_16x16x64_bf16 v[176:179], v[128:131], a[152:159], %[idx]\n\t"
      "s_cbranch_vccnz 20f\n\t"
      "s_waitcnt lgkmcnt(0)\n\t"
      "21:\n\t"
      "s_barrier\n\t"
      "ds_read_b128 v[136:139], %[ma] offset:6144\n\t"
      "ds_read_b128 v[140:143], %[ma] offset:7168\n\t"
      "v_smfmac_f32_16x16x64_bf16 v[164:167], v[132:135], a[160:167], %[idx]\n\t"
      "ds_read_b128 v[144:147], %[ma] offset:0\n\t"
      "ds_read_b128 v[148:151], %[ma] offset:1024\n\t"
      "v_smfmac_f32_16x16x64_bf16 v[168:171], v[132:135], a[168:175], %[idx]\n\t"
      "ds_read_b128 v[152:155], %[ma] offset:2048\n\t"
      "ds_read_b128 v[156:159], %[ma] offset:3072\n\t"
      "v_smfmac_f32_16x16x64_bf16 v[172:175], v[132:135], a[176:183], %[idx]\n\t"
      "ds_read_b128 v[160:163], %[ma] offset:8192\n\t"
      "v_smfmac_f32_16x16x64_bf16 v[176:179], v[132:135], a[184:191], %[idx]\n\t"
      "s_waitcnt lgkmcnt(6)\n\t"
      "v_smfmac_f32_16x16x64_bf16 v[164:167], v[136:139], a[192:199], %[idx]\n\t"
      "v_smfmac_f32_16x16x64_bf16 v[168:171], v[136:139], a[200:207], %[idx]\n\t"
      "s_waitcnt lgkmcnt(5)\n\t"
      "v_smfmac_f32_16x16x64_bf16 v[164:167], v[140:143], a[224:231], %[idx]\n\t"
      "v_smfmac_f32_16x16x64_bf16 v[168:171], v[140:143], a[232:239], %[idx]\n\t"
      "s_waitcnt lgkmcnt(4)\n\t"
      "v_smfmac_f32_16x16x64_bf16 v[164:167], v[144:147], a[0:7], %[idx]\n\t"
      "v_smfmac_f32_16x16x64_bf16 v[168:171], v[144:147], a[8:15], %[idx]\n\t"
      "s_waitcnt lgkmcnt(3)\n\t"
      "v_smfmac_f32_16x16x64_bf16 v[164:167], v[148:151], a[32:39], %[idx]\n\t"
      "v_smfmac_f32_16x16x64_bf16 v[168:171], v[148:151], a[40:47], %[idx]\n\t"
      "s_waitcnt lgkmcnt(2)\n\t"
      "v_smfmac_f32_16x16x64_bf16 v[164:167], v[152:155], a[64:71], %[idx]\n\t"
      "v_smfmac_f32_16x16x64_bf16 v[168:171], v[152:155], a[72:79], %[idx]\n\t"
      "s_waitcnt lgkmcnt(1)\n\t"
      "v_smfmac_f32_16x16x64_bf16 v[164:167], v[156:159], a[96:103], %[idx]\n\t"
      "v_smfmac_f32_16x16x64_bf16 v[168:171], v[156:159], a[104:111], %[idx]\n\t"
      "s_waitcnt lgkmcnt(0)\n\t"
      "v_smfmac_f32_16x16x64_bf16 v[164:167], v[160:163], v[224:231], %[idx]\n\t"
      "v_smfmac_f32_16x16x64_bf16 v[168:171], v[160:163], v[232:239], %[idx]\n\t"
      "v_smfmac_f32_16x16x64_bf16 v[172:175], v[136:139], a[208:215], %[idx]\n\t"
      "s_nop 2\n\t"
      "v_smfmac_f32_16x16x64_bf16 v[176:179], v[136:139], a[216:223], %[idx]\n\t"
      "s_nop 1\n\t"
      "v_add_f32 v164, v164, v165\n\t"
      "v_smfmac_f32_16x16x64_bf16 v[172:175], v[140:143], a[240:247], %[idx]\n\t"
      "v_add_f32 v166, v166, v167\n\t"
      "v_add_f32 v168, v168, v169\n\t"
      "v_smfmac_f32_16x16x64_bf16 v[176:179], v[140:143], a[248:255], %[idx]\n\t"
      "v_add_f32 v170, v170, v171\n\t"
      "v_cvt_pk_bf16_f32 v165, v164, v166\n\t"
      "v_smfmac_f32_16x16x64_bf16 v[172:175], v[144:147], a[16:23], %[idx]\n\t"
      "v_cvt_pk_bf16_f32 v169, v168, v170\n\t"
      "ds_write2_b32 %[wa], v165, v169 offset0:0 offset1:160\n\t"
      "v_smfmac_f32_16x16x64_bf16 v[176:179], v[144:147], a[24:31], %[idx]\n\t"
      "v_smfmac_f32_16x16x64_bf16 v[172:175], v[148:151], a[48:55], %[idx]\n\t"
      "v_smfmac_f32_16x16x64_bf16 v[176:179], v[148:151], a[56:63], %[idx]\n\t"
      "v_smfmac_f32_16x16x64_bf16 v[172:175], v[152:155], a[80:87], %[idx]\n\t"
      "s_waitcnt lgkmcnt(0)\n\t"
      "s_barrier\n\t"
      "v_smfmac_f32_16x16x64_bf16 v[176:179], v[152:155], a[88:95], %[idx]\n\t"
      "v_smfmac_f32_16x16x64_bf16 v[172:175], v[156:159], a[112:119], %[idx]\n\t"
      "v_smfmac_f32_16x16x64_bf16 v[176:179], v[156:159], a[120:127], %[idx]\n\t"
      "v_smfmac_f32_16x16x64_bf16 v[172:175], v[160:163], v[240:247], %[idx]\n\t"
      "v_smfmac_f32_16x16x64_bf16 v[176:179], v[160:163], v[248:255], %[idx]\n\t"
      "s_nop 6\n\t"
      "v_add_f32 v172, v172, v173\n\t"
      "v_add_f32 v174, v174, v175\n\t"
      "v_add_f32 v176, v176, v177\n\t"
      "v_add_f32 v178, v178, v179\n\t"
      "v_cvt_pk_bf16_f32 v173, v172, v174\n\t"
      "v_cvt_pk_bf16_f32 v177, v176, v178\n\t"
      "ds_write2_b32 %[wa], v173, v177 offset0:4 offset1:164\n\t"
      "s_waitcnt lgkmcnt(0)\n\t"
      "s_barrier\n\t"
      "buffer_load_dword v116, off, %[srd], %[fl] sc1\n\t"
      "ds_read_b64_tr_b16 v[128:129], %[ra] offset:16\n\t"
      "ds_read_b64_tr_b16 v[130:131], %[ra] offset:656\n\t"
      "ds_read_b64_tr_b16 v[132:133], %[ra] offset:1296\n\t"
      "ds_read_b64_tr_b16 v[134:135], %[ra] offset:1936\n\t"
      "ds_read_b64_tr_b16 v[136:137], %[ra] offset:2576\n\t"
      "ds_read_b64_tr_b16 v[138:139], %[ra] offset:3216\n\t"
      "ds_read_b64_tr_b16 v[140:141], %[ra] offset:3856\n\t"
      "ds_read_b64_tr_b16 v[142:143], %[ra] offset:4496\n\t"
      "v_mfma_f32_16x16x32_bf16 v[148:151], v[192:195], v[96:99], 0\n\t"
      "v_mfma_f32_16x16x32_bf16 v[152:155], v[208:211], v[96:99], 0\n\t"
      "v_mfma_f32_16x16x32_bf16 v[148:151], v[196:199], v[100:103], v[148:151]\n\t"
      "v_mfma_f32_16x16x32_bf16 v[152:155], v[212:215], v[100:103], v[152:155]\n\t"
      "v_mfma_f32_16x16x32_bf16 v[148:151], v[200:203], v[104:107], v[148:151]\n\t"
      "v_mfma_f32_16x16x32_bf16 v[152:155], v[216:219], v[104:107], v[152:155]\n\t"
      "v_mfma_f32_16x16x32_bf16 v[148:151], v[204:207], v[108:111], v[148:151]\n\t"
      "v_mfma_f32_16x16x32_bf16 v[152:155], v[220:223], v[108:111], v[152:155]\n\t"
      "s_waitcnt lgkmcnt(0)\n\t"
      "v_mfma_f32_16x16x32_bf16 v[144:147], v[128:131], v[96:99], %[ni]\n\t"
      "v_mfma_f32_16x16x32_bf16 v[144:147], v[132:135], v[100:103], v[144:147]\n\t"
      "v_mfma_f32_16x16x32_bf16 v[144:147], v[136:139], v[104:107], v[144:147]\n\t"
      "v_mfma_f32_16x16x32_bf16 v[144:147], v[140:143], v[108:111], v[144:147]\n\t"
      "s_mov_b32 s64, 0xf0000\n\t"
      "s_mov_b32 s65, 0xfff00ff\n\t"
      "s_mov_b32 s66, 0x1f0001\n\t"
      "s_mov_b32 s67, 0x1fff01ff\n\t"
      "s_mov_b32 s68, 0x3f0003\n\t"
      "s_mov_b32 s69, 0x3fff03ff\n\t"
      "s_mov_b32 s70, 0x7f0007\n\t"
      "s_mov_b32 s71, 0x7fff07ff\n\t"
      "v_mov_b32 v156, 0xd0289660\n\t"
      "v_cndmask_b32 v152, v152, v156, s[64:65]\n\t"
      "v_cndmask_b32 v153, v153, v156, s[66:67]\n\t"
      "v_cndmask_b32 v154, v154, v156, s[68:69]\n\t"
      "v_cndmask_b32 v155, v155, v156, s[70:71]\n\t"
      "v_max_f32 v157, v144, v145\n\t"
      "v_max3_f32 v157, v157, v146, v147\n\t"
      "v_max3_f32 v157, v157, v148, v149\n\t"
      "v_max3_f32 v157, v157, v150, v151\n\t"
      "v_max3_f32 v157, v157, v152, v153\n\t"
      "v_max3_f32 v157, v157, v154, v155\n\t"
      "v_mov_b32 v158, v157\n\t"
      "s_nop 1\n\t"
      "v_permlane16_swap_b32 v157, v158\n\t"
      "v_max_f32 v157, v157, v158\n\t"
      "v_mov_b32 v158, v157\n\t"
      "s_nop 1\n\t"
      "v_permlane32_swap_b32 v157, v158\n\t"
      "v_max_f32 v157, v157, v158\n\t"
      "v_mul_f32 v157, 0xbe0293ee, v157\n\t"
      "v_fmamk_f32 v144, v144, 0x3e0293ee, v157\n\t"
      "v_fmamk_f32 v145, v145, 0x3e0293ee, v157\n\t"
      "v_fmamk_f32 v146, v146, 0x3e0293ee, v157\n\t"
      "v_fmamk_f32 v147, v147, 0x3e0293ee, v157\n\t"
      "v_fmamk_f32 v148, v148, 0x3e0293ee, v157\n\t"
      "v_fmamk_f32 v149, v149, 0x3e0293ee, v157\n\t"
      "v_fmamk_f32 v150, v150, 0x3e0293ee, v157\n\t"
      "v_fmamk_f32 v151, v151, 0x3e0293ee, v157\n\t"
      "v_fmamk_f32 v152, v152, 0x3e0293ee, v157\n\t"
      "v_fmamk_f32 v153, v153, 0x3e0293ee, v157\n\t"
      "v_fmamk_f32 v154, v154, 0x3e0293ee, v157\n\t"
      "v_fmamk_f32 v155, v155, 0x3e0293ee, v157\n\t"
      "v_exp_f32 v144, v144\n\t"
      "v_exp_f32 v145, v145\n\t"
      "v_exp_f32 v146, v146\n\t"
      "v_exp_f32 v147, v147\n\t"
      "v_exp_f32 v148, v148\n\t"
      "v_exp_f32 v149, v149\n\t"
      "v_exp_f32 v150, v150\n\t"
      "v_exp_f32 v151, v151\n\t"
      "v_exp_f32 v152, v152\n\t"
      "v_exp_f32 v153, v153\n\t"
      "v_exp_f32 v154, v154\n\t"
      "v_exp_f32 v155, v155\n\t"
      "v_add_f32 v157, v144, v145\n\t"
      "v_add_f32 v157, v157, v146\n\t"
      "v_add_f32 v157, v157, v147\n\t"
      "v_add_f32 v157, v157, v148\n\t"
      "v_add_f32 v157, v157, v149\n\t"
      "v_add_f32 v157, v157, v150\n\t"
      "v_add_f32 v157, v157, v151\n\t"
      "v_add_f32 v157, v157, v152\n\t"
      "v_add_f32 v157, v157, v153\n\t"
      "v_add_f32 v157, v157, v154\n\t"
      "v_add_f32 v157, v157, v155\n\t"
      "v_mov_b32 v158, v157\n\t"
      "s_nop 1\n\t"
      "v_permlane16_swap_b32 v157, v158\n\t"
      "v_add_f32 v157, v157, v158\n\t"
      "v_mov_b32 v158, v157\n\t"
      "s_nop 1\n\t"
      "v_permlane32_swap_b32 v157, v158\n\t"
      "v_add_f32 v157, v157, v158\n\t"
      "v_rcp_f32 v157, v157\n\t"
      "s_nop 0\n\t"
      "v_mul_f32 v144, v144, v157\n\t"
      "v_mul_f32 v145, v145, v157\n\t"
      "v_mul_f32 v146, v146, v157\n\t"
      "v_mul_f32 v147, v147, v157\n\t"
      "v_mul_f32 v148, v148, v157\n\t"
      "v_mul_f32 v149, v149, v157\n\t"
      "v_mul_f32 v150, v150, v157\n\t"
      "v_mul_f32 v151, v151, v157\n\t"
      "v_mul_f32 v152, v152, v157\n\t"
      "v_mul_f32 v153, v153, v157\n\t"
      "v_mul_f32 v154, v154, v157\n\t"
      "v_mul_f32 v155, v155, v157\n\t"
      "v_cvt_pk_bf16_f32 v160, |v148|, |v149|\n\t"
      "v_cvt_pk_bf16_f32 v161, |v150|, |v151|\n\t"
      "v_cvt_pk_bf16_f32 v162, |v152|, |v153|\n\t"
      "v_cvt_pk_bf16_f32 v163, |v154|, |v155|\n\t"
      "v_cvt_pk_bf16_f32 v158, |v144|, |v145|\n\t"
      "v_cvt_pk_bf16_f32 v159, |v146|, |v147|\n\t"
      "buffer_store_dwordx4 v[160:163], %[px], %[srd], %[xb] offen sc1\n\t"
      "buffer_store_dwordx2 v[158:159], %[pm], %[srd], %[xb] offen sc1\n\t"
      "s_waitcnt vmcnt(2)\n\t"
      "ds_write_b128 %[l16], v[64:67] offset:4096\n\t"
      "ds_write_b128 %[l16], v[68:71] offset:5120\n\t"
      "ds_write_b128 %[l16], v[72:75] offset:6144\n\t"
      "ds_write_b128 %[l16], v[76:79] offset:7168\n\t"
      "v_mov_b64 v[96:97], v[80:81]\n\t"
      "v_mov_b64 v[98:99], v[82:83]\n\t"
      "v_mov_b64 v[100:101], v[84:85]\n\t"
      "v_mov_b64 v[102:103], v[86:87]\n\t"
      "v_mov_b64 v[104:105], v[88:89]\n\t"
      "v_mov_b64 v[106:107], v[90:91]\n\t"
      "v_mov_b64 v[108:109], v[92:93]\n\t"
      "v_mov_b64 v[110:111], v[94:95]\n\t"
      "v_readfirstlane_b32 %[te], v116\n\t"
      "s_waitcnt lgkmcnt(0)\n\t"
      "s_branch 29f\n\t"
      "20:\n\t"
      "s_mov_b32 s66, 0x20000\n\t"
      "s_mov_b32 %[flag], 1\n\t"
      "s_add_u32 s64, %[mb], %[da]\n\t"
      "s_add_u32 s65, %[mb], %[db]\n\t"
      "22:\n\t"
      "buffer_load_dwordx4 v[128:131], %[o0], %[srd], s64 offen sc1\n\t"
      "buffer_load_dwordx4 v[132:135], %[o0], %[srd], s64 offen offset:1024 sc1\n\t"
      "s_waitcnt vmcnt(0)\n\t"
      "v_max_u32 v160, v128, v129\n\t"
      "v_max3_u32 v160, v160, v130, v131\n\t"
      "v_max3_u32 v160, v160, v132, v133\n\t"
      "v_max3_u32 v160, v160, v134, v135\n\t"
      "v_cmp_eq_u32 vcc, -1, v160\n\t"
      "s_cbranch_vccz 23f\n\t"
      "s_sleep 1\n\t"
      "s_sub_u32 s66, s66, 1\n\t"
      "s_cbranch_scc0 22b\n\t"
      "s_endpgm\n\t"
      "23:\n\t"
      "ds_write_b128 %[ma], v[128:131] offset:4096\n\t"
      "ds_write_b128 %[ma], v[132:135] offset:5120\n\t"
      "v_mov_b64 v[164:165], 0\n\t"
      "v_mov_b64 v[166:167], 0\n\t"
      "v_mov_b64 v[168:169], 0\n\t"
      "v_mov_b64 v[170:171], 0\n\t"
      "v_mov_b64 v[172:173], 0\n\t"
      "v_mov_b64 v[174:175], 0\n\t"
      "v_mov_b64 v[176:177], 0\n\t"
      "v_mov_b64 v[178:179], 0\n\t"
      "s_nop 1\n\t"
      "v_smfmac_f32_16x16x64_bf16 v[164:167], v[128:131], a[128:135], %[idx]\n\t"
      "v_smfmac_f32_16x16x64_bf16 v[168:171], v[128:131], a[136:143], %[idx]\n\t"
      "v_smfmac_f32_16x16x64_bf16 v[172:175], v[128:131], a[144:151], %[idx]\n\t"
      "v_smfmac_f32_16x16x64_bf16 v[176:179], v[128:131], a[152:159], %[idx]\n\t"
      "s_waitcnt lgkmcnt(0)\n\t"
      "s_branch 21b\n\t"
      "29:\n\t"
      : [flag] "=&s"(flag), [te] "=&s"(te) , "+{v[96:111]}"(qf)
      : [o0] "v"(gl0), [l16] "v"(l16), [kb] "s"(US(kb)), [fl] "s"(US(fl)), [xb] "s"(US(xb)), [xn] "s"(US(xn)), [srd] "s"(srd), [mb] "s"(US(mb)), [ma0] "s"(US(mb + ca)), [mb8] "s"(US(mb + cb)), [da] "s"(US(da)), [db] "s"(US(db)), [idx] "v"(idx), [ma] "v"(ma), [wa] "v"(wa), [ra] "v"(ra),
        [ni] "v"(ni), [px] "v"(px_off), [pm] "v"(pm_off), WPIN(W)
      : "v64", "v65", "v66", "v67", "v68", "v69", "v70", "v71", "v72", "v73", "v74", "v75", "v76", "v77", "v78", "v79", "v80", "v81", "v82", "v83", "v84", "v85", "v86", "v87", "v88", "v89", "v90", "v91", "v92", "v93", "v94", "v95", "v112", "v113", "v114", "v115", "v116", "v117", "v118", "v119", "v120", "v121", "v122", "v123", "v124", "v125", "v126", "v127", "v128", "v129", "v130", "v131", "v132", "v133", "v134", "v135", "v136", "v137", "v138", "v139", "v140", "v141", "v142", "v143", "v144", "v145", "v146", "v147", "v148", "v149", "v150", "v151", "v152", "v153", "v154", "v155", "v156", "v157", "v158", "v159", "v160", "v161", "v162", "v163", "v164", "v165", "v166", "v167", "v168", "v169", "v170", "v171", "v172", "v173", "v174", "v175", "v176", "v177", "v178", "v179", "v180", "v181", "v182", "v183", "v184", "v185", "v186", "v187", "v192", "v193", "v194", "v195", "v196", "v197", "v198", "v199", "v200", "v201", "v202", "v203", "v204", "v205", "v206", "v207", "v208", "v209", "v210", "v211", "v212", "v213", "v214", "v215", "v216", "v217", "v218", "v219", "v220", "v221", "v222", "v223", "s64", "s65", "s66", "s67", "s68", "s69", "s70", "s71", "s72", "s73", "s74", "s75", "s76", "s77", "s78", "s79", "s80", "s81", "s82", "s83", "s84", "s85", "s86", "s87", "s88", "s89", "s90", "s91", "s92", "s93", "s94", "s95", "vcc", "scc", "memory");
      seen = __builtin_amdgcn_readfirstlane(te);
      if (!pre) {
        if (s < 0 && w == 1) { EVT(0) }
        if (!wait_flags(rs, fl, s + 2, seen)) return;
        if (s < 2 && w == 1) { EVT(2 + s) }
        const u32 k0 = kq + (w - 1) * 4096u, q0 = k0 + 16384u, kw = l16 + (w - 1) * 4096u;
        asm volatile("s_nop 4\n\tbuffer_load_dwordx4 v[64:67], %1, %2, %3 offen sc1\n\tbuffer_load_dwordx4 v[68:71], %1, %2, %3 offen offset:1024 sc1\n\tbuffer_load_dwordx4 v[72:75], %1, %2, %3 offen offset:2048 sc1\n\t"
                     "buffer_load_dwordx4 v[76:79], %1, %2, %3 offen offset:3072 sc1\n\tbuffer_load_dwordx4 v[96:99], %1, %2, %4 offen sc1\n\tbuffer_load_dwordx4 v[100:103], %1, %2, %4 offen offset:1024 sc1\n\t"
                     "buffer_load_dwordx4 v[104:107], %1, %2, %4 offen offset:2048 sc1\n\tbuffer_load_dwordx4 v[108:111], %1, %2, %4 offen offset:3072 sc1\n\ts_waitcnt vmcnt(4)\n\t"
                     "ds_write_b128 %5, v[64:67]\n\tds_write_b128 %5, v[68:71] offset:1024\n\tds_write_b128 %5, v[72:75] offset:2048\n\tds_write_b128 %5, v[76:79] offset:3072\n\ts_waitcnt vmcnt(0) lgkmcnt(0)"
                     : "=&{v[96:111]}"(qf) : "v"(l16), "s"(srd), "s"(US(k0)), "s"(US(q0)), "v"(kw)
                     : "v64", "v65", "v66", "v67", "v68", "v69", "v70", "v71", "v72", "v73", "v74", "v75", "v76", "v77", "v78", "v79", "memory");
      }
    }
  }
}

DI void role_VY(const Params& p, int h, u2 e) {
  const int tid = threadIdx.x, lane = tid & 63, w = __builtin_amdgcn_readfirstlane(tid >> 6);
  const int c = lane & 15, g = lane >> 4;
  u32v W[9];
  load_wvo<1>(W, p, h, w, lane);
  const u32 par = get_par(p, mk_tag(p, e), 1);
  const __amdgpu_buffer_rsrc_t rs = mkrsrc(p.ws, WS_SPAN);
  const u4 srd = mksrd(p.ws, WS_SPAN);
  const int idx = (c & 1) ? 0xEEEE : 0x4444;
  const u32 l16 = lane * 16, gl0 = l16 + w * 2048, l16c = 16384u + l16, ma = 147456u + l16;
  const u32 wa = 156672u + (16 * w + c) * 40 + g * 8;
  const int q4 = (lane >> 2) & 3, p4 = lane & 3;
  const u32 ra = 156672u + (8 * g + q4) * 40 + p4 * 8;
  const u32 y_off = (u32)(4 * g + w) * 16384u + h * 256 + c * 16;
  const u32 y_off8 = (u32)(32 + w) * 16384u + h * 256 + c * 16 + g * 4;
  const int cx = blockIdx.x & 3, cy = cx == 2 ? 0 : cx + 1;
  const u32 ca = cx == 0 ? 0u : cx == 1 ? 65536u : 114688u, cb = cx * 1024u, da = cy == 0 ? 0u : cy == 1 ? 65536u : 114688u, db = cy * 1024u;
  u32* stamp = (u32*)(p.ws + OFF_CTL) + CTL_STAMP + 32 * 64 * 4;
  u32 t0 = 0, t1 = 0, t2 = 0, te, du, flag;
  LOOPSTART
  if (w == 3) {
    u32 tgt = rtc() * 8u - 2000u, prov = 0;
    bool have = false;
    for (int s = 0; s < NSEG; ++s) {
      u32 mb = mp_base(s, par), yb = y_base(s, par), yn = y_base(s + 2, par);
      asm volatile("" : "+s"(mb), "+s"(yb), "+s"(yn));
      if (s > 0) tgt = next_tgt(rs, srd, mp_base(s - 1, par), mb, tgt, prov, have, lane);
      asm volatile(
      "v_mov_b64 v[164:165], 0\n\t"
      "v_mov_b64 v[166:167], 0\n\t"
      "v_mov_b64 v[168:169], 0\n\t"
      "v_mov_b64 v[170:171], 0\n\t"
      "v_mov_b64 v[184:185], 0\n\t"
      "v_mov_b64 v[186:187], 0\n\t"
      "v_mov_b64 v[188:189], 0\n\t"
      "v_mov_b64 v[190:191], 0\n\t"
      "v_mov_b64 v[192:193], 0\n\t"
      "v_mov_b64 v[194:195], 0\n\t"
      "v_mov_b64 v[196:197], 0\n\t"
      "v_mov_b64 v[198:199], 0\n\t"
      "v_mov_b64 v[200:201], 0\n\t"
      "v_mov_b64 v[202:203], 0\n\t"
      "v_mov_b64 v[204:205], 0\n\t"
      "v_mov_b64 v[206:207], 0\n\t"
      "v_mov_b64 v[208:209], 0\n\t"
      "v_mov_b64 v[210:211], 0\n\t"
      "v_mov_b64 v[212:213], 0\n\t"
      "v_mov_b64 v[214:215], 0\n\t"
      "v_mov_b64 v[216:217], 0\n\t"
      "v_mov_b64 v[218:219], 0\n\t"
      "s_mov_b32 %[flag], 0\n\t"
      "s_add_u32 s88, %[yb], 0x40000\n\t"
      "s_add_u32 s89, %[yn], 0x40000\n\t"
      "v_mov_b64 v[176:177], -1\n\t"
      "v_mov_b64 v[178:179], -1\n\t"
      "1:\n\t"
      "s_memrealtime s[64:65]\n\t"
      "s_waitcnt lgkmcnt(0)\n\t"
      "s_lshl_b32 s64, s64, 3\n\t"
      "s_sub_i32 s64, %[tgt], s64\n\t"
      "s_cmp_gt_i32 s64, 175\n\t"
      "s_cbranch_scc1 1b\n\t"
      "s_cmp_lt_i32 s64, 48\n\t"
      "s_cbranch_scc1 2f\n\t"
      "s_sub_i32 s64, s64, 64\n\t"
      "s_cmp_lt_i32 s64, 51\n\t"
      "s_cbranch_scc1 4f\n\t"
      "s_memrealtime s[64:65]\n\t"
      "s_nop 1\n\t"
      "s_memrealtime s[66:67]\n\t"
      "s_nop 1\n\t"
      "s_memrealtime s[68:69]\n\t"
      "s_nop 1\n\t"
      "s_memrealtime s[70:71]\n\t"
      "s_nop 1\n\t"
      "s_memrealtime s[72:73]\n\t"
      "s_nop 1\n\t"
      "s_memrealtime s[74:75]\n\t"
      "s_nop 1\n\t"
      "s_memrealtime s[76:77]\n\t"
      "s_nop 1\n\t"
      "s_memrealtime s[78:79]\n\t"
      "s_waitcnt lgkmcnt(0)\n\t"
      "s_add_u32 s64, s64, s66\n\t"
      "s_add_u32 s68, s68, s70\n\t"
      "s_add_u32 s72, s72, s74\n\t"
      "s_add_u32 s76, s76, s78\n\t"
      "s_add_u32 s64, s64, s68\n\t"
      "s_add_u32 s72, s72, s76\n\t"
      "s_add_u32 s64, s64, s72\n\t"
      "s_sub_i32 s64, %[tgt], s64\n\t"
      "s_sub_i32 s64, s64, 75\n\t"
      "4:\n\t"
      "s_mul_i32 s64, s64, 3\n\t"
      "s_and_b32 s64, s64, -4\n\t"
      "s_max_i32 s64, s64, 0\n\t"
      "s_min_i32 s64, s64, 288\n\t"
      "s_getpc_b64 s[66:67]\n\t"
      "5:\n\t"
      "s_sub_i32 s64, 2f-5b, s64\n\t"
      "s_add_u32 s66, s66, s64\n\t"
      "s_addc_u32 s67, s67, 0\n\t"
      "s_setpc_b64 s[66:67]\n\t"
      "s_nop 0\n\t"
      "s_nop 0\n\t"
      "s_nop 0\n\t"
      "s_nop 0\n\t"
      "s_nop 0\n\t"
      "s_nop 0\n\t"
      "s_nop 0\n\t"
      "s_nop 0\n\t"
      "s_nop 0\n\t"
      "s_nop 0\n\t"
      "s_nop 0\n\t"
      "s_nop 0\n\t"
      "s_nop 0\n\t"
      "s_nop 0\n\t"
      "s_nop 0\n\t"
      "s_nop 0\n\t"
      "s_nop 0\n\t"
      "s_nop 0\n\t"
      "s_nop 0\n\t"
      "s_nop 0\n\t"
      "s_nop 0\n\t"
      "s_nop 0\n\t"
      "s_nop 0\n\t"
      "s_nop 0\n\t"
      "s_nop 0\n\t"
      "s_nop 0\n\t"
      "s_nop 0\n\t"
      "s_nop 0\n\t"
      "s_nop 0\n\t"
      "s_nop 0\n\t"
      "s_nop 0\n\t"
      "s_nop 0\n\t"
      "s_nop 0\n\t"
      "s_nop 0\n\t"
      "s_nop 0\n\t"
      "s_nop 0\n\t"
      "s_nop 0\n\t"
      "s_nop 0\n\t"
      "s_nop 0\n\t"
      "s_nop 0\n\t"
      "s_nop 0\n\t"
      "s_nop 0\n\t"
      "s_nop 0\n\t"
      "s_nop 0\n\t"
      "s_nop 0\n\t"
      "s_nop 0\n\t"
      "s_nop 0\n\t"
      "s_nop 0\n\t"
      "s_nop 0\n\t"
      "s_nop 0\n\t"
      "s_nop 0\n\t"
      "s_nop 0\n\t"
      "s_nop 0\n\t"
      "s_nop 0\n\t"
      "s_nop 0\n\t"
      "s_nop 0\n\t"
      "s_nop 0\n\t"
      "s_nop 0\n\t"
      "s_nop 0\n\t"
      "s_nop 0\n\t"
      "s_nop 0\n\t"
      "s_nop 0\n\t"
      "s_nop 0\n\t"
      "s_nop 0\n\t"
      "s_nop 0\n\t"
      "s_nop 0\n\t"
      "s_nop 0\n\t"
      "s_nop 0\n\t"
      "s_nop 0\n\t"
      "s_nop 0\n\t"
      "s_nop 0\n\t"
      "s_nop 0\n\t"
      "2:\n\t"
      "s_barrier\n\t"
      "buffer_load_dwordx4 v[128:131], %[o0], %[srd], %[ma0] offen sc1\n\t"
      "buffer_load_dwordx4 v[132:135], %[o0], %[srd], %[ma0] offen offset:1024 sc1\n\t"
      "buffer_load_dwordx4 v[136:139], %[o2], %[srd], %[mb8] offen sc1\n\t"
      "s_nop 15\n\t"
      "s_nop 15\n\t"
      "buffer_store_dwordx4 v[176:179], %[yo], %[srd], %[yn] offen sc1\n\t"
      "buffer_store_dwordx4 v[176:179], %[yo], %[srd], s89 offen sc1\n\t"
      "buffer_store_dword v176, %[yo8], %[srd], %[yn] offen sc1\n\t"
      "s_waitcnt vmcnt(5)\n\t"
      "ds_write_b128 %[ma], v[128:131] offset:6144\n\t"
      "s_waitcnt vmcnt(4)\n\t"
      "ds_write_b128 %[ma], v[132:135] offset:7168\n\t"
      "s_waitcnt vmcnt(3)\n\t"
      "ds_write_b128 %[ma], v[136:139] offset:8192\n\t"
      "v_max_u32 v174, v128, v129\n\t"
      "v_max3_u32 v174, v174, v130, v131\n\t"
      "v_max3_u32 v174, v174, v132, v133\n\t"
      "v_max3_u32 v174, v174, v134, v135\n\t"
      "v_max3_u32 v174, v174, v136, v137\n\t"
      "v_max3_u32 v174, v174, v138, v139\n\t"
      "v_cmp_eq_u32 vcc, -1, v174\n\t"
      "s_cbranch_vccnz 20f\n\t"
      "s_waitcnt lgkmcnt(0)\n\t"
      "21:\n\t"
      "s_barrier\n\t"
      "ds_read_b128 v[140:143], %[ma] offset:4096\n\t"
      "ds_read_b128 v[144:147], %[ma] offset:5120\n\t"
      "v_smfmac_f32_16x16x64_bf16 v[164:167], v[128:131], a[96:103], %[idx]\n\t"
      "ds_read_b128 v[148:151], %[ma] offset:2048\n\t"
      "ds_read_b128 v[152:155], %[ma] offset:3072\n\t"
      "v_smfmac_f32_16x16x64_bf16 v[168:171], v[128:131], a[104:111], %[idx]\n\t"
      "ds_read_b128 v[156:159], %[ma] offset:0\n\t"
      "ds_read_b128 v[160:163], %[ma] offset:1024\n\t"
      "v_smfmac_f32_16x16x64_bf16 v[164:167], v[132:135], a[112:119], %[idx]\n\t"
      "v_smfmac_f32_16x16x64_bf16 v[168:171], v[132:135], a[120:127], %[idx]\n\t"
      "v_smfmac_f32_16x16x64_bf16 v[164:167], v[136:139], a[128:135], %[idx]\n\t"
      "v_smfmac_f32_16x16x64_bf16 v[168:171], v[136:139], a[136:143], %[idx]\n\t"
      "s_waitcnt lgkmcnt(5)\n\t"
      "v_smfmac_f32_16x16x64_bf16 v[164:167], v[140:143], a[64:71], %[idx]\n\t"
      "v_smfmac_f32_16x16x64_bf16 v[168:171], v[140:143], a[72:79], %[idx]\n\t"
      "s_waitcnt lgkmcnt(4)\n\t"
      "v_smfmac_f32_16x16x64_bf16 v[164:167], v[144:147], a[80:87], %[idx]\n\t"
      "v_smfmac_f32_16x16x64_bf16 v[168:171], v[144:147], a[88:95], %[idx]\n\t"
      "s_waitcnt lgkmcnt(3)\n\t"
      "v_smfmac_f32_16x16x64_bf16 v[164:167], v[148:151], a[32:39], %[idx]\n\t"
      "v_smfmac_f32_16x16x64_bf16 v[168:171], v[148:151], a[40:47], %[idx]\n\t"
      "s_waitcnt lgkmcnt(2)\n\t"
      "v_smfmac_f32_16x16x64_bf16 v[164:167], v[152:155], a[48:55], %[idx]\n\t"
      "v_smfmac_f32_16x16x64_bf16 v[168:171], v[152:155], a[56:63], %[idx]\n\t"
      "s_waitcnt lgkmcnt(1)\n\t"
      "v_smfmac_f32_16x16x64_bf16 v[164:167], v[156:159], a[0:7], %[idx]\n\t"
      "v_smfmac_f32_16x16x64_bf16 v[168:171], v[156:159], a[8:15], %[idx]\n\t"
      "s_waitcnt lgkmcnt(0)\n\t"
      "v_smfmac_f32_16x16x64_bf16 v[164:167], v[160:163], a[16:23], %[idx]\n\t"
      "v_smfmac_f32_16x16x64_bf16 v[168:171], v[160:163], a[24:31], %[idx]\n\t"
      "s_nop 6\n\t"
      "v_add_f32 v164, v164, v165\n\t"
      "v_add_f32 v166, v166, v167\n\t"
      "v_add_f32 v168, v168, v169\n\t"
      "v_add_f32 v170, v170, v171\n\t"
      "v_cvt_pk_bf16_f32 v172, v164, v168\n\t"
      "v_cvt_pk_bf16_f32 v173, v166, v170\n\t"
      "ds_write_b64 %[wa], v[172:173]\n\t"
      "s_waitcnt lgkmcnt(0)\n\t"
      "s_barrier\n\t"
      "ds_read_b64_tr_b16 v[128:129], %[ra]\n\t"
      "ds_read_b64_tr_b16 v[130:131], %[ra] offset:160\n\t"
      "ds_read_b64_tr_b16 v[132:133], %[ra] offset:1280\n\t"
      "ds_read_b64_tr_b16 v[134:135], %[ra] offset:1440\n\t"
      "s_waitcnt lgkmcnt(2)\n\t"
      "v_smfmac_f32_16x16x64_bf16 v[216:219], v[128:131], v[240:247], %[idx]\n\t"
      "v_smfmac_f32_16x16x64_bf16 v[184:187], v[128:131], a[144:151], %[idx]\n\t"
      "s_waitcnt lgkmcnt(0)\n\t"
      "v_smfmac_f32_16x16x64_bf16 v[216:219], v[132:135], v[248:255], %[idx]\n\t"
      "s_nop 0\n\t"
      "s_nop 0\n\t"
      "v_smfmac_f32_16x16x64_bf16 v[188:191], v[128:131], a[160:167], %[idx]\n\t"
      "s_nop 0\n\t"
      "s_nop 0\n\t"
      "v_smfmac_f32_16x16x64_bf16 v[184:187], v[132:135], a[152:159], %[idx]\n\t"
      "s_nop 0\n\t"
      "s_nop 0\n\t"
      "v_add_f32 v216, v216, v217\n\t"
      "v_add_f32 v218, v218, v219\n\t"
      "v_smfmac_f32_16x16x64_bf16 v[192:195], v[128:131], a[176:183], %[idx]\n\t"
      "v_cvt_pk_bf16_f32 v144, v216, v218\n\t"
      "buffer_store_dword v144, %[yo8], %[srd], %[yb] offen sc1\n\t"
      "v_smfmac_f32_16x16x64_bf16 v[188:191], v[132:135], a[168:175], %[idx]\n\t"
      "v_add_f32 v184, v184, v185\n\t"
      "v_add_f32 v186, v186, v187\n\t"
      "v_smfmac_f32_16x16x64_bf16 v[196:199], v[128:131], a[192:199], %[idx]\n\t"
      "v_cvt_pk_bf16_f32 v136, v184, v186\n\t"
      "v_smfmac_f32_16x16x64_bf16 v[192:195], v[132:135], a[184:191], %[idx]\n\t"
      "s_nop 0\n\t"
      "s_nop 0\n\t"
      "v_smfmac_f32_16x16x64_bf16 v[200:203], v[128:131], a[208:215], %[idx]\n\t"
      "v_add_f32 v188, v188, v189\n\t"
      "v_add_f32 v190, v190, v191\n\t"
      "v_smfmac_f32_16x16x64_bf16 v[196:199], v[132:135], a[200:207], %[idx]\n\t"
      "v_cvt_pk_bf16_f32 v137, v188, v190\n\t"
      "v_smfmac_f32_16x16x64_bf16 v[204:207], v[128:131], a[224:231], %[idx]\n\t"
      "v_add_f32 v192, v192, v193\n\t"
      "v_add_f32 v194, v194, v195\n\t"
      "v_smfmac_f32_16x16x64_bf16 v[200:203], v[132:135], a[216:223], %[idx]\n\t"
      "v_cvt_pk_bf16_f32 v138, v192, v194\n\t"
      "v_smfmac_f32_16x16x64_bf16 v[208:211], v[128:131], a[240:247], %[idx]\n\t"
      "s_nop 0\n\t"
      "v_add_f32 v196, v196, v197\n\t"
      "v_add_f32 v198, v198, v199\n\t"
      "v_smfmac_f32_16x16x64_bf16 v[204:207], v[132:135], a[232:239], %[idx]\n\t"
      "v_cvt_pk_bf16_f32 v139, v196, v198\n\t"
      "v_permlane32_swap_b32 v136, v138\n\t"
      "v_smfmac_f32_16x16x64_bf16 v[212:215], v[128:131], v[224:231], %[idx]\n\t"
      "s_nop 0\n\t"
      "v_permlane32_swap_b32 v137, v139\n\t"
      "s_nop 1\n\t"
      "v_permlane16_swap_b32 v136, v137\n\t"
      "v_smfmac_f32_16x16x64_bf16 v[208:211], v[132:135], a[248:255], %[idx]\n\t"
      "v_permlane16_swap_b32 v138, v139\n\t"
      "buffer_store_dwordx4 v[136:139], %[yo], %[srd], %[yb] offen sc1\n\t"
      "v_add_f32 v200, v200, v201\n\t"
      "v_smfmac_f32_16x16x64_bf16 v[212:215], v[132:135], v[232:239], %[idx]\n\t"
      "v_add_f32 v202, v202, v203\n\t"
      "v_cvt_pk_bf16_f32 v140, v200, v202\n\t"
      "v_add_f32 v204, v204, v205\n\t"
      "v_add_f32 v206, v206, v207\n\t"
      "v_cvt_pk_bf16_f32 v141, v204, v206\n\t"
      "v_add_f32 v208, v208, v209\n\t"
      "v_add_f32 v210, v210, v211\n\t"
      "v_cvt_pk_bf16_f32 v142, v208, v210\n\t"
      "v_add_f32 v212, v212, v213\n\t"
      "v_add_f32 v214, v214, v215\n\t"
      "v_cvt_pk_bf16_f32 v143, v212, v214\n\t"
      "v_permlane32_swap_b32 v140, v142\n\t"
      "s_nop 0\n\t"
      "v_permlane32_swap_b32 v141, v143\n\t"
      "s_nop 1\n\t"
      "v_permlane16_swap_b32 v140, v141\n\t"
      "v_permlane16_swap_b32 v142, v143\n\t"
      "buffer_store_dwordx4 v[140:143], %[yo], %[srd], s88 offen sc1\n\t"
      "s_waitcnt lgkmcnt(0)\n\t"
      "s_nop 1\n\t"
      "s_branch 29f\n\t"
      "20:\n\t"
      "s_mov_b32 s66, 0x20000\n\t"
      "s_mov_b32 %[flag], 1\n\t"
      "s_add_u32 s64, %[mb], %[da]\n\t"
      "s_add_u32 s65, %[mb], %[db]\n\t"
      "22:\n\t"
      "buffer_load_dwordx4 v[128:131], %[o0], %[srd], s64 offen sc1\n\t"
      "buffer_load_dwordx4 v[132:135], %[o0], %[srd], s64 offen offset:1024 sc1\n\t"
      "buffer_load_dwordx4 v[136:139], %[o2], %[srd], s65 offen sc1\n\t"
      "s_waitcnt vmcnt(0)\n\t"
      "v_max_u32 v160, v128, v129\n\t"
      "v_max3_u32 v160, v160, v130, v131\n\t"
      "v_max3_u32 v160, v160, v132, v133\n\t"
      "v_max3_u32 v160, v160, v134, v135\n\t"
      "v_max3_u32 v160, v160, v136, v137\n\t"
      "v_max3_u32 v160, v160, v138, v139\n\t"
      "v_cmp_eq_u32 vcc, -1, v160\n\t"
      "s_cbranch_vccz 23f\n\t"
      "s_sleep 1\n\t"
      "s_sub_u32 s66, s66, 1\n\t"
      "s_cbranch_scc0 22b\n\t"
      "s_endpgm\n\t"
      "23:\n\t"
      "ds_write_b128 %[ma], v[128:131] offset:6144\n\t"
      "ds_write_b128 %[ma], v[132:135] offset:7168\n\t"
      "ds_write_b128 %[ma], v[136:139] offset:8192\n\t"
      "s_nop 1\n\t"
      "s_waitcnt lgkmcnt(0)\n\t"
      "s_branch 21b\n\t"
      "29:\n\t"
      : [flag] "=&s"(flag)
      : [tgt] "s"(US(tgt)), [o2] "v"(l16c), [o0] "v"(gl0), [srd] "s"(srd), [mb] "s"(US(mb)), [ma0] "s"(US(mb + ca)), [mb8] "s"(US(mb + cb)), [da] "s"(US(da)), [db] "s"(US(db)), [yb] "s"(US(yb)), [yn] "s"(US(yn)), [idx] "v"(idx), [ma] "v"(ma), [wa] "v"(wa), [ra] "v"(ra), [yo] "v"(y_off), [yo8] "v"(y_off8), WPIN(W)
      : "v128", "v129", "v130", "v131", "v132", "v133", "v134", "v135", "v136", "v137", "v138", "v139", "v140", "v141", "v142", "v143", "v144", "v145", "v146", "v147", "v148", "v149", "v150", "v151", "v152", "v153", "v154", "v155", "v156", "v157", "v158", "v159", "v160", "v161", "v162", "v163", "v164", "v165", "v166", "v167", "v168", "v169", "v170", "v171", "v172", "v173", "v174", "v175", "v176", "v177", "v178", "v179", "v180", "v181", "v182", "v183", "v184", "v185", "v186", "v187", "v188", "v189", "v190", "v191", "v192", "v193", "v194", "v195", "v196", "v197", "v198", "v199", "v200", "v201", "v202", "v203", "v204", "v205", "v206", "v207", "v208", "v209", "v210", "v211", "v212", "v213", "v214", "v215", "v216", "v217", "v218", "v219", "s64", "s65", "s66", "s67", "s68", "s69", "s70", "s71", "s72", "s73", "s74", "s75", "s76", "s77", "s78", "s79", "s80", "s81", "s82", "s83", "s84", "s85", "s86", "s87", "s88", "s89", "s90", "s91", "s92", "s93", "s94", "s95", "vcc", "scc", "memory");
    }
  } else {
    for (int s = 0; s < NSEG; ++s) {
      const u32 mb = mp_base(s, par), yb = y_base(s, par), yn = y_base(s + 2, par);
      if (w == 0) {
        asm volatile(
      "v_mov_b64 v[164:165], 0\n\t"
      "v_mov_b64 v[166:167], 0\n\t"
      "v_mov_b64 v[168:169], 0\n\t"
      "v_mov_b64 v[170:171], 0\n\t"
      "v_mov_b64 v[184:185], 0\n\t"
      "v_mov_b64 v[186:187], 0\n\t"
      "v_mov_b64 v[188:189], 0\n\t"
      "v_mov_b64 v[190:191], 0\n\t"
      "v_mov_b64 v[192:193], 0\n\t"
      "v_mov_b64 v[194:195], 0\n\t"
      "v_mov_b64 v[196:197], 0\n\t"
      "v_mov_b64 v[198:199], 0\n\t"
      "v_mov_b64 v[200:201], 0\n\t"
      "v_mov_b64 v[202:203], 0\n\t"
      "v_mov_b64 v[204:205], 0\n\t"
      "v_mov_b64 v[206:207], 0\n\t"
      "v_mov_b64 v[208:209], 0\n\t"
      "v_mov_b64 v[210:211], 0\n\t"
      "v_mov_b64 v[212:213], 0\n\t"
      "v_mov_b64 v[214:215], 0\n\t"
      "v_mov_b64 v[216:217], 0\n\t"
      "v_mov_b64 v[218:219], 0\n\t"
      "s_mov_b32 %[flag], 0\n\t"
      "s_add_u32 s88, %[yb], 0x40000\n\t"
      "s_add_u32 s89, %[yn], 0x40000\n\t"
      "v_mov_b64 v[176:177], -1\n\t"
      "v_mov_b64 v[178:179], -1\n\t"
      "s_barrier\n\t"
      "buffer_load_dwordx4 v[128:131], %[o0], %[srd], %[ma0] offen sc1\n\t"
      "buffer_load_dwordx4 v[132:135], %[o0], %[srd], %[ma0] offen offset:1024 sc1\n\t"
      "s_memtime s[92:93]\n\t"
      "s_nop 15\n\t"
      "s_nop 15\n\t"
      "buffer_store_dwordx4 v[176:179], %[yo], %[srd], %[yn] offen sc1\n\t"
      "buffer_store_dwordx4 v[176:179], %[yo], %[srd], s89 offen sc1\n\t"
      "buffer_store_dword v176, %[yo8], %[srd], %[yn] offen sc1\n\t"
      "s_waitcnt vmcnt(4)\n\t"
      "ds_write_b128 %[ma], v[128:131] offset:0\n\t"
      "s_waitcnt vmcnt(3)\n\t"
      "ds_write_b128 %[ma], v[132:135] offset:1024\n\t"
      "v_max_u32 v174, v128, v129\n\t"
      "v_max3_u32 v174, v174, v130, v131\n\t"
      "v_max3_u32 v174, v174, v132, v133\n\t"
      "v_max3_u32 v174, v174, v134, v135\n\t"
      "v_cmp_eq_u32 vcc, -1, v174\n\t"
      "s_cbranch_vccnz 20f\n\t"
      "s_waitcnt lgkmcnt(0)\n\t"
      "21:\n\t"
      "s_barrier\n\t"
      "ds_read_b128 v[136:139], %[ma] offset:2048\n\t"
      "ds_read_b128 v[140:143], %[ma] offset:3072\n\t"
      "v_smfmac_f32_16x16x64_bf16 v[164:167], v[128:131], a[0:7], %[idx]\n\t"
      "ds_read_b128 v[144:147], %[ma] offset:4096\n\t"
      "ds_read_b128 v[148:151], %[ma] offset:5120\n\t"
      "v_smfmac_f32_16x16x64_bf16 v[168:171], v[128:131], a[8:15], %[idx]\n\t"
      "ds_read_b128 v[152:155], %[ma] offset:6144\n\t"
      "ds_read_b128 v[156:159], %[ma] offset:7168\n\t"
      "v_smfmac_f32_16x16x64_bf16 v[164:167], v[132:135], a[16:23], %[idx]\n\t"
      "ds_read_b128 v[160:163], %[ma] offset:8192\n\t"
      "v_smfmac_f32_16x16x64_bf16 v[168:171], v[132:135], a[24:31], %[idx]\n\t"
      "s_waitcnt lgkmcnt(6)\n\t"
      "v_smfmac_f32_16x16x64_bf16 v[164:167], v[136:139], a[32:39], %[idx]\n\t"
      "v_smfmac_f32_16x16x64_bf16 v[168:171], v[136:139], a[40:47], %[idx]\n\t"
      "s_waitcnt lgkmcnt(5)\n\t"
      "v_smfmac_f32_16x16x64_bf16 v[164:167], v[140:143], a[48:55], %[idx]\n\t"
      "v_smfmac_f32_16x16x64_bf16 v[168:171], v[140:143], a[56:63], %[idx]\n\t"
      "s_waitcnt lgkmcnt(4)\n\t"
      "v_smfmac_f32_16x16x64_bf16 v[164:167], v[144:147], a[64:71], %[idx]\n\t"
      "v_smfmac_f32_16x16x64_bf16 v[168:171], v[144:147], a[72:79], %[idx]\n\t"
      "s_waitcnt lgkmcnt(3)\n\t"
      "v_smfmac_f32_16x16x64_bf16 v[164:167], v[148:151], a[80:87], %[idx]\n\t"
      "v_smfmac_f32_16x16x64_bf16 v[168:171], v[148:151], a[88:95], %[idx]\n\t"
      "s_waitcnt lgkmcnt(2)\n\t"
      "v_smfmac_f32_16x16x64_bf16 v[164:167], v[152:155], a[96:103], %[idx]\n\t"
      "v_smfmac_f32_16x16x64_bf16 v[168:171], v[152:155], a[104:111], %[idx]\n\t"
      "s_waitcnt lgkmcnt(1)\n\t"
      "v_smfmac_f32_16x16x64_bf16 v[164:167], v[156:159], a[112:119], %[idx]\n\t"
      "v_smfmac_f32_16x16x64_bf16 v[168:171], v[156:159], a[120:127], %[idx]\n\t"
      "s_waitcnt lgkmcnt(0)\n\t"
      "v_smfmac_f32_16x16x64_bf16 v[164:167], v[160:163], a[128:135], %[idx]\n\t"
      "v_smfmac_f32_16x16x64_bf16 v[168:171], v[160:163], a[136:143], %[idx]\n\t"
      "s_nop 6\n\t"
      "v_add_f32 v164, v164, v165\n\t"
      "v_add_f32 v166, v166, v167\n\t"
      "v_add_f32 v168, v168, v169\n\t"
      "v_add_f32 v170, v170, v171\n\t"
      "v_cvt_pk_bf16_f32 v172, v164, v168\n\t"
      "v_cvt_pk_bf16_f32 v173, v166, v170\n\t"
      "ds_write_b64 %[wa], v[172:173]\n\t"
      "s_waitcnt lgkmcnt(0)\n\t"
      "s_barrier\n\t"
      "ds_read_b64_tr_b16 v[128:129], %[ra]\n\t"
      "ds_read_b64_tr_b16 v[130:131], %[ra] offset:160\n\t"
      "ds_read_b64_tr_b16 v[132:133], %[ra] offset:1280\n\t"
      "ds_read_b64_tr_b16 v[134:135], %[ra] offset:1440\n\t"
      "s_waitcnt lgkmcnt(2)\n\t"
      "v_smfmac_f32_16x16x64_bf16 v[216:219], v[128:131], v[240:247], %[idx]\n\t"
      "v_smfmac_f32_16x16x64_bf16 v[184:187], v[128:131], a[144:151], %[idx]\n\t"
      "s_waitcnt lgkmcnt(0)\n\t"
      "v_smfmac_f32_16x16x64_bf16 v[216:219], v[132:135], v[248:255], %[idx]\n\t"
      "s_nop 0\n\t"
      "s_nop 0\n\t"
      "v_smfmac_f32_16x16x64_bf16 v[188:191], v[128:131], a[160:167], %[idx]\n\t"
      "s_nop 0\n\t"
      "s_nop 0\n\t"
      "v_smfmac_f32_16x16x64_bf16 v[184:187], v[132:135], a[152:159], %[idx]\n\t"
      "s_nop 0\n\t"
      "s_nop 0\n\t"
      "v_add_f32 v216, v216, v217\n\t"
      "v_add_f32 v218, v218, v219\n\t"
      "v_smfmac_f32_16x16x64_bf16 v[192:195], v[128:131], a[176:183], %[idx]\n\t"
      "v_cvt_pk_bf16_f32 v144, v216, v218\n\t"
      "buffer_store_dword v144, %[yo8], %[srd], %[yb] offen sc1\n\t"
      "v_smfmac_f32_16x16x64_bf16 v[188:191], v[132:135], a[168:175], %[idx]\n\t"
      "v_add_f32 v184, v184, v185\n\t"
      "v_add_f32 v186, v186, v187\n\t"
      "v_smfmac_f32_16x16x64_bf16 v[196:199], v[128:131], a[192:199], %[idx]\n\t"
      "v_cvt_pk_bf16_f32 v136, v184, v186\n\t"
      "v_smfmac_f32_16x16x64_bf16 v[192:195], v[132:135], a[184:191], %[idx]\n\t"
      "s_nop 0\n\t"
      "s_nop 0\n\t"
      "v_smfmac_f32_16x16x64_bf16 v[200:203], v[128:131], a[208:215], %[idx]\n\t"
      "v_add_f32 v188, v188, v189\n\t"
      "v_add_f32 v190, v190, v191\n\t"
      "v_smfmac_f32_16x16x64_bf16 v[196:199], v[132:135], a[200:207], %[idx]\n\t"
      "v_cvt_pk_bf16_f32 v137, v188, v190\n\t"
      "v_smfmac_f32_16x16x64_bf16 v[204:207], v[128:131], a[224:231], %[idx]\n\t"
      "v_add_f32 v192, v192, v193\n\t"
      "v_add_f32 v194, v194, v195\n\t"
      "v_smfmac_f32_16x16x64_bf16 v[200:203], v[132:135], a[216:223], %[idx]\n\t"
      "v_cvt_pk_bf16_f32 v138, v192, v194\n\t"
      "v_smfmac_f32_16x16x64_bf16 v[208:211], v[128:131], a[240:247], %[idx]\n\t"
      "s_nop 0\n\t"
      "v_add_f32 v196, v196, v197\n\t"
      "v_add_f32 v198, v198, v199\n\t"
      "v_smfmac_f32_16x16x64_bf16 v[204:207], v[132:135], a[232:239], %[idx]\n\t"
      "v_cvt_pk_bf16_f32 v139, v196, v198\n\t"
      "v_permlane32_swap_b32 v136, v138\n\t"
      "v_smfmac_f32_16x16x64_bf16 v[212:215], v[128:131], v[224:231], %[idx]\n\t"
      "s_nop 0\n\t"
      "v_permlane32_swap_b32 v137, v139\n\t"
      "s_nop 1\n\t"
      "v_permlane16_swap_b32 v136, v137\n\t"
      "v_smfmac_f32_16x16x64_bf16 v[208:211], v[132:135], a[248:255], %[idx]\n\t"
      "v_permlane16_swap_b32 v138, v139\n\t"
      "buffer_store_dwordx4 v[136:139], %[yo], %[srd], %[yb] offen sc1\n\t"
      "v_add_f32 v200, v200, v201\n\t"
      "v_smfmac_f32_16x16x64_bf16 v[212:215], v[132:135], v[232:239], %[idx]\n\t"
      "v_add_f32 v202, v202, v203\n\t"
      "v_cvt_pk_bf16_f32 v140, v200, v202\n\t"
      "v_add_f32 v204, v204, v205\n\t"
      "v_add_f32 v206, v206, v207\n\t"
      "v_cvt_pk_bf16_f32 v141, v204, v206\n\t"
      "v_add_f32 v208, v208, v209\n\t"
      "v_add_f32 v210, v210, v211\n\t"
      "v_cvt_pk_bf16_f32 v142, v208, v210\n\t"
      "v_add_f32 v212, v212, v213\n\t"
      "v_add_f32 v214, v214, v215\n\t"
      "v_cvt_pk_bf16_f32 v143, v212, v214\n\t"
      "v_permlane32_swap_b32 v140, v142\n\t"
      "s_nop 0\n\t"
      "v_permlane32_swap_b32 v141, v143\n\t"
      "s_nop 1\n\t"
      "v_permlane16_swap_b32 v140, v141\n\t"
      "v_permlane16_swap_b32 v142, v143\n\t"
      "buffer_store_dwordx4 v[140:143], %[yo], %[srd], s88 offen sc1\n\t"
      "s_waitcnt vmcnt(0)\n\t"
      "s_memtime s[94:95]\n\t"
      "s_memrealtime s[64:65]\n\t"
      "s_nop 1\n\t"
      "s_memrealtime s[66:67]\n\t"
      "s_nop 1\n\t"
      "s_memrealtime s[68:69]\n\t"
      "s_nop 1\n\t"
      "s_memrealtime s[70:71]\n\t"
      "s_nop 1\n\t"
      "s_memrealtime s[72:73]\n\t"
      "s_nop 1\n\t"
      "s_memrealtime s[74:75]\n\t"
      "s_nop 1\n\t"
      "s_memrealtime s[76:77]\n\t"
      "s_nop 1\n\t"
      "s_memrealtime s[78:79]\n\t"
      "s_waitcnt lgkmcnt(0)\n\t"
      "s_add_u32 s64, s64, s66\n\t"
      "s_add_u32 s68, s68, s70\n\t"
      "s_add_u32 s72, s72, s74\n\t"
      "s_add_u32 s76, s76, s78\n\t"
      "s_add_u32 s64, s64, s68\n\t"
      "s_add_u32 s72, s72, s76\n\t"
      "s_add_u32 %[te], s64, s72\n\t"
      "s_sub_u32 %[du], s94, s92\n\t"
      "s_waitcnt lgkmcnt(0)\n\t"
      "s_nop 1\n\t"
      "s_branch 29f\n\t"
      "20:\n\t"
      "s_mov_b32 s66, 0x20000\n\t"
      "s_mov_b32 %[flag], 1\n\t"
      "s_add_u32 s64, %[mb], %[da]\n\t"
      "s_add_u32 s65, %[mb], %[db]\n\t"
      "22:\n\t"
      "buffer_load_dwordx4 v[128:131], %[o0], %[srd], s64 offen sc1\n\t"
      "buffer_load_dwordx4 v[132:135], %[o0], %[srd], s64 offen offset:1024 sc1\n\t"
      "s_waitcnt vmcnt(0)\n\t"
      "v_max_u32 v160, v128, v129\n\t"
      "v_max3_u32 v160, v160, v130, v131\n\t"
      "v_max3_u32 v160, v160, v132, v133\n\t"
      "v_max3_u32 v160, v160, v134, v135\n\t"
      "v_cmp_eq_u32 vcc, -1, v160\n\t"
      "s_cbranch_vccz 23f\n\t"
      "s_sleep 1\n\t"
      "s_sub_u32 s66, s66, 1\n\t"
      "s_cbranch_scc0 22b\n\t"
      "s_endpgm\n\t"
      "23:\n\t"
      "ds_write_b128 %[ma], v[128:131] offset:0\n\t"
      "ds_write_b128 %[ma], v[132:135] offset:1024\n\t"
      "s_nop 1\n\t"
      "s_waitcnt lgkmcnt(0)\n\t"
      "s_branch 21b\n\t"
      "29:\n\t"
      : [flag] "=&s"(flag) , [te] "=&s"(te), [du] "=&s"(du)
      : [o0] "v"(gl0), [srd] "s"(srd), [mb] "s"(US(mb)), [ma0] "s"(US(mb + ca)), [mb8] "s"(US(mb + cb)), [da] "s"(US(da)), [db] "s"(US(db)), [yb] "s"(US(yb)), [yn] "s"(US(yn)), [idx] "v"(idx), [ma] "v"(ma), [wa] "v"(wa), [ra] "v"(ra), [yo] "v"(y_off), [yo8] "v"(y_off8), WPIN(W)
      : "v128", "v129", "v130", "v131", "v132", "v133", "v134", "v135", "v136", "v137", "v138", "v139", "v140", "v141", "v142", "v143", "v144", "v145", "v146", "v147", "v148", "v149", "v150", "v151", "v152", "v153", "v154", "v155", "v156", "v157", "v158", "v159", "v160", "v161", "v162", "v163", "v164", "v165", "v166", "v167", "v168", "v169", "v170", "v171", "v172", "v173", "v174", "v175", "v176", "v177", "v178", "v179", "v180", "v181", "v182", "v183", "v184", "v185", "v186", "v187", "v188", "v189", "v190", "v191", "v192", "v193", "v194", "v195", "v196", "v197", "v198", "v199", "v200", "v201", "v202", "v203", "v204", "v205", "v206", "v207", "v208", "v209", "v210", "v211", "v212", "v213", "v214", "v215", "v216", "v217", "v218", "v219", "s64", "s65", "s66", "s67", "s68", "s69", "s70", "s71", "s72", "s73", "s74", "s75", "s76", "s77", "s78", "s79", "s80", "s81", "s82", "s83", "s84", "s85", "s86", "s87", "s88", "s89", "s90", "s91", "s92", "s93", "s94", "s95", "vcc", "scc", "memory");
        put_stamp(rs, mb, 32 + h, te, du, flag != 0, lane);
      } else if (w == 1) asm volatile(
      "v_mov_b64 v[164:165], 0\n\t"
      "v_mov_b64 v[166:167], 0\n\t"
      "v_mov_b64 v[168:169], 0\n\t"
      "v_mov_b64 v[170:171], 0\n\t"
      "v_mov_b64 v[184:185], 0\n\t"
      "v_mov_b64 v[186:187], 0\n\t"
      "v_mov_b64 v[188:189], 0\n\t"
      "v_mov_b64 v[190:191], 0\n\t"
      "v_mov_b64 v[192:193], 0\n\t"
      "v_mov_b64 v[194:195], 0\n\t"
      "v_mov_b64 v[196:197], 0\n\t"
      "v_mov_b64 v[198:199], 0\n\t"
      "v_mov_b64 v[200:201], 0\n\t"
      "v_mov_b64 v[202:203], 0\n\t"
      "v_mov_b64 v[204:205], 0\n\t"
      "v_mov_b64 v[206:207], 0\n\t"
      "v_mov_b64 v[208:209], 0\n\t"
      "v_mov_b64 v[210:211], 0\n\t"
      "v_mov_b64 v[212:213], 0\n\t"
      "v_mov_b64 v[214:215], 0\n\t"
      "v_mov_b64 v[216:217], 0\n\t"
      "v_mov_b64 v[218:219], 0\n\t"
      "s_mov_b32 %[flag], 0\n\t"
      "s_add_u32 s88, %[yb], 0x40000\n\t"
      "s_add_u32 s89, %[yn], 0x40000\n\t"
      "v_mov_b64 v[176:177], -1\n\t"
      "v_mov_b64 v[178:179], -1\n\t"
      "s_barrier\n\t"
      "buffer_load_dwordx4 v[128:131], %[o0], %[srd], %[ma0] offen sc1\n\t"
      "buffer_load_dwordx4 v[132:135], %[o0], %[srd], %[ma0] offen offset:1024 sc1\n\t"
      "s_nop 15\n\t"
      "s_nop 15\n\t"
      "buffer_store_dwordx4 v[176:179], %[yo], %[srd], %[yn] offen sc1\n\t"
      "buffer_store_dwordx4 v[176:179], %[yo], %[srd], s89 offen sc1\n\t"
      "buffer_store_dword v176, %[yo8], %[srd], %[yn] offen sc1\n\t"
      "s_waitcnt vmcnt(4)\n\t"
      "ds_write_b128 %[ma], v[128:131] offset:2048\n\t"
      "s_waitcnt vmcnt(3)\n\t"
      "ds_write_b128 %[ma], v[132:135] offset:3072\n\t"
      "v_max_u32 v174, v128, v129\n\t"
      "v_max3_u32 v174, v174, v130, v131\n\t"
      "v_max3_u32 v174, v174, v132, v133\n\t"
      "v_max3_u32 v174, v174, v134, v135\n\t"
      "v_cmp_eq_u32 vcc, -1, v174\n\t"
      "s_cbranch_vccnz 20f\n\t"
      "s_waitcnt lgkmcnt(0)\n\t"
      "21:\n\t"
      "s_barrier\n\t"
      "ds_read_b128 v[136:139], %[ma] offset:0\n\t"
      "ds_read_b128 v[140:143], %[ma] offset:1024\n\t"
      "v_smfmac_f32_16x16x64_bf16 v[164:167], v[128:131], a[32:39], %[idx]\n\t"
      "ds_read_b128 v[144:147], %[ma] offset:6144\n\t"
      "ds_read_b128 v[148:151], %[ma] offset:7168\n\t"
      "v_smfmac_f32_16x16x64_bf16 v[168:171], v[128:131], a[40:47], %[idx]\n\t"
      "ds_read_b128 v[152:155], %[ma] offset:4096\n\t"
      "ds_read_b128 v[156:159], %[ma] offset:5120\n\t"
      "v_smfmac_f32_16x16x64_bf16 v[164:167], v[132:135], a[48:55], %[idx]\n\t"
      "ds_read_b128 v[160:163], %[ma] offset:8192\n\t"
      "v_smfmac_f32_16x16x64_bf16 v[168:171], v[132:135], a[56:63], %[idx]\n\t"
      "s_waitcnt lgkmcnt(6)\n\t"
      "v_smfmac_f32_16x16x64_bf16 v[164:167], v[136:139], a[0:7], %[idx]\n\t"
      "v_smfmac_f32_16x16x64_bf16 v[168:171], v[136:139], a[8:15], %[idx]\n\t"
      "s_waitcnt lgkmcnt(5)\n\t"
      "v_smfmac_f32_16x16x64_bf16 v[164:167], v[140:143], a[16:23], %[idx]\n\t"
      "v_smfmac_f32_16x16x64_bf16 v[168:171], v[140:143], a[24:31], %[idx]\n\t"
      "s_waitcnt lgkmcnt(4)\n\t"
      "v_smfmac_f32_16x16x64_bf16 v[164:167], v[144:147], a[96:103], %[idx]\n\t"
      "v_smfmac_f32_16x16x64_bf16 v[168:171], v[144:147], a[104:111], %[idx]\n\t"
      "s_waitcnt lgkmcnt(3)\n\t"
      "v_smfmac_f32_16x16x64_bf16 v[164:167], v[148:151], a[112:119], %[idx]\n\t"
      "v_smfmac_f32_16x16x64_bf16 v[168:171], v[148:151], a[120:127], %[idx]\n\t"
      "s_waitcnt lgkmcnt(2)\n\t"
      "v_smfmac_f32_16x16x64_bf16 v[164:167], v[152:155], a[64:71], %[idx]\n\t"
      "v_smfmac_f32_16x16x64_bf16 v[168:171], v[152:155], a[72:79], %[idx]\n\t"
      "s_waitcnt lgkmcnt(1)\n\t"
      "v_smfmac_f32_16x16x64_bf16 v[164:167], v[156:159], a[80:87], %[idx]\n\t"
      "v_smfmac_f32_16x16x64_bf16 v[168:171], v[156:159], a[88:95], %[idx]\n\t"
      "s_waitcnt lgkmcnt(0)\n\t"
      "v_smfmac_f32_16x16x64_bf16 v[164:167], v[160:163], a[128:135], %[idx]\n\t"
      "v_smfmac_f32_16x16x64_bf16 v[168:171], v[160:163], a[136:143], %[idx]\n\t"
      "s_nop 6\n\t"
      "v_add_f32 v164, v164, v165\n\t"
      "v_add_f32 v166, v166, v167\n\t"
      "v_add_f32 v168, v168, v169\n\t"
      "v_add_f32 v170, v170, v171\n\t"
      "v_cvt_pk_bf16_f32 v172, v164, v168\n\t"
      "v_cvt_pk_bf16_f32 v173, v166, v170\n\t"
      "ds_write_b64 %[wa], v[172:173]\n\t"
      "s_waitcnt lgkmcnt(0)\n\t"
      "s_barrier\n\t"
      "ds_read_b64_tr_b16 v[128:129], %[ra]\n\t"
      "ds_read_b64_tr_b16 v[130:131], %[ra] offset:160\n\t"
      "ds_read_b64_tr_b16 v[132:133], %[ra] offset:1280\n\t"
      "ds_read_b64_tr_b16 v[134:135], %[ra] offset:1440\n\t"
      "s_waitcnt lgkmcnt(2)\n\t"
      "v_smfmac_f32_16x16x64_bf16 v[216:219], v[128:131], v[240:247], %[idx]\n\t"
      "v_smfmac_f32_16x16x64_bf16 v[184:187], v[128:131], a[144:151], %[idx]\n\t"
      "s_waitcnt lgkmcnt(0)\n\t"
      "v_smfmac_f32_16x16x64_bf16 v[216:219], v[132:135], v[248:255], %[idx]\n\t"
      "s_nop 0\n\t"
      "s_nop 0\n\t"
      "v_smfmac_f32_16x16x64_bf16 v[188:191], v[128:131], a[160:167], %[idx]\n\t"
      "s_nop 0\n\t"
      "s_nop 0\n\t"
      "v_smfmac_f32_16x16x64_bf16 v[184:187], v[132:135], a[152:159], %[idx]\n\t"
      "s_nop 0\n\t"
      "s_nop 0\n\t"
      "v_add_f32 v216, v216, v217\n\t"
      "v_add_f32 v218, v218, v219\n\t"
      "v_smfmac_f32_16x16x64_bf16 v[192:195], v[128:131], a[176:183], %[idx]\n\t"
      "v_cvt_pk_bf16_f32 v144, v216, v218\n\t"
      "buffer_store_dword v144, %[yo8], %[srd], %[yb] offen sc1\n\t"
      "v_smfmac_f32_16x16x64_bf16 v[188:191], v[132:135], a[168:175], %[idx]\n\t"
      "v_add_f32 v184, v184, v185\n\t"
      "v_add_f32 v186, v186, v187\n\t"
      "v_smfmac_f32_16x16x64_bf16 v[196:199], v[128:131], a[192:199], %[idx]\n\t"
      "v_cvt_pk_bf16_f32 v136, v184, v186\n\t"
      "v_smfmac_f32_16x16x64_bf16 v[192:195], v[132:135], a[184:191], %[idx]\n\t"
      "s_nop 0\n\t"
      "s_nop 0\n\t"
      "v_smfmac_f32_16x16x64_bf16 v[200:203], v[128:131], a[208:215], %[idx]\n\t"
      "v_add_f32 v188, v188, v189\n\t"
      "v_add_f32 v190, v190, v191\n\t"
      "v_smfmac_f32_16x16x64_bf16 v[196:199], v[132:135], a[200:207], %[idx]\n\t"
      "v_cvt_pk_bf16_f32 v137, v188, v190\n\t"
      "v_smfmac_f32_16x16x64_bf16 v[204:207], v[128:131], a[224:231], %[idx]\n\t"
      "v_add_f32 v192, v192, v193\n\t"
      "v_add_f32 v194, v194, v195\n\t"
      "v_smfmac_f32_16x16x64_bf16 v[200:203], v[132:135], a[216:223], %[idx]\n\t"
      "v_cvt_pk_bf16_f32 v138, v192, v194\n\t"
      "v_smfmac_f32_16x16x64_bf16 v[208:211], v[128:131], a[240:247], %[idx]\n\t"
      "s_nop 0\n\t"
      "v_add_f32 v196, v196, v197\n\t"
      "v_add_f32 v198, v198, v199\n\t"
      "v_smfmac_f32_16x16x64_bf16 v[204:207], v[132:135], a[232:239], %[idx]\n\t"
      "v_cvt_pk_bf16_f32 v139, v196, v198\n\t"
      "v_permlane32_swap_b32 v136, v138\n\t"
      "v_smfmac_f32_16x16x64_bf16 v[212:215], v[128:131], v[224:231], %[idx]\n\t"
      "s_nop 0\n\t"
      "v_permlane32_swap_b32 v137, v139\n\t"
      "s_nop 1\n\t"
      "v_permlane16_swap_b32 v136, v137\n\t"
      "v_smfmac_f32_16x16x64_bf16 v[208:211], v[132:135], a[248:255], %[idx]\n\t"
      "v_permlane16_swap_b32 v138, v139\n\t"
      "buffer_store_dwordx4 v[136:139], %[yo], %[srd], %[yb] offen sc1\n\t"
      "v_add_f32 v200, v200, v201\n\t"
      "v_smfmac_f32_16x16x64_bf16 v[212:215], v[132:135], v[232:239], %[idx]\n\t"
      "v_add_f32 v202, v202, v203\n\t"
      "v_cvt_pk_bf16_f32 v140, v200, v202\n\t"
      "v_add_f32 v204, v204, v205\n\t"
      "v_add_f32 v206, v206, v207\n\t"
      "v_cvt_pk_bf16_f32 v141, v204, v206\n\t"
      "v_add_f32 v208, v208, v209\n\t"
      "v_add_f32 v210, v210, v211\n\t"
      "v_cvt_pk_bf16_f32 v142, v208, v210\n\t"
      "v_add_f32 v212, v212, v213\n\t"
      "v_add_f32 v214, v214, v215\n\t"
      "v_cvt_pk_bf16_f32 v143, v212, v214\n\t"
      "v_permlane32_swap_b32 v140, v142\n\t"
      "s_nop 0\n\t"
      "v_permlane32_swap_b32 v141, v143\n\t"
      "s_nop 1\n\t"
      "v_permlane16_swap_b32 v140, v141\n\t"
      "v_permlane16_swap_b32 v142, v143\n\t"
      "buffer_store_dwordx4 v[140:143], %[yo], %[srd], s88 offen sc1\n\t"
      "s_waitcnt lgkmcnt(0)\n\t"
      "s_nop 1\n\t"
      "s_branch 29f\n\t"
      "20:\n\t"
      "s_mov_b32 s66, 0x20000\n\t"
      "s_mov_b32 %[flag], 1\n\t"
      "s_add_u32 s64, %[mb], %[da]\n\t"
      "s_add_u32 s65, %[mb], %[db]\n\t"
      "22:\n\t"
      "buffer_load_dwordx4 v[128:131], %[o0], %[srd], s64 offen sc1\n\t"
      "buffer_load_dwordx4 v[132:135], %[o0], %[srd], s64 offen offset:1024 sc1\n\t"
      "s_waitcnt vmcnt(0)\n\t"
      "v_max_u32 v160, v128, v129\n\t"
      "v_max3_u32 v160, v160, v130, v131\n\t"
      "v_max3_u32 v160, v160, v132, v133\n\t"
      "v_max3_u32 v160, v160, v134, v135\n\t"
      "v_cmp_eq_u32 vcc, -1, v160\n\t"
      "s_cbranch_vccz 23f\n\t"
      "s_sleep 1\n\t"
      "s_sub_u32 s66, s66, 1\n\t"
      "s_cbranch_scc0 22b\n\t"
      "s_endpgm\n\t"
      "23:\n\t"
      "ds_write_b128 %[ma], v[128:131] offset:2048\n\t"
      "ds_write_b128 %[ma], v[132:135] offset:3072\n\t"
      "s_nop 1\n\t"
      "s_waitcnt lgkmcnt(0)\n\t"
      "s_branch 21b\n\t"
      "29:\n\t"
      : [flag] "=&s"(flag)
      : [o0] "v"(gl0), [srd] "s"(srd), [mb] "s"(US(mb)), [ma0] "s"(US(mb + ca)), [mb8] "s"(US(mb + cb)), [da] "s"(US(da)), [db] "s"(US(db)), [yb] "s"(US(yb)), [yn] "s"(US(yn)), [idx] "v"(idx), [ma] "v"(ma), [wa] "v"(wa), [ra] "v"(ra), [yo] "v"(y_off), [yo8] "v"(y_off8), WPIN(W)
      : "v128", "v129", "v130", "v131", "v132", "v133", "v134", "v135", "v136", "v137", "v138", "v139", "v140", "v141", "v142", "v143", "v144", "v145", "v146", "v147", "v148", "v149", "v150", "v151", "v152", "v153", "v154", "v155", "v156", "v157", "v158", "v159", "v160", "v161", "v162", "v163", "v164", "v165", "v166", "v167", "v168", "v169", "v170", "v171", "v172", "v173", "v174", "v175", "v176", "v177", "v178", "v179", "v180", "v181", "v182", "v183", "v184", "v185", "v186", "v187", "v188", "v189", "v190", "v191", "v192", "v193", "v194", "v195", "v196", "v197", "v198", "v199", "v200", "v201", "v202", "v203", "v204", "v205", "v206", "v207", "v208", "v209", "v210", "v211", "v212", "v213", "v214", "v215", "v216", "v217", "v218", "v219", "s64", "s65", "s66", "s67", "s68", "s69", "s70", "s71", "s72", "s73", "s74", "s75", "s76", "s77", "s78", "s79", "s80", "s81", "s82", "s83", "s84", "s85", "s86", "s87", "s88", "s89", "s90", "s91", "s92", "s93", "s94", "s95", "vcc", "scc", "memory");
      else asm volatile(
      "v_mov_b64 v[164:165], 0\n\t"
      "v_mov_b64 v[166:167], 0\n\t"
      "v_mov_b64 v[168:169], 0\n\t"
      "v_mov_b64 v[170:171], 0\n\t"
      "v_mov_b64 v[184:185], 0\n\t"
      "v_mov_b64 v[186:187], 0\n\t"
      "v_mov_b64 v[188:189], 0\n\t"
      "v_mov_b64 v[190:191], 0\n\t"
      "v_mov_b64 v[192:193], 0\n\t"
      "v_mov_b64 v[194:195], 0\n\t"
      "v_mov_b64 v[196:197], 0\n\t"
      "v_mov_b64 v[198:199], 0\n\t"
      "v_mov_b64 v[200:201], 0\n\t"
      "v_mov_b64 v[202:203], 0\n\t"
      "v_mov_b64 v[204:205], 0\n\t"
      "v_mov_b64 v[206:207], 0\n\t"
      "v_mov_b64 v[208:209], 0\n\t"
      "v_mov_b64 v[210:211], 0\n\t"
      "v_mov_b64 v[212:213], 0\n\t"
      "v_mov_b64 v[214:215], 0\n\t"
      "v_mov_b64 v[216:217], 0\n\t"
      "v_mov_b64 v[218:219], 0\n\t"
      "s_mov_b32 %[flag], 0\n\t"
      "s_add_u32 s88, %[yb], 0x40000\n\t"
      "s_add_u32 s89, %[yn], 0x40000\n\t"
      "v_mov_b64 v[176:177], -1\n\t"
      "v_mov_b64 v[178:179], -1\n\t"
      "s_barrier\n\t"
      "buffer_load_dwordx4 v[128:131], %[o0], %[srd], %[ma0] offen sc1\n\t"
      "buffer_load_dwordx4 v[132:135], %[o0], %[srd], %[ma0] offen offset:1024 sc1\n\t"
      "s_nop 15\n\t"
      "s_nop 15\n\t"
      "buffer_store_dwordx4 v[176:179], %[yo], %[srd], %[yn] offen sc1\n\t"
      "buffer_store_dwordx4 v[176:179], %[yo], %[srd], s89 offen sc1\n\t"
      "buffer_store_dword v176, %[yo8], %[srd], %[yn] offen sc1\n\t"
      "s_waitcnt vmcnt(4)\n\t"
      "ds_write_b128 %[ma], v[128:131] offset:4096\n\t"
      "s_waitcnt vmcnt(3)\n\t"
      "ds_write_b128 %[ma], v[132:135] offset:5120\n\t"
      "v_max_u32 v174, v128, v129\n\t"
      "v_max3_u32 v174, v174, v130, v131\n\t"
      "v_max3_u32 v174, v174, v132, v133\n\t"
      "v_max3_u32 v174, v174, v134, v135\n\t"
      "v_cmp_eq_u32 vcc, -1, v174\n\t"
      "s_cbranch_vccnz 20f\n\t"
      "s_waitcnt lgkmcnt(0)\n\t"
      "21:\n\t"
      "s_barrier\n\t"
      "ds_read_b128 v[136:139], %[ma] offset:6144\n\t"
      "ds_read_b128 v[140:143], %[ma] offset:7168\n\t"
      "v_smfmac_f32_16x16x64_bf16 v[164:167], v[128:131], a[64:71], %[idx]\n\t"
      "ds_read_b128 v[144:147], %[ma] offset:0\n\t"
      "ds_read_b128 v[148:151], %[ma] offset:1024\n\t"
      "v_smfmac_f32_16x16x64_bf16 v[168:171], v[128:131], a[72:79], %[idx]\n\t"
      "ds_read_b128 v[152:155], %[ma] offset:2048\n\t"
      "ds_read_b128 v[156:159], %[ma] offset:3072\n\t"
      "v_smfmac_f32_16x16x64_bf16 v[164:167], v[132:135], a[80:87], %[idx]\n\t"
      "ds_read_b128 v[160:163], %[ma] offset:8192\n\t"
      "v_smfmac_f32_16x16x64_bf16 v[168:171], v[132:135], a[88:95], %[idx]\n\t"
      "s_waitcnt lgkmcnt(6)\n\t"
      "v_smfmac_f32_16x16x64_bf16 v[164:167], v[136:139], a[96:103], %[idx]\n\t"
      "v_smfmac_f32_16x16x64_bf16 v[168:171], v[136:139], a[104:111], %[idx]\n\t"
      "s_waitcnt lgkmcnt(5)\n\t"
      "v_smfmac_f32_16x16x64_bf16 v[164:167], v[140:143], a[112:119], %[idx]\n\t"
      "v_smfmac_f32_16x16x64_bf16 v[168:171], v[140:143], a[120:127], %[idx]\n\t"
      "s_waitcnt lgkmcnt(4)\n\t"
      "v_smfmac_f32_16x16x64_bf16 v[164:167], v[144:147], a[0:7], %[idx]\n\t"
      "v_smfmac_f32_16x16x64_bf16 v[168:171], v[144:147], a[8:15], %[idx]\n\t"
      "s_waitcnt lgkmcnt(3)\n\t"
      "v_smfmac_f32_16x16x64_bf16 v[164:167], v[148:151], a[16:23], %[idx]\n\t"
      "v_smfmac_f32_16x16x64_bf16 v[168:171], v[148:151], a[24:31], %[idx]\n\t"
      "s_waitcnt lgkmcnt(2)\n\t"
      "v_smfmac_f32_16x16x64_bf16 v[164:167], v[152:155], a[32:39], %[idx]\n\t"
      "v_smfmac_f32_16x16x64_bf16 v[168:171], v[152:155], a[40:47], %[idx]\n\t"
      "s_waitcnt lgkmcnt(1)\n\t"
      "v_smfmac_f32_16x16x64_bf16 v[164:167], v[156:159], a[48:55], %[idx]\n\t"
      "v_smfmac_f32_16x16x64_bf16 v[168:171], v[156:159], a[56:63], %[idx]\n\t"
      "s_waitcnt lgkmcnt(0)\n\t"
      "v_smfmac_f32_16x16x64_bf16 v[164:167], v[160:163], a[128:135], %[idx]\n\t"
      "v_smfmac_f32_16x16x64_bf16 v[168:171], v[160:163], a[136:143], %[idx]\n\t"
      "s_nop 6\n\t"
      "v_add_f32 v164, v164, v165\n\t"
      "v_add_f32 v166, v166, v167\n\t"
      "v_add_f32 v168, v168, v169\n\t"
      "v_add_f32 v170, v170, v171\n\t"
      "v_cvt_pk_bf16_f32 v172, v164, v168\n\t"
      "v_cvt_pk_bf16_f32 v173, v166, v170\n\t"
      "ds_write_b64 %[wa], v[172:173]\n\t"
      "s_waitcnt lgkmcnt(0)\n\t"
      "s_barrier\n\t"
      "ds_read_b64_tr_b16 v[128:129], %[ra]\n\t"
      "ds_read_b64_tr_b16 v[130:131], %[ra] offset:160\n\t"
      "ds_read_b64_tr_b16 v[132:133], %[ra] offset:1280\n\t"
      "ds_read_b64_tr_b16 v[134:135], %[ra] offset:1440\n\t"
      "s_waitcnt lgkmcnt(2)\n\t"
      "v_smfmac_f32_16x16x64_bf16 v[216:219], v[128:131], v[240:247], %[idx]\n\t"
      "v_smfmac_f32_16x16x64_bf16 v[184:187], v[128:131], a[144:151], %[idx]\n\t"
      "s_waitcnt lgkmcnt(0)\n\t"
      "v_smfmac_f32_16x16x64_bf16 v[216:219], v[132:135], v[248:255], %[idx]\n\t"
      "s_nop 0\n\t"
      "s_nop 0\n\t"
      "v_smfmac_f32_16x16x64_bf16 v[188:191], v[128:131], a[160:167], %[idx]\n\t"
      "s_nop 0\n\t"
      "s_nop 0\n\t"
      "v_smfmac_f32_16x16x64_bf16 v[184:187], v[132:135], a[152:159], %[idx]\n\t"
      "s_nop 0\n\t"
      "s_nop 0\n\t"
      "v_add_f32 v216, v216, v217\n\t"
      "v_add_f32 v218, v218, v219\n\t"
      "v_smfmac_f32_16x16x64_bf16 v[192:195], v[128:131], a[176:183], %[idx]\n\t"
      "v_cvt_pk_bf16_f32 v144, v216, v218\n\t"
      "buffer_store_dword v144, %[yo8], %[srd], %[yb] offen sc1\n\t"
      "v_smfmac_f32_16x16x64_bf16 v[188:191], v[132:135], a[168:175], %[idx]\n\t"
      "v_add_f32 v184, v184, v185\n\t"
      "v_add_f32 v186, v186, v187\n\t"
      "v_smfmac_f32_16x16x64_bf16 v[196:199], v[128:131], a[192:199], %[idx]\n\t"
      "v_cvt_pk_bf16_f32 v136, v184, v186\n\t"
      "v_smfmac_f32_16x16x64_bf16 v[192:195], v[132:135], a[184:191], %[idx]\n\t"
      "s_nop 0\n\t"
      "s_nop 0\n\t"
      "v_smfmac_f32_16x16x64_bf16 v[200:203], v[128:131], a[208:215], %[idx]\n\t"
      "v_add_f32 v188, v188, v189\n\t"
      "v_add_f32 v190, v190, v191\n\t"
      "v_smfmac_f32_16x16x64_bf16 v[196:199], v[132:135], a[200:207], %[idx]\n\t"
      "v_cvt_pk_bf16_f32 v137, v188, v190\n\t"
      "v_smfmac_f32_16x16x64_bf16 v[204:207], v[128:131], a[224:231], %[idx]\n\t"
      "v_add_f32 v192, v192, v193\n\t"
      "v_add_f32 v194, v194, v195\n\t"
      "v_smfmac_f32_16x16x64_bf16 v[200:203], v[132:135], a[216:223], %[idx]\n\t"
      "v_cvt_pk_bf16_f32 v138, v192, v194\n\t"
      "v_smfmac_f32_16x16x64_bf16 v[208:211], v[128:131], a[240:247], %[idx]\n\t"
      "s_nop 0\n\t"
      "v_add_f32 v196, v196, v197\n\t"
      "v_add_f32 v198, v198, v199\n\t"
      "v_smfmac_f32_16x16x64_bf16 v[204:207], v[132:135], a[232:239], %[idx]\n\t"
      "v_cvt_pk_bf16_f32 v139, v196, v198\n\t"
      "v_permlane32_swap_b32 v136, v138\n\t"
      "v_smfmac_f32_16x16x64_bf16 v[212:215], v[128:131], v[224:231], %[idx]\n\t"
      "s_nop 0\n\t"
      "v_permlane32_swap_b32 v137, v139\n\t"
      "s_nop 1\n\t"
      "v_permlane16_swap_b32 v136, v137\n\t"
      "v_smfmac_f32_16x16x64_bf16 v[208:211], v[132:135], a[248:255], %[idx]\n\t"
      "v_permlane16_swap_b32 v138, v139\n\t"
      "buffer_store_dwordx4 v[136:139], %[yo], %[srd], %[yb] offen sc1\n\t"
      "v_add_f32 v200, v200, v201\n\t"
      "v_smfmac_f32_16x16x64_bf16 v[212:215], v[132:135], v[232:239], %[idx]\n\t"
      "v_add_f32 v202, v202, v203\n\t"
      "v_cvt_pk_bf16_f32 v140, v200, v202\n\t"
      "v_add_f32 v204, v204, v205\n\t"
      "v_add_f32 v206, v206, v207\n\t"
      "v_cvt_pk_bf16_f32 v141, v204, v206\n\t"
      "v_add_f32 v208, v208, v209\n\t"
      "v_add_f32 v210, v210, v211\n\t"
      "v_cvt_pk_bf16_f32 v142, v208, v210\n\t"
      "v_add_f32 v212, v212, v213\n\t"
      "v_add_f32 v214, v214, v215\n\t"
      "v_cvt_pk_bf16_f32 v143, v212, v214\n\t"
      "v_permlane32_swap_b32 v140, v142\n\t"
      "s_nop 0\n\t"
      "v_permlane32_swap_b32 v141, v143\n\t"
      "s_nop 1\n\t"
      "v_permlane16_swap_b32 v140, v141\n\t"
      "v_permlane16_swap_b32 v142, v143\n\t"
      "buffer_store_dwordx4 v[140:143], %[yo], %[srd], s88 offen sc1\n\t"
      "s_waitcnt lgkmcnt(0)\n\t"
      "s_nop 1\n\t"
      "s_branch 29f\n\t"
      "20:\n\t"
      "s_mov_b32 s66, 0x20000\n\t"
      "s_mov_b32 %[flag], 1\n\t"
      "s_add_u32 s64, %[mb], %[da]\n\t"
      "s_add_u32 s65, %[mb], %[db]\n\t"
      "22:\n\t"
      "buffer_load_dwordx4 v[128:131], %[o0], %[srd], s64 offen sc1\n\t"
      "buffer_load_dwordx4 v[132:135], %[o0], %[srd], s64 offen offset:1024 sc1\n\t"
      "s_waitcnt vmcnt(0)\n\t"
      "v_max_u32 v160, v128, v129\n\t"
      "v_max3_u32 v160, v160, v130, v131\n\t"
      "v_max3_u32 v160, v160, v132, v133\n\t"
      "v_max3_u32 v160, v160, v134, v135\n\t"
      "v_cmp_eq_u32 vcc, -1, v160\n\t"
      "s_cbranch_vccz 23f\n\t"
      "s_sleep 1\n\t"
      "s_sub_u32 s66, s66, 1\n\t"
      "s_cbranch_scc0 22b\n\t"
      "s_endpgm\n\t"
      "23:\n\t"
      "ds_write_b128 %[ma], v[128:131] offset:4096\n\t"
      "ds_write_b128 %[ma], v[132:135] offset:5120\n\t"
      "s_nop 1\n\t"
      "s_waitcnt lgkmcnt(0)\n\t"
      "s_branch 21b\n\t"
      "29:\n\t"
      : [flag] "=&s"(flag)
      : [o0] "v"(gl0), [srd] "s"(srd), [mb] "s"(US(mb)), [ma0] "s"(US(mb + ca)), [mb8] "s"(US(mb + cb)), [da] "s"(US(da)), [db] "s"(US(db)), [yb] "s"(US(yb)), [yn] "s"(US(yn)), [idx] "v"(idx), [ma] "v"(ma), [wa] "v"(wa), [ra] "v"(ra), [yo] "v"(y_off), [yo8] "v"(y_off8), WPIN(W)
      : "v128", "v129", "v130", "v131", "v132", "v133", "v134", "v135", "v136", "v137", "v138", "v139", "v140", "v141", "v142", "v143", "v144", "v145", "v146", "v147", "v148", "v149", "v150", "v151", "v152", "v153", "v154", "v155", "v156", "v157", "v158", "v159", "v160", "v161", "v162", "v163", "v164", "v165", "v166", "v167", "v168", "v169", "v170", "v171", "v172", "v173", "v174", "v175", "v176", "v177", "v178", "v179", "v180", "v181", "v182", "v183", "v184", "v185", "v186", "v187", "v188", "v189", "v190", "v191", "v192", "v193", "v194", "v195", "v196", "v197", "v198", "v199", "v200", "v201", "v202", "v203", "v204", "v205", "v206", "v207", "v208", "v209", "v210", "v211", "v212", "v213", "v214", "v215", "v216", "v217", "v218", "v219", "s64", "s65", "s66", "s67", "s68", "s69", "s70", "s71", "s72", "s73", "s74", "s75", "s76", "s77", "s78", "s79", "s80", "s81", "s82", "s83", "s84", "s85", "s86", "s87", "s88", "s89", "s90", "s91", "s92", "s93", "s94", "s95", "vcc", "scc", "memory");
    }
  }
}

DI int wmax(int v) {
  v = max(v, __builtin_amdgcn_mov_dpp(v, 0xB1, 0xf, 0xf, true));
  v = max(v, __builtin_amdgcn_mov_dpp(v, 0x4E, 0xf, 0xf, true));
  v = max(v, __builtin_amdgcn_mov_dpp(v, 0x124, 0xf, 0xf, true));
  v = max(v, __builtin_amdgcn_mov_dpp(v, 0x128, 0xf, 0xf, true));
  return max(max(__builtin_amdgcn_readlane(v, 0), __builtin_amdgcn_readlane(v, 16)), max(__builtin_amdgcn_readlane(v, 32), __builtin_amdgcn_readlane(v, 48)));
}
DI void get_stamps(__amdgpu_buffer_rsrc_t rs, u32 mb, int i0, int n, int lane, u32 ref, int add32, int& te, bool& ok) {
  const u32 off = st_addr(i0 + (lane < n ? lane : 0));
  u2 v = {0, 0};
  for (int k = 0; k < 65536; ++k) {
    asm volatile("" ::: "memory");
    v = ld8(rs, off, mb);
    if (__builtin_amdgcn_ballot_w64(v[0] == SENT) == 0) break;
  }
  const int e = (int)(v[0] - ref);
  ok = __builtin_amdgcn_ballot_w64(v[1] >= 0x7000u) == 0;
  te = wmax(e + (lane >= 32 ? add32 : 0));
}
DI int late_shift(int tm, int d, int rs0, int rsh) { return tm + LTH > 0 ? max(rs0, tm + rsh) : 0; }
DI void role_C(const Params& p, int cc, u64 tag) {
  const int tid = threadIdx.x, lane = tid & 63, w = __builtin_amdgcn_readfirstlane(tid >> 6);
  if (w > 2) return;
  const u32 par = get_par(p, tag, 1);
  const int c = lane & 15, g = lane >> 4;
  const __amdgpu_buffer_rsrc_t rs = mkrsrc(p.ws, WS_SPAN);
  const u4 srd = mksrd(p.ws, WS_SPAN);
  const int d = p.dM & 0xFFFF;
  volatile LAS u32* box = (volatile LAS u32*)(uintptr_t)0;
  if (w == 1) {
    const u32 n1 = st_addr(64 + (lane < 36 ? lane : 0));
    for (int s = 0; s < NSEG - 1; ++s) {
      const u32 mbs = mp_base(s, par);
      if (s == 0) {
        int x; bool ok;
        get_stamps(rs, mbs, 0, 64, lane, 0, 0, x, ok);
        box[64] = rtc() * 8u + 420u;
        bar();
        continue;
      }
      const u32 gm = get_sched(rs, mbs, 0);
      u32 tgt = get_sched(rs, mbs, 1), gn = get_sched(rs, mbs, 2);
      {
        const u32 mb = mp_base(s - 1, par);
        box[lane] = gm - 4096u;
        wait_fine(gm + p.pad1 - HLEAD);
        u32 keep;
        asm volatile("s_waitcnt lgkmcnt(0)\n\ts_mov_b32 %0, m0\n\ts_mov_b32 m0, 0\n\ts_nop 2\n\tbuffer_load_dword %1, %2, %3 offen sc1 lds\n\ts_mov_b32 m0, %0" : "=&s"(keep) : "v"(n1), "s"(srd), "s"(US(mb)) : "memory");
        wait_fine(tgt - 230u);
        const u32 v = box[lane];
        int tm;
        if (__builtin_amdgcn_ballot_w64(v == SENT) != 0) { bool ok; get_stamps(rs, mb, 64, 36, lane, gm, 0, tm, ok); }
        else tm = wmax((int)(v - gm));
        const int sh = min(late_shift(tm, d, p.dV & 0xFFFF, p.dV >> 16), 30000);
        tgt += sh; gn += sh;
      }
      box[64 + (s & 1)] = gn;
      if (cc >= 32) tgt -= p.place;
      tgt = __builtin_amdgcn_readfirstlane(tgt);
      asm volatile(
      "1:\n\t"
      "s_memrealtime s[64:65]\n\t"
      "s_waitcnt lgkmcnt(0)\n\t"
      "s_lshl_b32 s64, s64, 3\n\t"
      "s_sub_i32 s64, %[tgt], s64\n\t"
      "s_cmp_gt_i32 s64, 175\n\t"
      "s_cbranch_scc1 1b\n\t"
      "s_cmp_lt_i32 s64, 48\n\t"
      "s_cbranch_scc1 2f\n\t"
      "s_sub_i32 s64, s64, 64\n\t"
      "s_cmp_lt_i32 s64, 51\n\t"
      "s_cbranch_scc1 4f\n\t"
      "s_memrealtime s[64:65]\n\t"
      "s_nop 1\n\t"
      "s_memrealtime s[66:67]\n\t"
      "s_nop 1\n\t"
      "s_memrealtime s[68:69]\n\t"
      "s_nop 1\n\t"
      "s_memrealtime s[70:71]\n\t"
      "s_nop 1\n\t"
      "s_memrealtime s[72:73]\n\t"
      "s_nop 1\n\t"
      "s_memrealtime s[74:75]\n\t"
      "s_nop 1\n\t"
      "s_memrealtime s[76:77]\n\t"
      "s_nop 1\n\t"
      "s_memrealtime s[78:79]\n\t"
      "s_waitcnt lgkmcnt(0)\n\t"
      "s_add_u32 s64, s64, s66\n\t"
      "s_add_u32 s68, s68, s70\n\t"
      "s_add_u32 s72, s72, s74\n\t"
      "s_add_u32 s76, s76, s78\n\t"
      "s_add_u32 s64, s64, s68\n\t"
      "s_add_u32 s72, s72, s76\n\t"
      "s_add_u32 s64, s64, s72\n\t"
      "s_sub_i32 s64, %[tgt], s64\n\t"
      "s_sub_i32 s64, s64, 75\n\t"
      "4:\n\t"
      "s_mul_i32 s64, s64, 3\n\t"
      "s_and_b32 s64, s64, -4\n\t"
      "s_max_i32 s64, s64, 0\n\t"
      "s_min_i32 s64, s64, 288\n\t"
      "s_getpc_b64 s[66:67]\n\t"
      "5:\n\t"
      "s_sub_i32 s64, 2f-5b, s64\n\t"
      "s_add_u32 s66, s66, s64\n\t"
      "s_addc_u32 s67, s67, 0\n\t"
      "s_setpc_b64 s[66:67]\n\t"
      "s_nop 0\n\t"
      "s_nop 0\n\t"
      "s_nop 0\n\t"
      "s_nop 0\n\t"
      "s_nop 0\n\t"
      "s_nop 0\n\t"
      "s_nop 0\n\t"
      "s_nop 0\n\t"
      "s_nop 0\n\t"
      "s_nop 0\n\t"
      "s_nop 0\n\t"
      "s_nop 0\n\t"
      "s_nop 0\n\t"
      "s_nop 0\n\t"
      "s_nop 0\n\t"
      "s_nop 0\n\t"
      "s_nop 0\n\t"
      "s_nop 0\n\t"
      "s_nop 0\n\t"
      "s_nop 0\n\t"
      "s_nop 0\n\t"
      "s_nop 0\n\t"
      "s_nop 0\n\t"
      "s_nop 0\n\t"
      "s_nop 0\n\t"
      "s_nop 0\n\t"
      "s_nop 0\n\t"
      "s_nop 0\n\t"
      "s_nop 0\n\t"
      "s_nop 0\n\t"
      "s_nop 0\n\t"
      "s_nop 0\n\t"
      "s_nop 0\n\t"
      "s_nop 0\n\t"
      "s_nop 0\n\t"
      "s_nop 0\n\t"
      "s_nop 0\n\t"
      "s_nop 0\n\t"
      "s_nop 0\n\t"
      "s_nop 0\n\t"
      "s_nop 0\n\t"
      "s_nop 0\n\t"
      "s_nop 0\n\t"
      "s_nop 0\n\t"
      "s_nop 0\n\t"
      "s_nop 0\n\t"
      "s_nop 0\n\t"
      "s_nop 0\n\t"
      "s_nop 0\n\t"
      "s_nop 0\n\t"
      "s_nop 0\n\t"
      "s_nop 0\n\t"
      "s_nop 0\n\t"
      "s_nop 0\n\t"
      "s_nop 0\n\t"
      "s_nop 0\n\t"
      "s_nop 0\n\t"
      "s_nop 0\n\t"
      "s_nop 0\n\t"
      "s_nop 0\n\t"
      "s_nop 0\n\t"
      "s_nop 0\n\t"
      "s_nop 0\n\t"
      "s_nop 0\n\t"
      "s_nop 0\n\t"
      "s_nop 0\n\t"
      "s_nop 0\n\t"
      "s_nop 0\n\t"
      "s_nop 0\n\t"
      "s_nop 0\n\t"
      "s_nop 0\n\t"
      "s_nop 0\n\t"
      "2:\n\t"
        "s_barrier" :: [tgt] "s"(US(tgt)) : "s64", "s65", "s66", "s67", "s68", "s69", "s70", "s71", "s72", "s73", "s74", "s75", "s76", "s77", "s78", "s79", "s80", "s81", "s82", "s83", "s84", "s85", "s86", "s87", "s88", "s89", "s90", "s91", "s92", "s93", "s94", "s95", "scc", "memory");
      __builtin_amdgcn_s_sleep(8);
    }
    return;
  }
  const int idx = (c & 1) ? 0xEEEE : 0x4444;
  const u32 l16 = lane * 16;
  const int rot = blockIdx.x & 3;
  const int irow = 2 * g + (c & 1), cp = c >> 1;
  const u32 mlog = cc * 256 + (2 * irow + (cp & 1)) * 16 + (cp >> 1) * 4;
  const u32 m_off = (mlog & 0x1FFF) | ((mlog >> 13) << 14);
  const u32 m_off2 = m_off + (cc < 32 ? 65536u : 1024u), m_off3 = m_off + (cc < 32 ? 114688u : 2048u);
  const unsigned long long evmask = 0x5555555555555555ull;
  const u32 fl = fnear(127, par);
  int seen = 0;
  u32* stamp = (u32*)(p.ws + OFF_CTL) + CTL_STAMP + 64 * 64 * 4;
  for (int s = w >> 1; s < NSEG - 1; s += 2) {
    const u32 mb = mp_base(s, par), yb = __builtin_amdgcn_readfirstlane(y_base(s, par) + cc * 16384u), mb1 = mp_base(s + 1, par);
    const u32 zb = z_base(s, cc);
    u32v Z[4];
    const bool early = seen > s || s == 0;
    if (!early) {
      __builtin_amdgcn_s_barrier();
      const u32 t = US(box[64 + ((s - 1) & 1)]);
      wait_fine(t - 60u);
      if (!wait_flags(rs, s < 2 ? ffar(32 + (lane & 31), par) : fl, s + 1, seen)) return;
      wait_fine(t + p.pad1 - 60u);
    } else { if (s == 0) { EVT(0) } if (!wait_flags(rs, ffar(32 + (lane & 31), par), s + 1, seen)) return; }
    if (s < 3) { EVT(1 + s) }
#pragma unroll
    for (int i = 0; i < 32; ++i) { u4 x = ld16(rs, l16 + (i & 3) * 1024, zb + (((i >> 3) + rot) & 3) * 8192 + ((i >> 2) & 1) * 4096); Z[i >> 3][(i & 7) * 4] = x[0]; Z[i >> 3][(i & 7) * 4 + 1] = x[1]; Z[i >> 3][(i & 7) * 4 + 2] = x[2]; Z[i >> 3][(i & 7) * 4 + 3] = x[3]; }
    if (early && s > 0) __builtin_amdgcn_s_barrier();
    int tries = 0;
    if (s == 0) { asm volatile("" :: "{a[0:31]}"(Z[0]), "{a[32:63]}"(Z[1]), "{a[64:95]}"(Z[2]), "{a[96:127]}"(Z[3])); put_stamp(rs, mp_base(NSEG - 1, par), 64 + cc, 0, 0, false, lane); EVT(4) }
    u32 t0 = 0, t1 = 0, t2 = 0, tf = 0, te, du;
    u32 pb[4];
#pragma unroll
    for (int j = 0; j < 4; ++j) { const int jj = (j + rot) & 3; pb[j] = mb + 32768u + (jj >> 1) * 16384u + (jj & 1) * 4096u; }
    for (;;) {
      u32 flag;
      asm volatile(
      "v_mov_b64 v[208:209], 0\n\t"
      "v_mov_b64 v[210:211], 0\n\t"
      "s_mov_b32 %[flag], 1\n\t"
      "s_add_u32 s85, %[yb], 0x1000\n\t"
      "s_add_u32 s86, %[mb], 0x5000\n\t"
      "s_cmp_lg_u32 %[rt], 0\n\t"
      "s_cbranch_scc1 3f\n\t"
      "s_barrier\n\t"
      "3:\n\t"
      "buffer_load_dwordx4 v[224:227], %[o0], %[srd], %[yb] offen sc1\n\t"
      "buffer_load_dwordx4 v[228:231], %[o0], %[srd], %[yb] offen offset:1024 sc1\n\t"
      "buffer_load_dwordx4 v[232:235], %[o0], %[srd], %[yb] offen offset:2048 sc1\n\t"
      "buffer_load_dwordx4 v[236:239], %[o0], %[srd], %[yb] offen offset:3072 sc1\n\t"
      "buffer_load_dwordx4 v[240:243], %[o0], %[srd], s85 offen sc1\n\t"
      "buffer_load_dwordx4 v[244:247], %[o0], %[srd], s85 offen offset:1024 sc1\n\t"
      "buffer_load_dwordx4 v[248:251], %[o0], %[srd], s85 offen offset:2048 sc1\n\t"
      "buffer_load_dwordx4 v[252:255], %[o0], %[srd], s85 offen offset:3072 sc1\n\t"
      "s_nop 15\n\t"
      "s_nop 15\n\t"
      "s_memtime s[92:93]\n\t"
      "buffer_load_dwordx4 v[128:131], %[o0], %[srd], %[p0] offen sc1\n\t"
      "buffer_load_dwordx4 v[132:135], %[o0], %[srd], %[p0] offen offset:1024 sc1\n\t"
      "buffer_load_dwordx4 v[136:139], %[o0], %[srd], %[p0] offen offset:2048 sc1\n\t"
      "buffer_load_dwordx4 v[140:143], %[o0], %[srd], %[p0] offen offset:3072 sc1\n\t"
      "buffer_load_dwordx4 v[144:147], %[o0], %[srd], %[p1] offen sc1\n\t"
      "buffer_load_dwordx4 v[148:151], %[o0], %[srd], %[p1] offen offset:1024 sc1\n\t"
      "buffer_load_dwordx4 v[152:155], %[o0], %[srd], %[p1] offen offset:2048 sc1\n\t"
      "buffer_load_dwordx4 v[156:159], %[o0], %[srd], %[p1] offen offset:3072 sc1\n\t"
      "buffer_load_dwordx4 v[160:163], %[o0], %[srd], %[p2] offen sc1\n\t"
      "buffer_load_dwordx4 v[164:167], %[o0], %[srd], %[p2] offen offset:1024 sc1\n\t"
      "buffer_load_dwordx4 v[168:171], %[o0], %[srd], %[p2] offen offset:2048 sc1\n\t"
      "buffer_load_dwordx4 v[172:175], %[o0], %[srd], %[p2] offen offset:3072 sc1\n\t"
      "buffer_load_dwordx4 v[176:179], %[o0], %[srd], %[p3] offen sc1\n\t"
      "buffer_load_dwordx4 v[180:183], %[o0], %[srd], %[p3] offen offset:1024 sc1\n\t"
      "buffer_load_dwordx4 v[184:187], %[o0], %[srd], %[p3] offen offset:2048 sc1\n\t"
      "buffer_load_dwordx4 v[188:191], %[o0], %[srd], %[p3] offen offset:3072 sc1\n\t"
      "buffer_load_dwordx4 v[192:195], %[o0], %[srd], s86 offen sc1\n\t"
      "buffer_load_dwordx4 v[196:199], %[o0], %[srd], s86 offen offset:1024 sc1\n\t"
      "buffer_load_dwordx4 v[200:203], %[o0], %[srd], s86 offen offset:2048 sc1\n\t"
      "buffer_load_dwordx4 v[204:207], %[o0], %[srd], s86 offen offset:3072 sc1\n\t"
      "s_mov_b64 vcc, %[ev]\n\t"
      "s_waitcnt vmcnt(19)\n\t"
      "v_smfmac_f32_16x16x64_bf16 v[208:211], v[128:131], a[0:7], %[idx]\n\t"
      "s_waitcnt vmcnt(18)\n\t"
      "v_smfmac_f32_16x16x64_bf16 v[208:211], v[132:135], a[8:15], %[idx]\n\t"
      "s_waitcnt vmcnt(17)\n\t"
      "v_smfmac_f32_16x16x64_bf16 v[208:211], v[136:139], a[16:23], %[idx]\n\t"
      "s_waitcnt vmcnt(16)\n\t"
      "v_smfmac_f32_16x16x64_bf16 v[208:211], v[140:143], a[24:31], %[idx]\n\t"
      "s_waitcnt vmcnt(15)\n\t"
      "v_smfmac_f32_16x16x64_bf16 v[208:211], v[144:147], a[32:39], %[idx]\n\t"
      "s_waitcnt vmcnt(14)\n\t"
      "v_smfmac_f32_16x16x64_bf16 v[208:211], v[148:151], a[40:47], %[idx]\n\t"
      "s_waitcnt vmcnt(13)\n\t"
      "v_smfmac_f32_16x16x64_bf16 v[208:211], v[152:155], a[48:55], %[idx]\n\t"
      "s_waitcnt vmcnt(12)\n\t"
      "v_smfmac_f32_16x16x64_bf16 v[208:211], v[156:159], a[56:63], %[idx]\n\t"
      "s_waitcnt vmcnt(11)\n\t"
      "v_smfmac_f32_16x16x64_bf16 v[208:211], v[160:163], a[64:71], %[idx]\n\t"
      "s_waitcnt vmcnt(10)\n\t"
      "v_smfmac_f32_16x16x64_bf16 v[208:211], v[164:167], a[72:79], %[idx]\n\t"
      "s_waitcnt vmcnt(9)\n\t"
      "v_smfmac_f32_16x16x64_bf16 v[208:211], v[168:171], a[80:87], %[idx]\n\t"
      "s_waitcnt vmcnt(8)\n\t"
      "v_smfmac_f32_16x16x64_bf16 v[208:211], v[172:175], a[88:95], %[idx]\n\t"
      "s_waitcnt vmcnt(7)\n\t"
      "v_smfmac_f32_16x16x64_bf16 v[208:211], v[176:179], a[96:103], %[idx]\n\t"
      "s_waitcnt vmcnt(6)\n\t"
      "v_smfmac_f32_16x16x64_bf16 v[208:211], v[180:183], a[104:111], %[idx]\n\t"
      "s_waitcnt vmcnt(5)\n\t"
      "v_smfmac_f32_16x16x64_bf16 v[208:211], v[184:187], a[112:119], %[idx]\n\t"
      "s_waitcnt vmcnt(4)\n\t"
      "v_smfmac_f32_16x16x64_bf16 v[208:211], v[188:191], a[120:127], %[idx]\n\t"
      "s_waitcnt vmcnt(3)\n\t"
      "v_smfmac_f32_16x16x64_bf16 v[208:211], v[192:195], v[224:231], %[idx]\n\t"
      "s_waitcnt vmcnt(2)\n\t"
      "v_smfmac_f32_16x16x64_bf16 v[208:211], v[196:199], v[232:239], %[idx]\n\t"
      "s_waitcnt vmcnt(1)\n\t"
      "v_smfmac_f32_16x16x64_bf16 v[208:211], v[200:203], v[240:247], %[idx]\n\t"
      "s_waitcnt vmcnt(0)\n\t"
      "v_smfmac_f32_16x16x64_bf16 v[208:211], v[204:207], v[248:255], %[idx]\n\t"
      "s_nop 7\n\t"
      "v_add_f32 v210, v210, v211\n\t"
      "v_add_f32 v208, v208, v209\n\t"
      "s_nop 0\n\t"
      "v_cndmask_b32_dpp v216, v210, v208, vcc quad_perm:[1,0,3,2] row_mask:0xf bank_mask:0xf\n\t"
      "s_not_b64 vcc, vcc\n\t"
      "v_cndmask_b32_dpp v217, v208, v210, vcc quad_perm:[1,0,3,2] row_mask:0xf bank_mask:0xf\n\t"
      "v_cmp_u_f32 vcc, v208, v210\n\t"
      "v_cvt_pk_bf16_f32 v216, v216, v217\n\t"
      "s_cbranch_vccnz 7f\n\t"
      "6:\n\t"
      "buffer_store_dword v216, %[mo], %[srd], %[mb1] offen sc1\n\t"
      "buffer_store_dword v216, %[mo3], %[srd], %[mb1] offen sc1\n\t"
      "buffer_store_dword v216, %[mo2], %[srd], %[mb1] offen sc1\n\t"
      "s_memtime s[94:95]\n\t"
      "s_memrealtime s[64:65]\n\t"
      "s_nop 1\n\t"
      "s_memrealtime s[66:67]\n\t"
      "s_nop 1\n\t"
      "s_memrealtime s[68:69]\n\t"
      "s_nop 1\n\t"
      "s_memrealtime s[70:71]\n\t"
      "s_nop 1\n\t"
      "s_memrealtime s[72:73]\n\t"
      "s_nop 1\n\t"
      "s_memrealtime s[74:75]\n\t"
      "s_nop 1\n\t"
      "s_memrealtime s[76:77]\n\t"
      "s_nop 1\n\t"
      "s_memrealtime s[78:79]\n\t"
      "s_waitcnt lgkmcnt(0)\n\t"
      "s_add_u32 s64, s64, s66\n\t"
      "s_add_u32 s68, s68, s70\n\t"
      "s_add_u32 s72, s72, s74\n\t"
      "s_add_u32 s76, s76, s78\n\t"
      "s_add_u32 s64, s64, s68\n\t"
      "s_add_u32 s72, s72, s76\n\t"
      "s_add_u32 %[te], s64, s72\n\t"
      "s_sub_u32 %[du], s94, s92\n\t"
      "s_mov_b32 %[flag], 0\n\t"
      "s_branch 9f\n\t"
      "7:\n\t"
      "v_mov_b32 v218, 0\n\t"
      "v_max3_u32 v218, v218, v128, v129\n\t"
      "v_max3_u32 v218, v218, v130, v131\n\t"
      "v_max3_u32 v218, v218, v132, v133\n\t"
      "v_max3_u32 v218, v218, v134, v135\n\t"
      "v_max3_u32 v218, v218, v136, v137\n\t"
      "v_max3_u32 v218, v218, v138, v139\n\t"
      "v_max3_u32 v218, v218, v140, v141\n\t"
      "v_max3_u32 v218, v218, v142, v143\n\t"
      "v_max3_u32 v218, v218, v144, v145\n\t"
      "v_max3_u32 v218, v218, v146, v147\n\t"
      "v_max3_u32 v218, v218, v148, v149\n\t"
      "v_max3_u32 v218, v218, v150, v151\n\t"
      "v_max3_u32 v218, v218, v152, v153\n\t"
      "v_max3_u32 v218, v218, v154, v155\n\t"
      "v_max3_u32 v218, v218, v156, v157\n\t"
      "v_max3_u32 v218, v218, v158, v159\n\t"
      "v_max3_u32 v218, v218, v160, v161\n\t"
      "v_max3_u32 v218, v218, v162, v163\n\t"
      "v_max3_u32 v218, v218, v164, v165\n\t"
      "v_max3_u32 v218, v218, v166, v167\n\t"
      "v_max3_u32 v218, v218, v168, v169\n\t"
      "v_max3_u32 v218, v218, v170, v171\n\t"
      "v_max3_u32 v218, v218, v172, v173\n\t"
      "v_max3_u32 v218, v218, v174, v175\n\t"
      "v_max3_u32 v218, v218, v176, v177\n\t"
      "v_max3_u32 v218, v218, v178, v179\n\t"
      "v_max3_u32 v218, v218, v180, v181\n\t"
      "v_max3_u32 v218, v218, v182, v183\n\t"
      "v_max3_u32 v218, v218, v184, v185\n\t"
      "v_max3_u32 v218, v218, v186, v187\n\t"
      "v_max3_u32 v218, v218, v188, v189\n\t"
      "v_max3_u32 v218, v218, v190, v191\n\t"
      "v_max3_u32 v218, v218, v192, v193\n\t"
      "v_max3_u32 v218, v218, v194, v195\n\t"
      "v_max3_u32 v218, v218, v196, v197\n\t"
      "v_max3_u32 v218, v218, v198, v199\n\t"
      "v_max3_u32 v218, v218, v200, v201\n\t"
      "v_max3_u32 v218, v218, v202, v203\n\t"
      "v_max3_u32 v218, v218, v204, v205\n\t"
      "v_max3_u32 v218, v218, v206, v207\n\t"
      "v_max3_u32 v218, v218, v224, v225\n\t"
      "v_max3_u32 v218, v218, v226, v227\n\t"
      "v_max3_u32 v218, v218, v228, v229\n\t"
      "v_max3_u32 v218, v218, v230, v231\n\t"
      "v_max3_u32 v218, v218, v232, v233\n\t"
      "v_max3_u32 v218, v218, v234, v235\n\t"
      "v_max3_u32 v218, v218, v236, v237\n\t"
      "v_max3_u32 v218, v218, v238, v239\n\t"
      "v_max3_u32 v218, v218, v240, v241\n\t"
      "v_max3_u32 v218, v218, v242, v243\n\t"
      "v_max3_u32 v218, v218, v244, v245\n\t"
      "v_max3_u32 v218, v218, v246, v247\n\t"
      "v_max3_u32 v218, v218, v248, v249\n\t"
      "v_max3_u32 v218, v218, v250, v251\n\t"
      "v_max3_u32 v218, v218, v252, v253\n\t"
      "v_max3_u32 v218, v218, v254, v255\n\t"
      "v_min_u32 v216, -2, v216\n\t"
      "v_cmp_eq_u32 vcc, -1, v218\n\t"
      "s_cbranch_vccz 6b\n\t"
      "9:\n\t"
      "s_waitcnt lgkmcnt(0)\n\t"
      : [flag] "=&s"(flag) , [te] "=&s"(te), [du] "=&s"(du)
      : [rt] "s"(US(tries)), [o0] "v"(l16), [p0] "s"(US(pb[0])), [p1] "s"(US(pb[1])), [p2] "s"(US(pb[2])), [p3] "s"(US(pb[3])), [srd] "s"(srd), [mb] "s"(US(mb)), [mb1] "s"(US(mb1)), [idx] "v"(idx), [ev] "s"(evmask), [mo] "v"(m_off), [mo2] "v"(m_off2), [mo3] "v"(m_off3), [yb] "s"(US(yb)),
        "{a[0:31]}"(Z[0]), "{a[32:63]}"(Z[1]), "{a[64:95]}"(Z[2]), "{a[96:127]}"(Z[3])
      : "v128", "v129", "v130", "v131", "v132", "v133", "v134", "v135", "v136", "v137", "v138", "v139", "v140", "v141", "v142", "v143", "v144", "v145", "v146", "v147", "v148", "v149", "v150", "v151", "v152", "v153", "v154", "v155", "v156", "v157", "v158", "v159", "v160", "v161", "v162", "v163", "v164", "v165", "v166", "v167", "v168", "v169", "v170", "v171", "v172", "v173", "v174", "v175", "v176", "v177", "v178", "v179", "v180", "v181", "v182", "v183", "v184", "v185", "v186", "v187", "v188", "v189", "v190", "v191", "v192", "v193", "v194", "v195", "v196", "v197", "v198", "v199", "v200", "v201", "v202", "v203", "v204", "v205", "v206", "v207", "v208", "v209", "v210", "v211", "v212", "v213", "v214", "v215", "v216", "v217", "v218", "v224", "v225", "v226", "v227", "v228", "v229", "v230", "v231", "v232", "v233", "v234", "v235", "v236", "v237", "v238", "v239", "v240", "v241", "v242", "v243", "v244", "v245", "v246", "v247", "v248", "v249", "v250", "v251", "v252", "v253", "v254", "v255", "s64", "s65", "s66", "s67", "s68", "s69", "s70", "s71", "s72", "s73", "s74", "s75", "s76", "s77", "s78", "s79", "s80", "s81", "s82", "s83", "s84", "s85", "s86", "s87", "s88", "s89", "s90", "s91", "s92", "s93", "s94", "s95", "vcc", "scc", "memory");
      if (tries == 0) tf = t0;
      if (__builtin_expect(flag == 0, 1)) break;
      if (++tries > SPIN_CAP) return;
    }
    put_stamp(rs, mb, 64 + cc, te, du, tries != 0, lane);
    if (seen <= s + 2) seen = max(seen, US((int)__builtin_amdgcn_raw_buffer_load_b32(rs, fl, 0, 16)));
    { const u32 t = US(box[64 + (s & 1)]) + p.pad1 + (early ? 0 : 150); wait_fine(t); }
  }
}

DI int med3(int a, int b, int c) { return max(min(a, b), min(max(a, b), c)); }
DI int med5(int a, int b, int c, int d, int e) { return med3(max(min(a, b), min(c, d)), min(max(a, b), max(c, d)), e); }
DI void role_T(const Params& p, u64 tag) {
  if (threadIdx.x >= 64) return;
  const u32 par = get_par(p, tag, 1);
  const int lane = threadIdx.x;
  const __amdgpu_buffer_rsrc_t rs = mkrsrc(p.ws, WS_SPAN);
  const int d = p.dM & 0xFFFF, d2 = p.dM >> 16, mar = 0, YL = p.dP, LEAD = 680;
  int A = 780, C = 390, ah[4] = {780, 780, 700, 700}, ch[4] = {390, 390, 340, 340};
  int a, c, shPrev = 1; bool da, dc;
  u32* dbg = (u32*)(p.ws + OFF_CTL) + 8192 + 256;
  auto publish = [&](u32 base, u32 gm, u32 gp, u32 gn) { u4 v = {gm & ~1u, (gp - YL) & ~1u, gn & ~1u, 0u}; st16(v, rs, lane == 0 ? SCH_OFF : OOB, base); };
  get_stamps(rs, mp_base(NSEG - 1, par), 64, 36, lane, 0, 0, a, da);
  get_stamps(rs, mp_base(0, par), 0, 64, lane, 0, 0, a, da);
  u32 gmPrev = 0, gpPrev = fine_now();
  const int WX = p.pad2 & 0xFFFF, WN = p.pad2 >> 16;
  u32 gm = gpPrev + LEAD, gp = gm + A + d, gn = gp + C + d2 + WX;
  publish(mp_base(1, par), gm, gp, gn);
  for (int s = 1; s < NSEG - 1; ++s) {
    const u32 mb = mp_base(s - 1, par);
    u32 mn = mp_base(s + 1, par);
    wait_fine(gm - 170u);
    get_stamps(rs, mb, 0, 64, lane, gmPrev, YL - p.pad0, a, da);
    if (shPrev == 0 && da) { A = med5(a, ah[0], ah[1], ah[2], ah[3]) + mar; ah[0] = ah[1]; ah[1] = ah[2]; ah[2] = ah[3]; ah[3] = a; }
    asm volatile("" : "+s"(mn), "+v"(A));
    wait_fine(gm + 130u);
    get_stamps(rs, mb, 64, 36, lane, gpPrev, 0, c, dc);
    const int sh = min(late_shift((int)(gpPrev + c - gm), d, p.dV & 0xFFFF, p.dV >> 16), 30000);
    gmPrev = gm; gpPrev = gp + sh; gm = gn + sh; gp = gm + A + d; gn = gp + C + d2 + (s < WN ? WX : 0); const int shPP = shPrev; shPrev = sh;
    publish(mn, gm, gp, gn);
    if (s > 1 && dc && shPP == 0) { C = med5(c, ch[0], ch[1], ch[2], ch[3]) + mar; ch[0] = ch[1]; ch[1] = ch[2]; ch[2] = ch[3]; ch[3] = c; }
  }
}

DI void dma_seg(u4 xsrd, u32 xv, u32 so, u32 dl) {
#pragma unroll
  for (int b = 0; b < 9; ++b) {
    u32 keep;
    asm volatile("s_mov_b32 %0, m0\n\ts_mov_b32 m0, %4\n\ts_nop 2\n\tbuffer_load_dwordx4 %1, %2, %3 offen lds\n\ts_mov_b32 m0, %0" : "=&s"(keep) : "v"(xv), "s"(xsrd), "s"(so + b * 128), "s"(dl + b * 4096) : "memory");
  }
}

template <bool FFN> DI void role_G1(const Params& p, int id, u2 e, u64 tag) {
  const int tid = threadIdx.x, lane = tid & 63, w = __builtin_amdgcn_readfirstlane(tid >> 6);
  const int c = lane & 15, g = lane >> 4;
  const int cg = id % 18, s0 = FFN ? id / 18 : 0;
  const __amdgpu_buffer_rsrc_t rs = mkrsrc(p.ws, WS_SPAN);
  const u4 srd = mksrd(p.ws, WS_SPAN), xsrd = mksrd(p.x, 2048 * 1152);
  const u32 xv = (8 * w + (lane >> 3)) * 1152 + (((lane & 7) ^ ((lane >> 3) & 7)) * 16);
  const u32 wl = w * 1024;
  dma_seg(xsrd, xv, s0 * 36864, wl);
  u32v W[9];
  if constexpr (FFN) load_w2x<9216>(W, p.w1 + cg * 256, p.w1 + cg * 256 + 128, w, lane);
  else load_w2x<8192>(W, p.Wq + id * 128, p.Wk + id * 128, w, lane);
  const u32 par = get_par(p, FFN ? tag : mk_tag(p, e), 0);
  const u32 ra0 = c * 128 + ((g ^ (c & 7)) * 16), ra1 = c * 128 + (((4 + g) ^ (c & 7)) * 16);
  const u32 fo = tid == 0 ? (FFN ? ffar(64 + id, par) : fnear(id, par)) : OOB;
  const u32 l16 = lane * 16, fm = ~par;
  if constexpr (FFN) {
    u16v ci;
#pragma unroll
    for (int i = 0; i < 16; ++i) ci[i] = (u32)p.b1[cg * 256 + (i >> 3) * 128 + 32 * w + ((i >> 2) & 1) * 16 + 4 * g + (i & 3)] << 16;
    const u32 ob = OFF_H + (u32)cg * 8192u + wl;
    wait_vm<0>(); bar();
    LOOPSTART
    asm volatile(
      "s_mov_b32 s73, m0\n\t"
      "s_mov_b32 s68, 0\n\t"
      "s_mov_b32 s69, 36864\n\t"
      "s_add_u32 s70, %[wl], 73728\n\t"
      "s_mov_b32 s71, 73728\n\t"
      "v_mov_b32 v62, 0\n\t"
      "v_mov_b32 v56, %[ra0]\n\t"
      "v_mov_b32 v57, %[ra1]\n\t"
      "v_add_u32 v58, 36864, %[ra0]\n\t"
      "v_add_u32 v59, 36864, %[ra1]\n\t"
      "v_mov_b32 v61, 0\n\t"
      "ds_read_b128 v[160:163], v56 offset:0\n\t"
      "ds_read_b128 v[164:167], v57 offset:0\n\t"
      "ds_read_b128 v[168:171], v56 offset:4096\n\t"
      "ds_read_b128 v[172:175], v57 offset:4096\n\t"
      "ds_read_b128 v[176:179], v56 offset:8192\n\t"
      "s_waitcnt lgkmcnt(0)\n\t"
      "s_mov_b32 s78, 0xf0000000\n\t"
      "s_mov_b32 s79, 0xf0000000\n\t"
      "s_sub_u32 s69, s69, 36864\n\t"
      "s_add_u32 s70, %[wl], 36864\n\t"
      "s_mov_b32 s68, %[s0]\n\t"
      "s_add_u32 s71, s68, 1\n\t"
      "s_mul_i32 s71, s71, 36864\n\t"
      "s_mov_b32 s75, 0xbdd2d3e8\n\t"
      "v_mov_b32 v60, 0xc0135761\n\t"
      "10:\n\t"
      "s_waitcnt lgkmcnt(4)\n\t"
      "v_mfma_f32_16x16x32_bf16 v[128:131], a[0:3], v[160:163], v[96:99]\n\t"
      "ds_read_b128 v[180:183], v57 offset:8192\n\t"
      "v_mfma_f32_16x16x32_bf16 v[136:139], a[8:11], v[160:163], v[100:103]\n\t"
      "v_mfma_f32_16x16x32_bf16 v[144:147], a[16:19], v[160:163], v[104:107]\n\t"
      "s_add_u32 s69, s69, 36864\n\t"
      "s_cmp_eq_u32 s69, 110592\n\t"
      "s_cselect_b32 s69, 0, s69\n\t"
      "v_mfma_f32_16x16x32_bf16 v[152:155], a[24:27], v[160:163], v[108:111]\n\t"
      "v_add_u32 v58, s69, %[ra0]\n\t"
      "s_waitcnt lgkmcnt(4)\n\t"
      "v_mfma_f32_16x16x32_bf16 v[128:131], a[4:7], v[164:167], v[128:131]\n\t"
      "ds_read_b128 v[160:163], v56 offset:12288\n\t"
      "v_add_u32 v59, s69, %[ra1]\n\t"
      "v_mfma_f32_16x16x32_bf16 v[136:139], a[12:15], v[164:167], v[136:139]\n\t"
      "v_mfma_f32_16x16x32_bf16 v[144:147], a[20:23], v[164:167], v[144:147]\n\t"
      "s_mov_b32 m0, s70\n\t"
      "s_nop 0\n\t"
      "buffer_load_dwordx4 %[xv], %[xsrd], s71 offen lds\n\t"
      "v_mfma_f32_16x16x32_bf16 v[152:155], a[28:31], v[164:167], v[152:155]\n\t"
      "v_mul_f32 v80, v132, v132\n\t"
      "v_mul_f32 v81, v133, v133\n\t"
      "v_mul_f32 v82, v134, v134\n\t"
      "s_waitcnt lgkmcnt(4)\n\t"
      "v_mfma_f32_16x16x32_bf16 v[128:131], a[32:35], v[168:171], v[128:131]\n\t"
      "ds_read_b128 v[164:167], v57 offset:12288\n\t"
      "v_mul_f32 v83, v135, v135\n\t"
      "v_fma_f32 v80, v80, s75, v60\n\t"
      "v_mfma_f32_16x16x32_bf16 v[136:139], a[40:43], v[168:171], v[136:139]\n\t"
      "v_fma_f32 v81, v81, s75, v60\n\t"
      "v_fma_f32 v82, v82, s75, v60\n\t"
      "v_fma_f32 v83, v83, s75, v60\n\t"
      "v_mfma_f32_16x16x32_bf16 v[144:147], a[48:51], v[168:171], v[144:147]\n\t"
      "s_add_u32 m0, m0, 3968\n\t"
      "s_nop 0\n\t"
      "buffer_load_dwordx4 %[xv], %[xsrd], s71 offen offset:128 lds\n\t"
      "v_mul_f32 v80, v80, v132\n\t"
      "v_mul_f32 v81, v81, v133\n\t"
      "v_mul_f32 v82, v82, v134\n\t"
      "v_mfma_f32_16x16x32_bf16 v[152:155], a[56:59], v[168:171], v[152:155]\n\t"
      "v_mul_f32 v83, v83, v135\n\t"
      "v_exp_f32 v80, v80\n\t"
      "s_waitcnt lgkmcnt(4)\n\t"
      "v_mfma_f32_16x16x32_bf16 v[128:131], a[36:39], v[172:175], v[128:131]\n\t"
      "ds_read_b128 v[168:171], v56 offset:16384\n\t"
      "v_exp_f32 v81, v81\n\t"
      "v_mfma_f32_16x16x32_bf16 v[136:139], a[44:47], v[172:175], v[136:139]\n\t"
      "v_exp_f32 v82, v82\n\t"
      "v_mfma_f32_16x16x32_bf16 v[144:147], a[52:55], v[172:175], v[144:147]\n\t"
      "s_add_u32 m0, m0, 3968\n\t"
      "s_nop 0\n\t"
      "buffer_load_dwordx4 %[xv], %[xsrd], s71 offen offset:256 lds\n\t"
      "v_exp_f32 v83, v83\n\t"
      "v_add_f32 v80, 1.0, v80\n\t"
      "v_mfma_f32_16x16x32_bf16 v[152:155], a[60:63], v[172:175], v[152:155]\n\t"
      "v_add_f32 v81, 1.0, v81\n\t"
      "v_add_f32 v82, 1.0, v82\n\t"
      "v_add_f32 v83, 1.0, v83\n\t"
      "s_waitcnt lgkmcnt(4)\n\t"
      "v_mfma_f32_16x16x32_bf16 v[128:131], a[64:67], v[176:179], v[128:131]\n\t"
      "ds_read_b128 v[172:175], v57 offset:16384\n\t"
      "v_rcp_f32 v80, v80\n\t"
      "v_mfma_f32_16x16x32_bf16 v[136:139], a[72:75], v[176:179], v[136:139]\n\t"
      "v_rcp_f32 v81, v81\n\t"
      "v_mfma_f32_16x16x32_bf16 v[144:147], a[80:83], v[176:179], v[144:147]\n\t"
      "s_add_u32 m0, m0, 3968\n\t"
      "s_nop 0\n\t"
      "buffer_load_dwordx4 %[xv], %[xsrd], s71 offen offset:384 lds\n\t"
      "v_rcp_f32 v82, v82\n\t"
      "v_mfma_f32_16x16x32_bf16 v[152:155], a[88:91], v[176:179], v[152:155]\n\t"
      "v_rcp_f32 v83, v83\n\t"
      "v_mul_f32 v80, v80, v132\n\t"
      "s_waitcnt lgkmcnt(4)\n\t"
      "v_mfma_f32_16x16x32_bf16 v[128:131], a[68:71], v[180:183], v[128:131]\n\t"
      "ds_read_b128 v[176:179], v56 offset:20480\n\t"
      "v_mul_f32 v81, v81, v133\n\t"
      "v_mul_f32 v82, v82, v134\n\t"
      "v_mfma_f32_16x16x32_bf16 v[136:139], a[76:79], v[180:183], v[136:139]\n\t"
      "v_mul_f32 v83, v83, v135\n\t"
      "v_cvt_pk_bf16_f32 v72, v80, v81\n\t"
      "v_cvt_pk_bf16_f32 v73, v82, v83\n\t"
      "v_mfma_f32_16x16x32_bf16 v[144:147], a[84:87], v[180:183], v[144:147]\n\t"
      "s_add_u32 m0, m0, 3968\n\t"
      "s_nop 0\n\t"
      "buffer_load_dwordx4 %[xv], %[xsrd], s71 offen offset:512 lds\n\t"
      "v_mul_f32 v80, v140, v140\n\t"
      "v_mul_f32 v81, v141, v141\n\t"
      "v_mul_f32 v82, v142, v142\n\t"
      "v_mfma_f32_16x16x32_bf16 v[152:155], a[92:95], v[180:183], v[152:155]\n\t"
      "v_mul_f32 v83, v143, v143\n\t"
      "v_fma_f32 v80, v80, s75, v60\n\t"
      "v_fma_f32 v81, v81, s75, v60\n\t"
      "s_waitcnt lgkmcnt(4)\n\t"
      "v_mfma_f32_16x16x32_bf16 v[128:131], a[96:99], v[160:163], v[128:131]\n\t"
      "ds_read_b128 v[180:183], v57 offset:20480\n\t"
      "v_fma_f32 v82, v82, s75, v60\n\t"
      "v_fma_f32 v83, v83, s75, v60\n\t"
      "v_mfma_f32_16x16x32_bf16 v[136:139], a[104:107], v[160:163], v[136:139]\n\t"
      "v_mul_f32 v80, v80, v140\n\t"
      "v_mul_f32 v81, v81, v141\n\t"
      "v_mul_f32 v82, v82, v142\n\t"
      "v_mfma_f32_16x16x32_bf16 v[144:147], a[112:115], v[160:163], v[144:147]\n\t"
      "s_add_u32 m0, m0, 3968\n\t"
      "s_nop 0\n\t"
      "buffer_load_dwordx4 %[xv], %[xsrd], s71 offen offset:640 lds\n\t"
      "v_mul_f32 v83, v83, v143\n\t"
      "v_exp_f32 v80, v80\n\t"
      "v_mfma_f32_16x16x32_bf16 v[152:155], a[120:123], v[160:163], v[152:155]\n\t"
      "v_exp_f32 v81, v81\n\t"
      "s_waitcnt lgkmcnt(4)\n\t"
      "v_mfma_f32_16x16x32_bf16 v[128:131], a[100:103], v[164:167], v[128:131]\n\t"
      "ds_read_b128 v[160:163], v56 offset:24576\n\t"
      "v_exp_f32 v82, v82\n\t"
      "v_mfma_f32_16x16x32_bf16 v[136:139], a[108:111], v[164:167], v[136:139]\n\t"
      "v_exp_f32 v83, v83\n\t"
      "v_add_f32 v80, 1.0, v80\n\t"
      "v_mfma_f32_16x16x32_bf16 v[144:147], a[116:119], v[164:167], v[144:147]\n\t"
      "s_add_u32 m0, m0, 3968\n\t"
      "s_nop 0\n\t"
      "buffer_load_dwordx4 %[xv], %[xsrd], s71 offen offset:768 lds\n\t"
      "v_add_f32 v81, 1.0, v81\n\t"
      "v_add_f32 v82, 1.0, v82\n\t"
      "v_add_f32 v83, 1.0, v83\n\t"
      "v_mfma_f32_16x16x32_bf16 v[152:155], a[124:127], v[164:167], v[152:155]\n\t"
      "v_rcp_f32 v80, v80\n\t"
      "s_waitcnt lgkmcnt(4)\n\t"
      "v_mfma_f32_16x16x32_bf16 v[128:131], a[128:131], v[168:171], v[128:131]\n\t"
      "ds_read_b128 v[164:167], v57 offset:24576\n\t"
      "v_rcp_f32 v81, v81\n\t"
      "v_mfma_f32_16x16x32_bf16 v[136:139], a[136:139], v[168:171], v[136:139]\n\t"
      "v_rcp_f32 v82, v82\n\t"
      "v_mfma_f32_16x16x32_bf16 v[144:147], a[144:147], v[168:171], v[144:147]\n\t"
      "s_add_u32 m0, m0, 3968\n\t"
      "s_nop 0\n\t"
      "buffer_load_dwordx4 %[xv], %[xsrd], s71 offen offset:896 lds\n\t"
      "v_rcp_f32 v83, v83\n\t"
      "v_mul_f32 v80, v80, v140\n\t"
      "v_mfma_f32_16x16x32_bf16 v[152:155], a[152:155], v[168:171], v[152:155]\n\t"
      "v_mul_f32 v81, v81, v141\n\t"
      "v_mul_f32 v82, v82, v142\n\t"
      "v_mul_f32 v83, v83, v143\n\t"
      "s_waitcnt lgkmcnt(4)\n\t"
      "v_mfma_f32_16x16x32_bf16 v[128:131], a[132:135], v[172:175], v[128:131]\n\t"
      "ds_read_b128 v[168:171], v56 offset:28672\n\t"
      "v_cvt_pk_bf16_f32 v74, v80, v81\n\t"
      "v_cvt_pk_bf16_f32 v75, v82, v83\n\t"
      "v_mfma_f32_16x16x32_bf16 v[136:139], a[140:143], v[172:175], v[136:139]\n\t"
      "buffer_store_dwordx4 v[72:75], %[l16], %[srd], s78 offen sc1\n\t"
      "v_mul_f32 v80, v148, v148\n\t"
      "v_mul_f32 v81, v149, v149\n\t"
      "v_mfma_f32_16x16x32_bf16 v[144:147], a[148:151], v[172:175], v[144:147]\n\t"
      "s_add_u32 m0, m0, 3968\n\t"
      "s_nop 0\n\t"
      "buffer_load_dwordx4 %[xv], %[xsrd], s71 offen offset:1024 lds\n\t"
      "v_mul_f32 v82, v150, v150\n\t"
      "v_mul_f32 v83, v151, v151\n\t"
      "v_fma_f32 v80, v80, s75, v60\n\t"
      "v_mfma_f32_16x16x32_bf16 v[152:155], a[156:159], v[172:175], v[152:155]\n\t"
      "v_fma_f32 v81, v81, s75, v60\n\t"
      "v_fma_f32 v82, v82, s75, v60\n\t"
      "v_fma_f32 v83, v83, s75, v60\n\t"
      "s_waitcnt lgkmcnt(4)\n\t"
      "v_mfma_f32_16x16x32_bf16 v[128:131], a[160:163], v[176:179], v[128:131]\n\t"
      "ds_read_b128 v[172:175], v57 offset:28672\n\t"
      "v_mul_f32 v80, v80, v148\n\t"
      "v_mul_f32 v81, v81, v149\n\t"
      "v_mfma_f32_16x16x32_bf16 v[136:139], a[168:171], v[176:179], v[136:139]\n\t"
      "v_mul_f32 v82, v82, v150\n\t"
      "v_mul_f32 v83, v83, v151\n\t"
      "v_mfma_f32_16x16x32_bf16 v[144:147], a[176:179], v[176:179], v[144:147]\n\t"
      "v_exp_f32 v80, v80\n\t"
      "v_mfma_f32_16x16x32_bf16 v[152:155], a[184:187], v[176:179], v[152:155]\n\t"
      "v_exp_f32 v81, v81\n\t"
      "s_waitcnt lgkmcnt(4)\n\t"
      "v_mfma_f32_16x16x32_bf16 v[128:131], a[164:167], v[180:183], v[128:131]\n\t"
      "ds_read_b128 v[176:179], v56 offset:32768\n\t"
      "v_exp_f32 v82, v82\n\t"
      "v_mfma_f32_16x16x32_bf16 v[136:139], a[172:175], v[180:183], v[136:139]\n\t"
      "s_mul_i32 s72, s68, 294912\n\t"
      "s_add_u32 s72, s72, %[ob]\n\t"
      "v_exp_f32 v83, v83\n\t"
      "v_add_f32 v80, 1.0, v80\n\t"
      "v_mfma_f32_16x16x32_bf16 v[144:147], a[180:183], v[180:183], v[144:147]\n\t"
      "v_add_f32 v81, 1.0, v81\n\t"
      "v_add_f32 v82, 1.0, v82\n\t"
      "v_add_f32 v83, 1.0, v83\n\t"
      "v_mfma_f32_16x16x32_bf16 v[152:155], a[188:191], v[180:183], v[152:155]\n\t"
      "v_rcp_f32 v80, v80\n\t"
      "s_waitcnt lgkmcnt(4)\n\t"
      "v_mfma_f32_16x16x32_bf16 v[128:131], a[192:195], v[160:163], v[128:131]\n\t"
      "ds_read_b128 v[180:183], v57 offset:32768\n\t"
      "v_rcp_f32 v81, v81\n\t"
      "v_mfma_f32_16x16x32_bf16 v[136:139], a[200:203], v[160:163], v[136:139]\n\t"
      "s_add_u32 s76, s72, 0\n\t"
      "s_add_u32 s77, s72, 4096\n\t"
      "v_rcp_f32 v82, v82\n\t"
      "v_mfma_f32_16x16x32_bf16 v[144:147], a[208:211], v[160:163], v[144:147]\n\t"
      "v_rcp_f32 v83, v83\n\t"
      "v_mul_f32 v80, v80, v148\n\t"
      "v_mfma_f32_16x16x32_bf16 v[152:155], a[216:219], v[160:163], v[152:155]\n\t"
      "v_mul_f32 v81, v81, v149\n\t"
      "v_mul_f32 v82, v82, v150\n\t"
      "v_mul_f32 v83, v83, v151\n\t"
      "s_waitcnt lgkmcnt(4)\n\t"
      "v_mfma_f32_16x16x32_bf16 v[128:131], a[196:199], v[164:167], v[128:131]\n\t"
      "ds_read_b128 v[160:163], v56 offset:2048\n\t"
      "v_cvt_pk_bf16_f32 v76, v80, v81\n\t"
      "v_cvt_pk_bf16_f32 v77, v82, v83\n\t"
      "v_mfma_f32_16x16x32_bf16 v[136:139], a[204:207], v[164:167], v[136:139]\n\t"
      "v_mul_f32 v80, v156, v156\n\t"
      "v_mul_f32 v81, v157, v157\n\t"
      "v_mul_f32 v82, v158, v158\n\t"
      "v_mfma_f32_16x16x32_bf16 v[144:147], a[212:215], v[164:167], v[144:147]\n\t"
      "v_mul_f32 v83, v159, v159\n\t"
      "v_fma_f32 v80, v80, s75, v60\n\t"
      "v_fma_f32 v81, v81, s75, v60\n\t"
      "v_mfma_f32_16x16x32_bf16 v[152:155], a[220:223], v[164:167], v[152:155]\n\t"
      "v_fma_f32 v82, v82, s75, v60\n\t"
      "v_fma_f32 v83, v83, s75, v60\n\t"
      "v_mul_f32 v80, v80, v156\n\t"
      "s_waitcnt lgkmcnt(4)\n\t"
      "v_mfma_f32_16x16x32_bf16 v[128:131], a[224:227], v[168:171], v[128:131]\n\t"
      "ds_read_b128 v[164:167], v57 offset:2048\n\t"
      "v_mul_f32 v81, v81, v157\n\t"
      "v_mul_f32 v82, v82, v158\n\t"
      "v_mfma_f32_16x16x32_bf16 v[136:139], a[232:235], v[168:171], v[136:139]\n\t"
      "v_mul_f32 v83, v83, v159\n\t"
      "v_exp_f32 v80, v80\n\t"
      "v_mfma_f32_16x16x32_bf16 v[144:147], a[240:243], v[168:171], v[144:147]\n\t"
      "v_exp_f32 v81, v81\n\t"
      "v_mfma_f32_16x16x32_bf16 v[152:155], a[248:251], v[168:171], v[152:155]\n\t"
      "v_exp_f32 v82, v82\n\t"
      "s_waitcnt lgkmcnt(4)\n\t"
      "v_mfma_f32_16x16x32_bf16 v[128:131], a[228:231], v[172:175], v[128:131]\n\t"
      "ds_read_b128 v[168:171], v56 offset:6144\n\t"
      "v_exp_f32 v83, v83\n\t"
      "v_mfma_f32_16x16x32_bf16 v[136:139], a[236:239], v[172:175], v[136:139]\n\t"
      "v_add_f32 v80, 1.0, v80\n\t"
      "v_add_f32 v81, 1.0, v81\n\t"
      "v_add_f32 v82, 1.0, v82\n\t"
      "v_mfma_f32_16x16x32_bf16 v[144:147], a[244:247], v[172:175], v[144:147]\n\t"
      "v_add_f32 v83, 1.0, v83\n\t"
      "v_rcp_f32 v80, v80\n\t"
      "v_mfma_f32_16x16x32_bf16 v[152:155], a[252:255], v[172:175], v[152:155]\n\t"
      "v_rcp_f32 v81, v81\n\t"
      "s_waitcnt lgkmcnt(4)\n\t"
      "v_mfma_f32_16x16x32_bf16 v[128:131], v[224:227], v[176:179], v[128:131]\n\t"
      "ds_read_b128 v[172:175], v57 offset:6144\n\t"
      "v_rcp_f32 v82, v82\n\t"
      "v_mfma_f32_16x16x32_bf16 v[136:139], v[232:235], v[176:179], v[136:139]\n\t"
      "v_rcp_f32 v83, v83\n\t"
      "v_mul_f32 v80, v80, v156\n\t"
      "v_mfma_f32_16x16x32_bf16 v[144:147], v[240:243], v[176:179], v[144:147]\n\t"
      "v_mul_f32 v81, v81, v157\n\t"
      "v_mul_f32 v82, v82, v158\n\t"
      "v_mul_f32 v83, v83, v159\n\t"
      "v_mfma_f32_16x16x32_bf16 v[152:155], v[248:251], v[176:179], v[152:155]\n\t"
      "v_cvt_pk_bf16_f32 v78, v80, v81\n\t"
      "v_cvt_pk_bf16_f32 v79, v82, v83\n\t"
      "buffer_store_dwordx4 v[76:79], %[l16], %[srd], s79 offen sc1\n\t"
      "s_waitcnt lgkmcnt(4)\n\t"
      "v_mfma_f32_16x16x32_bf16 v[128:131], v[228:231], v[180:183], v[128:131]\n\t"
      "ds_read_b128 v[176:179], v56 offset:10240\n\t"
      "v_mfma_f32_16x16x32_bf16 v[136:139], v[236:239], v[180:183], v[136:139]\n\t"
      "s_add_u32 s70, s70, 36864\n\t"
      "s_sub_u32 s74, s70, 110592\n\t"
      "s_cselect_b32 s70, s70, s74\n\t"
      "v_mfma_f32_16x16x32_bf16 v[144:147], v[244:247], v[180:183], v[144:147]\n\t"
      "s_add_u32 s71, s71, 36864\n\t"
      "v_mfma_f32_16x16x32_bf16 v[152:155], v[252:255], v[180:183], v[152:155]\n\t"
      "s_waitcnt lgkmcnt(4)\n\t"
      "v_mfma_f32_16x16x32_bf16 v[132:135], a[0:3], v[160:163], v[96:99]\n\t"
      "ds_read_b128 v[180:183], v57 offset:10240\n\t"
      "v_mfma_f32_16x16x32_bf16 v[140:143], a[8:11], v[160:163], v[100:103]\n\t"
      "s_add_u32 s78, s72, 147456\n\t"
      "s_add_u32 s79, s72, 151552\n\t"
      "v_mfma_f32_16x16x32_bf16 v[148:151], a[16:19], v[160:163], v[104:107]\n\t"
      "v_mfma_f32_16x16x32_bf16 v[156:159], a[24:27], v[160:163], v[108:111]\n\t"
      "s_waitcnt lgkmcnt(4)\n\t"
      "v_mfma_f32_16x16x32_bf16 v[132:135], a[4:7], v[164:167], v[132:135]\n\t"
      "ds_read_b128 v[160:163], v56 offset:14336\n\t"
      "v_mfma_f32_16x16x32_bf16 v[140:143], a[12:15], v[164:167], v[140:143]\n\t"
      "v_mov_b32 v61, s68\n\t"
      "v_mfma_f32_16x16x32_bf16 v[148:151], a[20:23], v[164:167], v[148:151]\n\t"
      "v_mfma_f32_16x16x32_bf16 v[156:159], a[28:31], v[164:167], v[156:159]\n\t"
      "v_mul_f32 v80, v128, v128\n\t"
      "v_mul_f32 v81, v129, v129\n\t"
      "v_mul_f32 v82, v130, v130\n\t"
      "s_waitcnt lgkmcnt(4)\n\t"
      "v_mfma_f32_16x16x32_bf16 v[132:135], a[32:35], v[168:171], v[132:135]\n\t"
      "ds_read_b128 v[164:167], v57 offset:14336\n\t"
      "v_mul_f32 v83, v131, v131\n\t"
      "v_fma_f32 v80, v80, s75, v60\n\t"
      "v_mfma_f32_16x16x32_bf16 v[140:143], a[40:43], v[168:171], v[140:143]\n\t"
      "v_fma_f32 v81, v81, s75, v60\n\t"
      "v_fma_f32 v82, v82, s75, v60\n\t"
      "v_fma_f32 v83, v83, s75, v60\n\t"
      "v_mfma_f32_16x16x32_bf16 v[148:151], a[48:51], v[168:171], v[148:151]\n\t"
      "v_mul_f32 v80, v80, v128\n\t"
      "v_mul_f32 v81, v81, v129\n\t"
      "v_mul_f32 v82, v82, v130\n\t"
      "v_mfma_f32_16x16x32_bf16 v[156:159], a[56:59], v[168:171], v[156:159]\n\t"
      "v_mul_f32 v83, v83, v131\n\t"
      "v_exp_f32 v80, v80\n\t"
      "s_waitcnt lgkmcnt(4)\n\t"
      "v_mfma_f32_16x16x32_bf16 v[132:135], a[36:39], v[172:175], v[132:135]\n\t"
      "ds_read_b128 v[168:171], v56 offset:18432\n\t"
      "v_exp_f32 v81, v81\n\t"
      "v_mfma_f32_16x16x32_bf16 v[140:143], a[44:47], v[172:175], v[140:143]\n\t"
      "v_exp_f32 v82, v82\n\t"
      "v_mfma_f32_16x16x32_bf16 v[148:151], a[52:55], v[172:175], v[148:151]\n\t"
      "v_exp_f32 v83, v83\n\t"
      "v_add_f32 v80, 1.0, v80\n\t"
      "v_mfma_f32_16x16x32_bf16 v[156:159], a[60:63], v[172:175], v[156:159]\n\t"
      "v_add_f32 v81, 1.0, v81\n\t"
      "v_add_f32 v82, 1.0, v82\n\t"
      "v_add_f32 v83, 1.0, v83\n\t"
      "s_waitcnt lgkmcnt(4)\n\t"
      "v_mfma_f32_16x16x32_bf16 v[132:135], a[64:67], v[176:179], v[132:135]\n\t"
      "ds_read_b128 v[172:175], v57 offset:18432\n\t"
      "v_rcp_f32 v80, v80\n\t"
      "v_mfma_f32_16x16x32_bf16 v[140:143], a[72:75], v[176:179], v[140:143]\n\t"
      "v_rcp_f32 v81, v81\n\t"
      "v_mfma_f32_16x16x32_bf16 v[148:151], a[80:83], v[176:179], v[148:151]\n\t"
      "v_rcp_f32 v82, v82\n\t"
      "v_mfma_f32_16x16x32_bf16 v[156:159], a[88:91], v[176:179], v[156:159]\n\t"
      "v_rcp_f32 v83, v83\n\t"
      "v_mul_f32 v80, v80, v128\n\t"
      "s_waitcnt lgkmcnt(4)\n\t"
      "v_mfma_f32_16x16x32_bf16 v[132:135], a[68:71], v[180:183], v[132:135]\n\t"
      "ds_read_b128 v[176:179], v56 offset:22528\n\t"
      "v_mul_f32 v81, v81, v129\n\t"
      "v_mul_f32 v82, v82, v130\n\t"
      "v_mfma_f32_16x16x32_bf16 v[140:143], a[76:79], v[180:183], v[140:143]\n\t"
      "v_mul_f32 v83, v83, v131\n\t"
      "v_cvt_pk_bf16_f32 v64, v80, v81\n\t"
      "v_cvt_pk_bf16_f32 v65, v82, v83\n\t"
      "v_mfma_f32_16x16x32_bf16 v[148:151], a[84:87], v[180:183], v[148:151]\n\t"
      "v_mul_f32 v80, v136, v136\n\t"
      "v_mul_f32 v81, v137, v137\n\t"
      "v_mul_f32 v82, v138, v138\n\t"
      "v_mfma_f32_16x16x32_bf16 v[156:159], a[92:95], v[180:183], v[156:159]\n\t"
      "v_mul_f32 v83, v139, v139\n\t"
      "v_fma_f32 v80, v80, s75, v60\n\t"
      "v_fma_f32 v81, v81, s75, v60\n\t"
      "s_waitcnt lgkmcnt(4)\n\t"
      "v_mfma_f32_16x16x32_bf16 v[132:135], a[96:99], v[160:163], v[132:135]\n\t"
      "ds_read_b128 v[180:183], v57 offset:22528\n\t"
      "v_fma_f32 v82, v82, s75, v60\n\t"
      "v_fma_f32 v83, v83, s75, v60\n\t"
      "v_mfma_f32_16x16x32_bf16 v[140:143], a[104:107], v[160:163], v[140:143]\n\t"
      "v_mul_f32 v80, v80, v136\n\t"
      "v_mul_f32 v81, v81, v137\n\t"
      "v_mul_f32 v82, v82, v138\n\t"
      "v_mfma_f32_16x16x32_bf16 v[148:151], a[112:115], v[160:163], v[148:151]\n\t"
      "v_mul_f32 v83, v83, v139\n\t"
      "v_exp_f32 v80, v80\n\t"
      "v_mfma_f32_16x16x32_bf16 v[156:159], a[120:123], v[160:163], v[156:159]\n\t"
      "v_exp_f32 v81, v81\n\t"
      "s_waitcnt lgkmcnt(4)\n\t"
      "v_mfma_f32_16x16x32_bf16 v[132:135], a[100:103], v[164:167], v[132:135]\n\t"
      "ds_read_b128 v[160:163], v56 offset:26624\n\t"
      "v_exp_f32 v82, v82\n\t"
      "v_mfma_f32_16x16x32_bf16 v[140:143], a[108:111], v[164:167], v[140:143]\n\t"
      "v_exp_f32 v83, v83\n\t"
      "v_add_f32 v80, 1.0, v80\n\t"
      "v_mfma_f32_16x16x32_bf16 v[148:151], a[116:119], v[164:167], v[148:151]\n\t"
      "v_add_f32 v81, 1.0, v81\n\t"
      "v_add_f32 v82, 1.0, v82\n\t"
      "v_add_f32 v83, 1.0, v83\n\t"
      "v_mfma_f32_16x16x32_bf16 v[156:159], a[124:127], v[164:167], v[156:159]\n\t"
      "v_rcp_f32 v80, v80\n\t"
      "s_waitcnt lgkmcnt(4)\n\t"
      "v_mfma_f32_16x16x32_bf16 v[132:135], a[128:131], v[168:171], v[132:135]\n\t"
      "ds_read_b128 v[164:167], v57 offset:26624\n\t"
      "v_rcp_f32 v81, v81\n\t"
      "v_mfma_f32_16x16x32_bf16 v[140:143], a[136:139], v[168:171], v[140:143]\n\t"
      "v_rcp_f32 v82, v82\n\t"
      "v_mfma_f32_16x16x32_bf16 v[148:151], a[144:147], v[168:171], v[148:151]\n\t"
      "v_rcp_f32 v83, v83\n\t"
      "v_mul_f32 v80, v80, v136\n\t"
      "v_mfma_f32_16x16x32_bf16 v[156:159], a[152:155], v[168:171], v[156:159]\n\t"
      "v_mul_f32 v81, v81, v137\n\t"
      "v_mul_f32 v82, v82, v138\n\t"
      "v_mul_f32 v83, v83, v139\n\t"
      "s_waitcnt lgkmcnt(4)\n\t"
      "v_mfma_f32_16x16x32_bf16 v[132:135], a[132:135], v[172:175], v[132:135]\n\t"
      "ds_read_b128 v[168:171], v56 offset:30720\n\t"
      "v_cvt_pk_bf16_f32 v66, v80, v81\n\t"
      "v_cvt_pk_bf16_f32 v67, v82, v83\n\t"
      "v_mfma_f32_16x16x32_bf16 v[140:143], a[140:143], v[172:175], v[140:143]\n\t"
      "buffer_store_dwordx4 v[64:67], %[l16], %[srd], s76 offen sc1\n\t"
      "v_mul_f32 v80, v144, v144\n\t"
      "v_mul_f32 v81, v145, v145\n\t"
      "v_mfma_f32_16x16x32_bf16 v[148:151], a[148:151], v[172:175], v[148:151]\n\t"
      "v_mul_f32 v82, v146, v146\n\t"
      "v_mul_f32 v83, v147, v147\n\t"
      "v_fma_f32 v80, v80, s75, v60\n\t"
      "v_mfma_f32_16x16x32_bf16 v[156:159], a[156:159], v[172:175], v[156:159]\n\t"
      "v_fma_f32 v81, v81, s75, v60\n\t"
      "v_fma_f32 v82, v82, s75, v60\n\t"
      "v_fma_f32 v83, v83, s75, v60\n\t"
      "s_waitcnt lgkmcnt(4)\n\t"
      "v_mfma_f32_16x16x32_bf16 v[132:135], a[160:163], v[176:179], v[132:135]\n\t"
      "ds_read_b128 v[172:175], v57 offset:30720\n\t"
      "v_mul_f32 v80, v80, v144\n\t"
      "v_mul_f32 v81, v81, v145\n\t"
      "v_mfma_f32_16x16x32_bf16 v[140:143], a[168:171], v[176:179], v[140:143]\n\t"
      "v_mul_f32 v82, v82, v146\n\t"
      "v_mul_f32 v83, v83, v147\n\t"
      "v_mfma_f32_16x16x32_bf16 v[148:151], a[176:179], v[176:179], v[148:151]\n\t"
      "v_exp_f32 v80, v80\n\t"
      "v_mfma_f32_16x16x32_bf16 v[156:159], a[184:187], v[176:179], v[156:159]\n\t"
      "v_exp_f32 v81, v81\n\t"
      "s_waitcnt lgkmcnt(4)\n\t"
      "v_mfma_f32_16x16x32_bf16 v[132:135], a[164:167], v[180:183], v[132:135]\n\t"
      "ds_read_b128 v[176:179], v56 offset:34816\n\t"
      "v_exp_f32 v82, v82\n\t"
      "v_mfma_f32_16x16x32_bf16 v[140:143], a[172:175], v[180:183], v[140:143]\n\t"
      "v_exp_f32 v83, v83\n\t"
      "v_add_f32 v80, 1.0, v80\n\t"
      "v_mfma_f32_16x16x32_bf16 v[148:151], a[180:183], v[180:183], v[148:151]\n\t"
      "s_waitcnt vmcnt(1)\n\t"
      "s_barrier\n\t"
      "buffer_store_dword v61, %[fo], %[srd], 0 offen sc1\n\t"
      "v_add_f32 v81, 1.0, v81\n\t"
      "v_add_f32 v82, 1.0, v82\n\t"
      "v_add_f32 v83, 1.0, v83\n\t"
      "v_mfma_f32_16x16x32_bf16 v[156:159], a[188:191], v[180:183], v[156:159]\n\t"
      "v_rcp_f32 v80, v80\n\t"
      "s_waitcnt lgkmcnt(4)\n\t"
      "v_mfma_f32_16x16x32_bf16 v[132:135], a[192:195], v[160:163], v[132:135]\n\t"
      "ds_read_b128 v[180:183], v57 offset:34816\n\t"
      "v_rcp_f32 v81, v81\n\t"
      "v_mfma_f32_16x16x32_bf16 v[140:143], a[200:203], v[160:163], v[140:143]\n\t"
      "v_rcp_f32 v82, v82\n\t"
      "v_mov_b32 v56, v58\n\t"
      "v_mfma_f32_16x16x32_bf16 v[148:151], a[208:211], v[160:163], v[148:151]\n\t"
      "v_rcp_f32 v83, v83\n\t"
      "v_mul_f32 v80, v80, v144\n\t"
      "v_mov_b32 v57, v59\n\t"
      "v_mfma_f32_16x16x32_bf16 v[156:159], a[216:219], v[160:163], v[156:159]\n\t"
      "v_mul_f32 v81, v81, v145\n\t"
      "v_mul_f32 v82, v82, v146\n\t"
      "v_mul_f32 v83, v83, v147\n\t"
      "s_waitcnt lgkmcnt(4)\n\t"
      "v_mfma_f32_16x16x32_bf16 v[132:135], a[196:199], v[164:167], v[132:135]\n\t"
      "ds_read_b128 v[160:163], v56 offset:0\n\t"
      "v_cvt_pk_bf16_f32 v68, v80, v81\n\t"
      "v_cvt_pk_bf16_f32 v69, v82, v83\n\t"
      "v_mfma_f32_16x16x32_bf16 v[140:143], a[204:207], v[164:167], v[140:143]\n\t"
      "v_mul_f32 v80, v152, v152\n\t"
      "v_mul_f32 v81, v153, v153\n\t"
      "v_mul_f32 v82, v154, v154\n\t"
      "v_mfma_f32_16x16x32_bf16 v[148:151], a[212:215], v[164:167], v[148:151]\n\t"
      "v_mul_f32 v83, v155, v155\n\t"
      "v_fma_f32 v80, v80, s75, v60\n\t"
      "v_fma_f32 v81, v81, s75, v60\n\t"
      "v_mfma_f32_16x16x32_bf16 v[156:159], a[220:223], v[164:167], v[156:159]\n\t"
      "v_fma_f32 v82, v82, s75, v60\n\t"
      "v_fma_f32 v83, v83, s75, v60\n\t"
      "v_mul_f32 v80, v80, v152\n\t"
      "s_waitcnt lgkmcnt(4)\n\t"
      "v_mfma_f32_16x16x32_bf16 v[132:135], a[224:227], v[168:171], v[132:135]\n\t"
      "ds_read_b128 v[164:167], v57 offset:0\n\t"
      "v_mul_f32 v81, v81, v153\n\t"
      "v_mul_f32 v82, v82, v154\n\t"
      "v_mfma_f32_16x16x32_bf16 v[140:143], a[232:235], v[168:171], v[140:143]\n\t"
      "v_mul_f32 v83, v83, v155\n\t"
      "v_exp_f32 v80, v80\n\t"
      "v_mfma_f32_16x16x32_bf16 v[148:151], a[240:243], v[168:171], v[148:151]\n\t"
      "v_exp_f32 v81, v81\n\t"
      "v_mfma_f32_16x16x32_bf16 v[156:159], a[248:251], v[168:171], v[156:159]\n\t"
      "v_exp_f32 v82, v82\n\t"
      "s_waitcnt lgkmcnt(4)\n\t"
      "v_mfma_f32_16x16x32_bf16 v[132:135], a[228:231], v[172:175], v[132:135]\n\t"
      "ds_read_b128 v[168:171], v56 offset:4096\n\t"
      "v_exp_f32 v83, v83\n\t"
      "v_mfma_f32_16x16x32_bf16 v[140:143], a[236:239], v[172:175], v[140:143]\n\t"
      "v_add_f32 v80, 1.0, v80\n\t"
      "v_add_f32 v81, 1.0, v81\n\t"
      "v_add_f32 v82, 1.0, v82\n\t"
      "v_mfma_f32_16x16x32_bf16 v[148:151], a[244:247], v[172:175], v[148:151]\n\t"
      "v_add_f32 v83, 1.0, v83\n\t"
      "v_rcp_f32 v80, v80\n\t"
      "v_mfma_f32_16x16x32_bf16 v[156:159], a[252:255], v[172:175], v[156:159]\n\t"
      "v_rcp_f32 v81, v81\n\t"
      "s_waitcnt lgkmcnt(4)\n\t"
      "v_mfma_f32_16x16x32_bf16 v[132:135], v[224:227], v[176:179], v[132:135]\n\t"
      "ds_read_b128 v[172:175], v57 offset:4096\n\t"
      "v_rcp_f32 v82, v82\n\t"
      "v_mfma_f32_16x16x32_bf16 v[140:143], v[232:235], v[176:179], v[140:143]\n\t"
      "v_rcp_f32 v83, v83\n\t"
      "v_mul_f32 v80, v80, v152\n\t"
      "v_mfma_f32_16x16x32_bf16 v[148:151], v[240:243], v[176:179], v[148:151]\n\t"
      "v_mul_f32 v81, v81, v153\n\t"
      "v_mul_f32 v82, v82, v154\n\t"
      "v_mul_f32 v83, v83, v155\n\t"
      "v_mfma_f32_16x16x32_bf16 v[156:159], v[248:251], v[176:179], v[156:159]\n\t"
      "v_cvt_pk_bf16_f32 v70, v80, v81\n\t"
      "v_cvt_pk_bf16_f32 v71, v82, v83\n\t"
      "buffer_store_dwordx4 v[68:71], %[l16], %[srd], s77 offen sc1\n\t"
      "s_waitcnt lgkmcnt(4)\n\t"
      "v_mfma_f32_16x16x32_bf16 v[132:135], v[228:231], v[180:183], v[132:135]\n\t"
      "ds_read_b128 v[176:179], v56 offset:8192\n\t"
      "v_mfma_f32_16x16x32_bf16 v[140:143], v[236:239], v[180:183], v[140:143]\n\t"
      "v_mfma_f32_16x16x32_bf16 v[148:151], v[244:247], v[180:183], v[148:151]\n\t"
      "v_mfma_f32_16x16x32_bf16 v[156:159], v[252:255], v[180:183], v[156:159]\n\t"
      "s_add_u32 s68, s68, 1\n\t"
      "s_cmp_lt_u32 s68, 64\n\t"
      "s_cbranch_scc1 10b\n\t"
      "s_nop 7\n\t"
      "v_mul_f32 v80, v132, v132\n\t"
      "v_mul_f32 v81, v133, v133\n\t"
      "v_mul_f32 v82, v134, v134\n\t"
      "v_mul_f32 v83, v135, v135\n\t"
      "v_fma_f32 v80, v80, s75, v60\n\t"
      "v_fma_f32 v81, v81, s75, v60\n\t"
      "v_fma_f32 v82, v82, s75, v60\n\t"
      "v_fma_f32 v83, v83, s75, v60\n\t"
      "v_mul_f32 v80, v80, v132\n\t"
      "v_mul_f32 v81, v81, v133\n\t"
      "v_mul_f32 v82, v82, v134\n\t"
      "v_mul_f32 v83, v83, v135\n\t"
      "v_exp_f32 v80, v80\n\t"
      "v_exp_f32 v81, v81\n\t"
      "v_exp_f32 v82, v82\n\t"
      "v_exp_f32 v83, v83\n\t"
      "v_add_f32 v80, 1.0, v80\n\t"
      "v_add_f32 v81, 1.0, v81\n\t"
      "v_add_f32 v82, 1.0, v82\n\t"
      "v_add_f32 v83, 1.0, v83\n\t"
      "v_rcp_f32 v80, v80\n\t"
      "v_rcp_f32 v81, v81\n\t"
      "v_rcp_f32 v82, v82\n\t"
      "v_rcp_f32 v83, v83\n\t"
      "v_mul_f32 v80, v80, v132\n\t"
      "v_mul_f32 v81, v81, v133\n\t"
      "v_mul_f32 v82, v82, v134\n\t"
      "v_mul_f32 v83, v83, v135\n\t"
      "v_cvt_pk_bf16_f32 v72, v80, v81\n\t"
      "v_cvt_pk_bf16_f32 v73, v82, v83\n\t"
      "v_mul_f32 v80, v140, v140\n\t"
      "v_mul_f32 v81, v141, v141\n\t"
      "v_mul_f32 v82, v142, v142\n\t"
      "v_mul_f32 v83, v143, v143\n\t"
      "v_fma_f32 v80, v80, s75, v60\n\t"
      "v_fma_f32 v81, v81, s75, v60\n\t"
      "v_fma_f32 v82, v82, s75, v60\n\t"
      "v_fma_f32 v83, v83, s75, v60\n\t"
      "v_mul_f32 v80, v80, v140\n\t"
      "v_mul_f32 v81, v81, v141\n\t"
      "v_mul_f32 v82, v82, v142\n\t"
      "v_mul_f32 v83, v83, v143\n\t"
      "v_exp_f32 v80, v80\n\t"
      "v_exp_f32 v81, v81\n\t"
      "v_exp_f32 v82, v82\n\t"
      "v_exp_f32 v83, v83\n\t"
      "v_add_f32 v80, 1.0, v80\n\t"
      "v_add_f32 v81, 1.0, v81\n\t"
      "v_add_f32 v82, 1.0, v82\n\t"
      "v_add_f32 v83, 1.0, v83\n\t"
      "v_rcp_f32 v80, v80\n\t"
      "v_rcp_f32 v81, v81\n\t"
      "v_rcp_f32 v82, v82\n\t"
      "v_rcp_f32 v83, v83\n\t"
      "v_mul_f32 v80, v80, v140\n\t"
      "v_mul_f32 v81, v81, v141\n\t"
      "v_mul_f32 v82, v82, v142\n\t"
      "v_mul_f32 v83, v83, v143\n\t"
      "v_cvt_pk_bf16_f32 v74, v80, v81\n\t"
      "v_cvt_pk_bf16_f32 v75, v82, v83\n\t"
      "buffer_store_dwordx4 v[72:75], %[l16], %[srd], s78 offen sc1\n\t"
      "v_mul_f32 v80, v148, v148\n\t"
      "v_mul_f32 v81, v149, v149\n\t"
      "v_mul_f32 v82, v150, v150\n\t"
      "v_mul_f32 v83, v151, v151\n\t"
      "v_fma_f32 v80, v80, s75, v60\n\t"
      "v_fma_f32 v81, v81, s75, v60\n\t"
      "v_fma_f32 v82, v82, s75, v60\n\t"
      "v_fma_f32 v83, v83, s75, v60\n\t"
      "v_mul_f32 v80, v80, v148\n\t"
      "v_mul_f32 v81, v81, v149\n\t"
      "v_mul_f32 v82, v82, v150\n\t"
      "v_mul_f32 v83, v83, v151\n\t"
      "v_exp_f32 v80, v80\n\t"
      "v_exp_f32 v81, v81\n\t"
      "v_exp_f32 v82, v82\n\t"
      "v_exp_f32 v83, v83\n\t"
      "v_add_f32 v80, 1.0, v80\n\t"
      "v_add_f32 v81, 1.0, v81\n\t"
      "v_add_f32 v82, 1.0, v82\n\t"
      "v_add_f32 v83, 1.0, v83\n\t"
      "v_rcp_f32 v80, v80\n\t"
      "v_rcp_f32 v81, v81\n\t"
      "v_rcp_f32 v82, v82\n\t"
      "v_rcp_f32 v83, v83\n\t"
      "v_mul_f32 v80, v80, v148\n\t"
      "v_mul_f32 v81, v81, v149\n\t"
      "v_mul_f32 v82, v82, v150\n\t"
      "v_mul_f32 v83, v83, v151\n\t"
      "v_cvt_pk_bf16_f32 v76, v80, v81\n\t"
      "v_cvt_pk_bf16_f32 v77, v82, v83\n\t"
      "v_mul_f32 v80, v156, v156\n\t"
      "v_mul_f32 v81, v157, v157\n\t"
      "v_mul_f32 v82, v158, v158\n\t"
      "v_mul_f32 v83, v159, v159\n\t"
      "v_fma_f32 v80, v80, s75, v60\n\t"
      "v_fma_f32 v81, v81, s75, v60\n\t"
      "v_fma_f32 v82, v82, s75, v60\n\t"
      "v_fma_f32 v83, v83, s75, v60\n\t"
      "v_mul_f32 v80, v80, v156\n\t"
      "v_mul_f32 v81, v81, v157\n\t"
      "v_mul_f32 v82, v82, v158\n\t"
      "v_mul_f32 v83, v83, v159\n\t"
      "v_exp_f32 v80, v80\n\t"
      "v_exp_f32 v81, v81\n\t"
      "v_exp_f32 v82, v82\n\t"
      "v_exp_f32 v83, v83\n\t"
      "v_add_f32 v80, 1.0, v80\n\t"
      "v_add_f32 v81, 1.0, v81\n\t"
      "v_add_f32 v82, 1.0, v82\n\t"
      "v_add_f32 v83, 1.0, v83\n\t"
      "v_rcp_f32 v80, v80\n\t"
      "v_rcp_f32 v81, v81\n\t"
      "v_rcp_f32 v82, v82\n\t"
      "v_rcp_f32 v83, v83\n\t"
      "v_mul_f32 v80, v80, v156\n\t"
      "v_mul_f32 v81, v81, v157\n\t"
      "v_mul_f32 v82, v82, v158\n\t"
      "v_mul_f32 v83, v83, v159\n\t"
      "v_cvt_pk_bf16_f32 v78, v80, v81\n\t"
      "v_cvt_pk_bf16_f32 v79, v82, v83\n\t"
      "buffer_store_dwordx4 v[76:79], %[l16], %[srd], s79 offen sc1\n\t"
      "s_mov_b32 m0, s73\n\t"
      "s_waitcnt vmcnt(0) lgkmcnt(0)\n\t"
      :: [ra0] "v"(ra0), [ra1] "v"(ra1), [wl] "s"(US(wl)), [ob] "s"(US(ob)), [xv] "v"(xv), [xsrd] "s"(xsrd), [fo] "v"(fo), [srd] "s"(srd), [l16] "v"(l16), [s0] "s"(s0), "{v[96:111]}"(ci), WPIN(W)
      : "v56", "v57", "v58", "v59", "v60", "v61", "v62", "v63", "v64", "v65", "v66", "v67", "v68", "v69", "v70", "v71", "v72", "v73", "v74", "v75", "v76", "v77", "v78", "v79", "v80", "v81", "v82", "v83", "v84", "v85", "v86", "v87", "v88", "v89", "v90", "v91", "v92", "v93", "v94", "v95", "v128", "v129", "v130", "v131", "v132", "v133", "v134", "v135", "v136", "v137", "v138", "v139", "v140", "v141", "v142", "v143", "v144", "v145", "v146", "v147", "v148", "v149", "v150", "v151", "v152", "v153", "v154", "v155", "v156", "v157", "v158", "v159", "v160", "v161", "v162", "v163", "v164", "v165", "v166", "v167", "v168", "v169", "v170", "v171", "v172", "v173", "v174", "v175", "v176", "v177", "v178", "v179", "v180", "v181", "v182", "v183", "v184", "v185", "v186", "v187", "v188", "v189", "v190", "v191", "v192", "v193", "v194", "v195", "s64", "s65", "s66", "s67", "s68", "s69", "s70", "s71", "s72", "s73", "s74", "s75", "s76", "s77", "s78", "s79", "s80", "s81", "s82", "s83", "s84", "s85", "s86", "s87", "s88", "s89", "s90", "s91", "s92", "s93", "s94", "s95", "scc", "memory");
  } else {
    const u32 ob = (u32)id * 32768u + wl;
    wait_vm<0>(); bar();
    LOOPSTART
    asm volatile(
      "s_mov_b32 s73, m0\n\t"
      "s_mov_b32 s68, 0\n\t"
      "s_mov_b32 s69, 36864\n\t"
      "s_add_u32 s70, %[wl], 73728\n\t"
      "s_mov_b32 s71, 73728\n\t"
      "v_mov_b32 v62, 0\n\t"
      "v_mov_b32 v56, %[ra0]\n\t"
      "v_mov_b32 v57, %[ra1]\n\t"
      "v_add_u32 v58, 36864, %[ra0]\n\t"
      "v_add_u32 v59, 36864, %[ra1]\n\t"
      "v_mov_b32 v61, 0\n\t"
      "ds_read_b128 v[160:163], v56 offset:0\n\t"
      "ds_read_b128 v[164:167], v57 offset:0\n\t"
      "ds_read_b128 v[168:171], v56 offset:4096\n\t"
      "ds_read_b128 v[172:175], v57 offset:4096\n\t"
      "ds_read_b128 v[176:179], v56 offset:8192\n\t"
      "s_waitcnt lgkmcnt(0)\n\t"
      "s_mov_b32 s78, 0xf0000000\n\t"
      "s_mov_b32 s79, 0xf0000000\n\t"
      "s_sub_u32 s69, s69, 36864\n\t"
      "s_add_u32 s70, %[wl], 36864\n\t"
      "s_mov_b32 s68, %[s0]\n\t"
      "s_add_u32 s71, s68, 1\n\t"
      "s_mul_i32 s71, s71, 36864\n\t"
      "10:\n\t"
      "s_waitcnt lgkmcnt(4)\n\t"
      "v_mfma_f32_16x16x32_bf16 v[128:131], a[0:3], v[160:163], 0\n\t"
      "ds_read_b128 v[180:183], v57 offset:8192\n\t"
      "v_mfma_f32_16x16x32_bf16 v[136:139], a[8:11], v[160:163], 0\n\t"
      "v_mfma_f32_16x16x32_bf16 v[144:147], a[16:19], v[160:163], 0\n\t"
      "s_add_u32 s69, s69, 36864\n\t"
      "s_cmp_eq_u32 s69, 110592\n\t"
      "s_cselect_b32 s69, 0, s69\n\t"
      "v_mfma_f32_16x16x32_bf16 v[152:155], a[24:27], v[160:163], 0\n\t"
      "v_add_u32 v58, s69, %[ra0]\n\t"
      "s_waitcnt lgkmcnt(4)\n\t"
      "v_mfma_f32_16x16x32_bf16 v[128:131], a[4:7], v[164:167], v[128:131]\n\t"
      "ds_read_b128 v[160:163], v56 offset:12288\n\t"
      "v_add_u32 v59, s69, %[ra1]\n\t"
      "v_mfma_f32_16x16x32_bf16 v[136:139], a[12:15], v[164:167], v[136:139]\n\t"
      "s_lshr_b32 s74, s68, 1\n\t"
      "s_lshr_b32 s72, %[fm], s74\n\t"
      "s_and_b32 s72, s72, 1\n\t"
      "v_mfma_f32_16x16x32_bf16 v[144:147], a[20:23], v[164:167], v[144:147]\n\t"
      "v_mfma_f32_16x16x32_bf16 v[152:155], a[28:31], v[164:167], v[152:155]\n\t"
      "s_waitcnt lgkmcnt(4)\n\t"
      "v_mfma_f32_16x16x32_bf16 v[128:131], a[32:35], v[168:171], v[128:131]\n\t"
      "ds_read_b128 v[164:167], v57 offset:12288\n\t"
      "v_mfma_f32_16x16x32_bf16 v[136:139], a[40:43], v[168:171], v[136:139]\n\t"
      "s_lshl_b32 s72, s72, 13\n\t"
      "s_lshl_b32 s74, s68, 20\n\t"
      "s_or_b32 s72, s72, s74\n\t"
      "v_cvt_pk_bf16_f32 v72, v132, v133\n\t"
      "v_mfma_f32_16x16x32_bf16 v[144:147], a[48:51], v[168:171], v[144:147]\n\t"
      "v_cvt_pk_bf16_f32 v73, v134, v135\n\t"
      "v_mfma_f32_16x16x32_bf16 v[152:155], a[56:59], v[168:171], v[152:155]\n\t"
      "v_cvt_pk_bf16_f32 v74, v140, v141\n\t"
      "s_waitcnt lgkmcnt(4)\n\t"
      "v_mfma_f32_16x16x32_bf16 v[128:131], a[36:39], v[172:175], v[128:131]\n\t"
      "ds_read_b128 v[168:171], v56 offset:16384\n\t"
      "v_cvt_pk_bf16_f32 v75, v142, v143\n\t"
      "v_mfma_f32_16x16x32_bf16 v[136:139], a[44:47], v[172:175], v[136:139]\n\t"
      "s_add_u32 s72, s72, %[ob]\n\t"
      "buffer_store_dwordx4 v[72:75], %[l16], %[srd], s78 offen sc1\n\t"
      "v_mfma_f32_16x16x32_bf16 v[144:147], a[52:55], v[172:175], v[144:147]\n\t"
      "v_cvt_pk_bf16_f32 v76, v148, v149\n\t"
      "v_mfma_f32_16x16x32_bf16 v[152:155], a[60:63], v[172:175], v[152:155]\n\t"
      "v_cvt_pk_bf16_f32 v77, v150, v151\n\t"
      "s_waitcnt lgkmcnt(4)\n\t"
      "v_mfma_f32_16x16x32_bf16 v[128:131], a[64:67], v[176:179], v[128:131]\n\t"
      "ds_read_b128 v[172:175], v57 offset:16384\n\t"
      "v_cvt_pk_bf16_f32 v78, v156, v157\n\t"
      "v_mfma_f32_16x16x32_bf16 v[136:139], a[72:75], v[176:179], v[136:139]\n\t"
      "s_add_u32 s76, s72, 16384\n\t"
      "s_add_u32 s77, s72, 0\n\t"
      "v_cvt_pk_bf16_f32 v79, v158, v159\n\t"
      "v_mfma_f32_16x16x32_bf16 v[144:147], a[80:83], v[176:179], v[144:147]\n\t"
      "buffer_store_dwordx4 v[76:79], %[l16], %[srd], s79 offen sc1\n\t"
      "v_mfma_f32_16x16x32_bf16 v[152:155], a[88:91], v[176:179], v[152:155]\n\t"
      "s_waitcnt lgkmcnt(4)\n\t"
      "v_mfma_f32_16x16x32_bf16 v[128:131], a[68:71], v[180:183], v[128:131]\n\t"
      "ds_read_b128 v[176:179], v56 offset:20480\n\t"
      "v_mfma_f32_16x16x32_bf16 v[136:139], a[76:79], v[180:183], v[136:139]\n\t"
      "s_sub_u32 s83, s68, 1\n\t"
      "s_cmp_lt_u32 s83, 1\n\t"
      "s_cbranch_scc1 34f\n\t"
      "35:\n\t"
      "v_mfma_f32_16x16x32_bf16 v[144:147], a[84:87], v[180:183], v[144:147]\n\t"
      "v_mfma_f32_16x16x32_bf16 v[152:155], a[92:95], v[180:183], v[152:155]\n\t"
      "s_waitcnt lgkmcnt(4)\n\t"
      "v_mfma_f32_16x16x32_bf16 v[128:131], a[96:99], v[160:163], v[128:131]\n\t"
      "ds_read_b128 v[180:183], v57 offset:20480\n\t"
      "v_mfma_f32_16x16x32_bf16 v[136:139], a[104:107], v[160:163], v[136:139]\n\t"
      "v_mfma_f32_16x16x32_bf16 v[144:147], a[112:115], v[160:163], v[144:147]\n\t"
      "v_mfma_f32_16x16x32_bf16 v[152:155], a[120:123], v[160:163], v[152:155]\n\t"
      "s_waitcnt lgkmcnt(4)\n\t"
      "v_mfma_f32_16x16x32_bf16 v[128:131], a[100:103], v[164:167], v[128:131]\n\t"
      "ds_read_b128 v[160:163], v56 offset:24576\n\t"
      "v_mfma_f32_16x16x32_bf16 v[136:139], a[108:111], v[164:167], v[136:139]\n\t"
      "v_mfma_f32_16x16x32_bf16 v[144:147], a[116:119], v[164:167], v[144:147]\n\t"
      "v_mfma_f32_16x16x32_bf16 v[152:155], a[124:127], v[164:167], v[152:155]\n\t"
      "s_mov_b32 m0, s70\n\t"
      "s_nop 0\n\t"
      "buffer_load_dwordx4 %[xv], %[xsrd], s71 offen lds\n\t"
      "s_waitcnt lgkmcnt(4)\n\t"
      "v_mfma_f32_16x16x32_bf16 v[128:131], a[128:131], v[168:171], v[128:131]\n\t"
      "ds_read_b128 v[164:167], v57 offset:24576\n\t"
      "v_mfma_f32_16x16x32_bf16 v[136:139], a[136:139], v[168:171], v[136:139]\n\t"
      "v_mfma_f32_16x16x32_bf16 v[144:147], a[144:147], v[168:171], v[144:147]\n\t"
      "v_mfma_f32_16x16x32_bf16 v[152:155], a[152:155], v[168:171], v[152:155]\n\t"
      "s_add_u32 m0, m0, 3968\n\t"
      "s_nop 0\n\t"
      "buffer_load_dwordx4 %[xv], %[xsrd], s71 offen offset:128 lds\n\t"
      "s_waitcnt lgkmcnt(4)\n\t"
      "v_mfma_f32_16x16x32_bf16 v[128:131], a[132:135], v[172:175], v[128:131]\n\t"
      "ds_read_b128 v[168:171], v56 offset:28672\n\t"
      "v_mfma_f32_16x16x32_bf16 v[136:139], a[140:143], v[172:175], v[136:139]\n\t"
      "v_mfma_f32_16x16x32_bf16 v[144:147], a[148:151], v[172:175], v[144:147]\n\t"
      "v_mfma_f32_16x16x32_bf16 v[152:155], a[156:159], v[172:175], v[152:155]\n\t"
      "s_add_u32 m0, m0, 3968\n\t"
      "s_nop 0\n\t"
      "buffer_load_dwordx4 %[xv], %[xsrd], s71 offen offset:256 lds\n\t"
      "s_waitcnt lgkmcnt(4)\n\t"
      "v_mfma_f32_16x16x32_bf16 v[128:131], a[160:163], v[176:179], v[128:131]\n\t"
      "ds_read_b128 v[172:175], v57 offset:28672\n\t"
      "v_mfma_f32_16x16x32_bf16 v[136:139], a[168:171], v[176:179], v[136:139]\n\t"
      "v_mfma_f32_16x16x32_bf16 v[144:147], a[176:179], v[176:179], v[144:147]\n\t"
      "v_mfma_f32_16x16x32_bf16 v[152:155], a[184:187], v[176:179], v[152:155]\n\t"
      "s_add_u32 m0, m0, 3968\n\t"
      "s_nop 0\n\t"
      "buffer_load_dwordx4 %[xv], %[xsrd], s71 offen offset:384 lds\n\t"
      "s_waitcnt lgkmcnt(4)\n\t"
      "v_mfma_f32_16x16x32_bf16 v[128:131], a[164:167], v[180:183], v[128:131]\n\t"
      "ds_read_b128 v[176:179], v56 offset:32768\n\t"
      "v_mfma_f32_16x16x32_bf16 v[136:139], a[172:175], v[180:183], v[136:139]\n\t"
      "v_mfma_f32_16x16x32_bf16 v[144:147], a[180:183], v[180:183], v[144:147]\n\t"
      "v_mfma_f32_16x16x32_bf16 v[152:155], a[188:191], v[180:183], v[152:155]\n\t"
      "s_add_u32 m0, m0, 3968\n\t"
      "s_nop 0\n\t"
      "buffer_load_dwordx4 %[xv], %[xsrd], s71 offen offset:512 lds\n\t"
      "s_waitcnt lgkmcnt(4)\n\t"
      "v_mfma_f32_16x16x32_bf16 v[128:131], a[192:195], v[160:163], v[128:131]\n\t"
      "ds_read_b128 v[180:183], v57 offset:32768\n\t"
      "v_mfma_f32_16x16x32_bf16 v[136:139], a[200:203], v[160:163], v[136:139]\n\t"
      "v_mfma_f32_16x16x32_bf16 v[144:147], a[208:211], v[160:163], v[144:147]\n\t"
      "v_mfma_f32_16x16x32_bf16 v[152:155], a[216:219], v[160:163], v[152:155]\n\t"
      "s_add_u32 m0, m0, 3968\n\t"
      "s_nop 0\n\t"
      "buffer_load_dwordx4 %[xv], %[xsrd], s71 offen offset:640 lds\n\t"
      "s_waitcnt lgkmcnt(4)\n\t"
      "v_mfma_f32_16x16x32_bf16 v[128:131], a[196:199], v[164:167], v[128:131]\n\t"
      "ds_read_b128 v[160:163], v56 offset:2048\n\t"
      "v_mfma_f32_16x16x32_bf16 v[136:139], a[204:207], v[164:167], v[136:139]\n\t"
      "v_mfma_f32_16x16x32_bf16 v[144:147], a[212:215], v[164:167], v[144:147]\n\t"
      "v_mfma_f32_16x16x32_bf16 v[152:155], a[220:223], v[164:167], v[152:155]\n\t"
      "s_add_u32 m0, m0, 3968\n\t"
      "s_nop 0\n\t"
      "buffer_load_dwordx4 %[xv], %[xsrd], s71 offen offset:768 lds\n\t"
      "s_waitcnt lgkmcnt(4)\n\t"
      "v_mfma_f32_16x16x32_bf16 v[128:131], a[224:227], v[168:171], v[128:131]\n\t"
      "ds_read_b128 v[164:167], v57 offset:2048\n\t"
      "v_mfma_f32_16x16x32_bf16 v[136:139], a[232:235], v[168:171], v[136:139]\n\t"
      "v_mfma_f32_16x16x32_bf16 v[144:147], a[240:243], v[168:171], v[144:147]\n\t"
      "v_mfma_f32_16x16x32_bf16 v[152:155], a[248:251], v[168:171], v[152:155]\n\t"
      "s_add_u32 m0, m0, 3968\n\t"
      "s_nop 0\n\t"
      "buffer_load_dwordx4 %[xv], %[xsrd], s71 offen offset:896 lds\n\t"
      "s_waitcnt lgkmcnt(4)\n\t"
      "v_mfma_f32_16x16x32_bf16 v[128:131], a[228:231], v[172:175], v[128:131]\n\t"
      "ds_read_b128 v[168:171], v56 offset:6144\n\t"
      "v_mfma_f32_16x16x32_bf16 v[136:139], a[236:239], v[172:175], v[136:139]\n\t"
      "v_mfma_f32_16x16x32_bf16 v[144:147], a[244:247], v[172:175], v[144:147]\n\t"
      "v_mfma_f32_16x16x32_bf16 v[152:155], a[252:255], v[172:175], v[152:155]\n\t"
      "s_add_u32 m0, m0, 3968\n\t"
      "s_nop 0\n\t"
      "buffer_load_dwordx4 %[xv], %[xsrd], s71 offen offset:1024 lds\n\t"
      "s_waitcnt lgkmcnt(4)\n\t"
      "v_mfma_f32_16x16x32_bf16 v[128:131], v[224:227], v[176:179], v[128:131]\n\t"
      "ds_read_b128 v[172:175], v57 offset:6144\n\t"
      "v_mfma_f32_16x16x32_bf16 v[136:139], v[232:235], v[176:179], v[136:139]\n\t"
      "v_mfma_f32_16x16x32_bf16 v[144:147], v[240:243], v[176:179], v[144:147]\n\t"
      "v_mfma_f32_16x16x32_bf16 v[152:155], v[248:251], v[176:179], v[152:155]\n\t"
      "s_waitcnt lgkmcnt(4)\n\t"
      "v_mfma_f32_16x16x32_bf16 v[128:131], v[228:231], v[180:183], v[128:131]\n\t"
      "ds_read_b128 v[176:179], v56 offset:10240\n\t"
      "v_mfma_f32_16x16x32_bf16 v[136:139], v[236:239], v[180:183], v[136:139]\n\t"
      "s_add_u32 s70, s70, 36864\n\t"
      "s_sub_u32 s74, s70, 110592\n\t"
      "s_cselect_b32 s70, s70, s74\n\t"
      "v_mfma_f32_16x16x32_bf16 v[144:147], v[244:247], v[180:183], v[144:147]\n\t"
      "s_add_u32 s71, s71, 36864\n\t"
      "v_mfma_f32_16x16x32_bf16 v[152:155], v[252:255], v[180:183], v[152:155]\n\t"
      "s_waitcnt lgkmcnt(4)\n\t"
      "v_mfma_f32_16x16x32_bf16 v[132:135], a[0:3], v[160:163], 0\n\t"
      "ds_read_b128 v[180:183], v57 offset:10240\n\t"
      "v_mfma_f32_16x16x32_bf16 v[140:143], a[8:11], v[160:163], 0\n\t"
      "s_add_u32 s78, s72, 20480\n\t"
      "s_add_u32 s79, s72, 4096\n\t"
      "v_mfma_f32_16x16x32_bf16 v[148:151], a[16:19], v[160:163], 0\n\t"
      "v_mfma_f32_16x16x32_bf16 v[156:159], a[24:27], v[160:163], 0\n\t"
      "s_waitcnt lgkmcnt(4)\n\t"
      "v_mfma_f32_16x16x32_bf16 v[132:135], a[4:7], v[164:167], v[132:135]\n\t"
      "ds_read_b128 v[160:163], v56 offset:14336\n\t"
      "v_mfma_f32_16x16x32_bf16 v[140:143], a[12:15], v[164:167], v[140:143]\n\t"
      "v_mov_b32 v61, s68\n\t"
      "v_mfma_f32_16x16x32_bf16 v[148:151], a[20:23], v[164:167], v[148:151]\n\t"
      "v_mfma_f32_16x16x32_bf16 v[156:159], a[28:31], v[164:167], v[156:159]\n\t"
      "s_waitcnt lgkmcnt(4)\n\t"
      "v_mfma_f32_16x16x32_bf16 v[132:135], a[32:35], v[168:171], v[132:135]\n\t"
      "ds_read_b128 v[164:167], v57 offset:14336\n\t"
      "v_mfma_f32_16x16x32_bf16 v[140:143], a[40:43], v[168:171], v[140:143]\n\t"
      "v_cvt_pk_bf16_f32 v64, v128, v129\n\t"
      "v_mfma_f32_16x16x32_bf16 v[148:151], a[48:51], v[168:171], v[148:151]\n\t"
      "v_cvt_pk_bf16_f32 v65, v130, v131\n\t"
      "v_mfma_f32_16x16x32_bf16 v[156:159], a[56:59], v[168:171], v[156:159]\n\t"
      "v_cvt_pk_bf16_f32 v66, v136, v137\n\t"
      "s_waitcnt lgkmcnt(4)\n\t"
      "v_mfma_f32_16x16x32_bf16 v[132:135], a[36:39], v[172:175], v[132:135]\n\t"
      "ds_read_b128 v[168:171], v56 offset:18432\n\t"
      "v_cvt_pk_bf16_f32 v67, v138, v139\n\t"
      "v_mfma_f32_16x16x32_bf16 v[140:143], a[44:47], v[172:175], v[140:143]\n\t"
      "buffer_store_dwordx4 v[64:67], %[l16], %[srd], s76 offen sc1\n\t"
      "v_mfma_f32_16x16x32_bf16 v[148:151], a[52:55], v[172:175], v[148:151]\n\t"
      "v_cvt_pk_bf16_f32 v68, v144, v145\n\t"
      "v_mfma_f32_16x16x32_bf16 v[156:159], a[60:63], v[172:175], v[156:159]\n\t"
      "v_cvt_pk_bf16_f32 v69, v146, v147\n\t"
      "s_waitcnt lgkmcnt(4)\n\t"
      "v_mfma_f32_16x16x32_bf16 v[132:135], a[64:67], v[176:179], v[132:135]\n\t"
      "ds_read_b128 v[172:175], v57 offset:18432\n\t"
      "v_cvt_pk_bf16_f32 v70, v152, v153\n\t"
      "v_mfma_f32_16x16x32_bf16 v[140:143], a[72:75], v[176:179], v[140:143]\n\t"
      "v_cvt_pk_bf16_f32 v71, v154, v155\n\t"
      "v_mfma_f32_16x16x32_bf16 v[148:151], a[80:83], v[176:179], v[148:151]\n\t"
      "buffer_store_dwordx4 v[68:71], %[l16], %[srd], s77 offen sc1\n\t"
      "v_mfma_f32_16x16x32_bf16 v[156:159], a[88:91], v[176:179], v[156:159]\n\t"
      "s_waitcnt lgkmcnt(4)\n\t"
      "v_mfma_f32_16x16x32_bf16 v[132:135], a[68:71], v[180:183], v[132:135]\n\t"
      "ds_read_b128 v[176:179], v56 offset:22528\n\t"
      "v_mfma_f32_16x16x32_bf16 v[140:143], a[76:79], v[180:183], v[140:143]\n\t"
      "v_mfma_f32_16x16x32_bf16 v[148:151], a[84:87], v[180:183], v[148:151]\n\t"
      "v_mfma_f32_16x16x32_bf16 v[156:159], a[92:95], v[180:183], v[156:159]\n\t"
      "s_waitcnt lgkmcnt(4)\n\t"
      "v_mfma_f32_16x16x32_bf16 v[132:135], a[96:99], v[160:163], v[132:135]\n\t"
      "ds_read_b128 v[180:183], v57 offset:22528\n\t"
      "v_mfma_f32_16x16x32_bf16 v[140:143], a[104:107], v[160:163], v[140:143]\n\t"
      "v_mfma_f32_16x16x32_bf16 v[148:151], a[112:115], v[160:163], v[148:151]\n\t"
      "v_mfma_f32_16x16x32_bf16 v[156:159], a[120:123], v[160:163], v[156:159]\n\t"
      "s_waitcnt lgkmcnt(4)\n\t"
      "v_mfma_f32_16x16x32_bf16 v[132:135], a[100:103], v[164:167], v[132:135]\n\t"
      "ds_read_b128 v[160:163], v56 offset:26624\n\t"
      "v_mfma_f32_16x16x32_bf16 v[140:143], a[108:111], v[164:167], v[140:143]\n\t"
      "v_mfma_f32_16x16x32_bf16 v[148:151], a[116:119], v[164:167], v[148:151]\n\t"
      "v_mfma_f32_16x16x32_bf16 v[156:159], a[124:127], v[164:167], v[156:159]\n\t"
      "s_waitcnt lgkmcnt(4)\n\t"
      "v_mfma_f32_16x16x32_bf16 v[132:135], a[128:131], v[168:171], v[132:135]\n\t"
      "ds_read_b128 v[164:167], v57 offset:26624\n\t"
      "v_mfma_f32_16x16x32_bf16 v[140:143], a[136:139], v[168:171], v[140:143]\n\t"
      "v_mfma_f32_16x16x32_bf16 v[148:151], a[144:147], v[168:171], v[148:151]\n\t"
      "v_mfma_f32_16x16x32_bf16 v[156:159], a[152:155], v[168:171], v[156:159]\n\t"
      "s_waitcnt lgkmcnt(4)\n\t"
      "v_mfma_f32_16x16x32_bf16 v[132:135], a[132:135], v[172:175], v[132:135]\n\t"
      "ds_read_b128 v[168:171], v56 offset:30720\n\t"
      "v_mfma_f32_16x16x32_bf16 v[140:143], a[140:143], v[172:175], v[140:143]\n\t"
      "v_mfma_f32_16x16x32_bf16 v[148:151], a[148:151], v[172:175], v[148:151]\n\t"
      "v_mfma_f32_16x16x32_bf16 v[156:159], a[156:159], v[172:175], v[156:159]\n\t"
      "s_waitcnt lgkmcnt(4)\n\t"
      "v_mfma_f32_16x16x32_bf16 v[132:135], a[160:163], v[176:179], v[132:135]\n\t"
      "ds_read_b128 v[172:175], v57 offset:30720\n\t"
      "v_mfma_f32_16x16x32_bf16 v[140:143], a[168:171], v[176:179], v[140:143]\n\t"
      "v_mfma_f32_16x16x32_bf16 v[148:151], a[176:179], v[176:179], v[148:151]\n\t"
      "v_mfma_f32_16x16x32_bf16 v[156:159], a[184:187], v[176:179], v[156:159]\n\t"
      "s_waitcnt lgkmcnt(4)\n\t"
      "v_mfma_f32_16x16x32_bf16 v[132:135], a[164:167], v[180:183], v[132:135]\n\t"
      "ds_read_b128 v[176:179], v56 offset:34816\n\t"
      "v_mfma_f32_16x16x32_bf16 v[140:143], a[172:175], v[180:183], v[140:143]\n\t"
      "v_mfma_f32_16x16x32_bf16 v[148:151], a[180:183], v[180:183], v[148:151]\n\t"
      "s_waitcnt vmcnt(2)\n\t"
      "s_barrier\n\t"
      "buffer_store_dword v61, %[fo], %[srd], 0 offen sc1\n\t"
      "v_mfma_f32_16x16x32_bf16 v[156:159], a[188:191], v[180:183], v[156:159]\n\t"
      "s_waitcnt lgkmcnt(4)\n\t"
      "v_mfma_f32_16x16x32_bf16 v[132:135], a[192:195], v[160:163], v[132:135]\n\t"
      "ds_read_b128 v[180:183], v57 offset:34816\n\t"
      "v_mfma_f32_16x16x32_bf16 v[140:143], a[200:203], v[160:163], v[140:143]\n\t"
      "v_mov_b32 v56, v58\n\t"
      "v_mfma_f32_16x16x32_bf16 v[148:151], a[208:211], v[160:163], v[148:151]\n\t"
      "v_mov_b32 v57, v59\n\t"
      "v_mfma_f32_16x16x32_bf16 v[156:159], a[216:219], v[160:163], v[156:159]\n\t"
      "s_waitcnt lgkmcnt(4)\n\t"
      "v_mfma_f32_16x16x32_bf16 v[132:135], a[196:199], v[164:167], v[132:135]\n\t"
      "ds_read_b128 v[160:163], v56 offset:0\n\t"
      "v_mfma_f32_16x16x32_bf16 v[140:143], a[204:207], v[164:167], v[140:143]\n\t"
      "v_mfma_f32_16x16x32_bf16 v[148:151], a[212:215], v[164:167], v[148:151]\n\t"
      "v_mfma_f32_16x16x32_bf16 v[156:159], a[220:223], v[164:167], v[156:159]\n\t"
      "s_waitcnt lgkmcnt(4)\n\t"
      "v_mfma_f32_16x16x32_bf16 v[132:135], a[224:227], v[168:171], v[132:135]\n\t"
      "ds_read_b128 v[164:167], v57 offset:0\n\t"
      "v_mfma_f32_16x16x32_bf16 v[140:143], a[232:235], v[168:171], v[140:143]\n\t"
      "v_mfma_f32_16x16x32_bf16 v[148:151], a[240:243], v[168:171], v[148:151]\n\t"
      "v_mfma_f32_16x16x32_bf16 v[156:159], a[248:251], v[168:171], v[156:159]\n\t"
      "s_waitcnt lgkmcnt(4)\n\t"
      "v_mfma_f32_16x16x32_bf16 v[132:135], a[228:231], v[172:175], v[132:135]\n\t"
      "ds_read_b128 v[168:171], v56 offset:4096\n\t"
      "v_mfma_f32_16x16x32_bf16 v[140:143], a[236:239], v[172:175], v[140:143]\n\t"
      "v_mfma_f32_16x16x32_bf16 v[148:151], a[244:247], v[172:175], v[148:151]\n\t"
      "v_mfma_f32_16x16x32_bf16 v[156:159], a[252:255], v[172:175], v[156:159]\n\t"
      "s_waitcnt lgkmcnt(4)\n\t"
      "v_mfma_f32_16x16x32_bf16 v[132:135], v[224:227], v[176:179], v[132:135]\n\t"
      "ds_read_b128 v[172:175], v57 offset:4096\n\t"
      "v_mfma_f32_16x16x32_bf16 v[140:143], v[232:235], v[176:179], v[140:143]\n\t"
      "v_mfma_f32_16x16x32_bf16 v[148:151], v[240:243], v[176:179], v[148:151]\n\t"
      "v_mfma_f32_16x16x32_bf16 v[156:159], v[248:251], v[176:179], v[156:159]\n\t"
      "s_waitcnt lgkmcnt(4)\n\t"
      "v_mfma_f32_16x16x32_bf16 v[132:135], v[228:231], v[180:183], v[132:135]\n\t"
      "ds_read_b128 v[176:179], v56 offset:8192\n\t"
      "v_mfma_f32_16x16x32_bf16 v[140:143], v[236:239], v[180:183], v[140:143]\n\t"
      "v_mfma_f32_16x16x32_bf16 v[148:151], v[244:247], v[180:183], v[148:151]\n\t"
      "v_mfma_f32_16x16x32_bf16 v[156:159], v[252:255], v[180:183], v[156:159]\n\t"
      "s_add_u32 s68, s68, 1\n\t"
      "s_cmp_lt_u32 s68, 64\n\t"
      "s_cbranch_scc1 10b\n\t"
      "s_nop 7\n\t"
      "v_cvt_pk_bf16_f32 v72, v132, v133\n\t"
      "v_cvt_pk_bf16_f32 v73, v134, v135\n\t"
      "v_cvt_pk_bf16_f32 v74, v140, v141\n\t"
      "v_cvt_pk_bf16_f32 v75, v142, v143\n\t"
      "buffer_store_dwordx4 v[72:75], %[l16], %[srd], s78 offen sc1\n\t"
      "v_cvt_pk_bf16_f32 v76, v148, v149\n\t"
      "v_cvt_pk_bf16_f32 v77, v150, v151\n\t"
      "v_cvt_pk_bf16_f32 v78, v156, v157\n\t"
      "v_cvt_pk_bf16_f32 v79, v158, v159\n\t"
      "buffer_store_dwordx4 v[76:79], %[l16], %[srd], s79 offen sc1\n\t"
      "s_mov_b32 m0, s73\n\t"
      "s_waitcnt vmcnt(0) lgkmcnt(0)\n\t"
      "s_branch 37f\n\t"
      "34:\n\t"
      "v_mov_b32 v61, s68\n\t"
      "s_waitcnt vmcnt(0)\n\t"
      "s_barrier\n\t"
      "buffer_store_dword v61, %[fo], %[srd], 0 offen sc1\n\t"
      "s_branch 35b\n\t"
      "37:\n\t"
      :: [ra0] "v"(ra0), [ra1] "v"(ra1), [wl] "s"(US(wl)), [ob] "s"(US(ob)), [xv] "v"(xv), [xsrd] "s"(xsrd), [fo] "v"(fo), [srd] "s"(srd), [l16] "v"(l16), [s0] "s"(s0), [fm] "s"(US(fm)), WPIN(W)
      : "v56", "v57", "v58", "v59", "v60", "v61", "v62", "v63", "v64", "v65", "v66", "v67", "v68", "v69", "v70", "v71", "v72", "v73", "v74", "v75", "v76", "v77", "v78", "v79", "v80", "v81", "v82", "v83", "v84", "v85", "v86", "v87", "v88", "v89", "v90", "v91", "v92", "v93", "v94", "v95", "v128", "v129", "v130", "v131", "v132", "v133", "v134", "v135", "v136", "v137", "v138", "v139", "v140", "v141", "v142", "v143", "v144", "v145", "v146", "v147", "v148", "v149", "v150", "v151", "v152", "v153", "v154", "v155", "v156", "v157", "v158", "v159", "v160", "v161", "v162", "v163", "v164", "v165", "v166", "v167", "v168", "v169", "v170", "v171", "v172", "v173", "v174", "v175", "v176", "v177", "v178", "v179", "v180", "v181", "v182", "v183", "v184", "v185", "v186", "v187", "v188", "v189", "v190", "v191", "v192", "v193", "v194", "v195", "s64", "s65", "s66", "s67", "s68", "s69", "s70", "s71", "s72", "s73", "s74", "s75", "s76", "s77", "s78", "s79", "s80", "s81", "s82", "s83", "s84", "s85", "s86", "s87", "s88", "s89", "s90", "s91", "s92", "s93", "s94", "s95", "scc", "memory");
  }
  wait_vm<0>(); bar();
  st4(NSEG, rs, fo, 0);
}

DI void role_Z(const Params& p, int h, u2 e) {
  const int tid = threadIdx.x, lane = tid & 63, w = __builtin_amdgcn_readfirstlane(tid >> 6);
  const int c = lane & 15, g = lane >> 4;
  const __amdgpu_buffer_rsrc_t rs = mkrsrc(p.ws, WS_SPAN);
  const u4 srd = mksrd(p.ws, WS_SPAN), xsrd = mksrd(p.x, 2048 * 1152);
  const u32 xv = (8 * w + (lane >> 3)) * 1152 + (((lane & 7) ^ ((lane >> 3) & 7)) * 16);
  const u32 wl = w * 1024;
  dma_seg(xsrd, xv, 0, wl);
  dma_seg(xsrd, xv, 36864, 36864 + wl);
  u32v W[9];
  load_wvo<2>(W, p, h, w, lane);
  const u32 ra0 = c * 128 + ((g ^ (c & 7)) * 16), ra1 = c * 128 + (((4 + g) ^ (c & 7)) * 16);
  const u32 fo = fboth(32 + h, tid);
  const u32 zo = w * 32768 + h * 1024 + lane * 16, z0 = OFF_Z;
  const u32 vr_ = 110592u + lane * 16, vw = vr_ + wl;
  wait_vm<0>(); bar();
  LOOPSTART
  asm volatile(
      "s_mov_b32 s73, m0\n\t"
      "s_mov_b32 s68, 0\n\t"
      "s_mov_b32 s69, 36864\n\t"
      "s_add_u32 s70, %[wl], 73728\n\t"
      "s_mov_b32 s71, 73728\n\t"
      "v_mov_b32 v62, 0\n\t"
      "v_mov_b32 v56, %[ra0]\n\t"
      "v_mov_b32 v57, %[ra1]\n\t"
      "v_add_u32 v58, 36864, %[ra0]\n\t"
      "v_add_u32 v59, 36864, %[ra1]\n\t"
      "v_mov_b32 v61, 0\n\t"
      "ds_read_b128 v[160:163], v56 offset:0\n\t"
      "ds_read_b128 v[164:167], v57 offset:0\n\t"
      "ds_read_b128 v[168:171], v56 offset:4096\n\t"
      "ds_read_b128 v[172:175], v57 offset:4096\n\t"
      "ds_read_b128 v[176:179], v56 offset:8192\n\t"
      "ds_read_b128 v[180:183], v57 offset:8192\n\t"
      "ds_read_b128 v[184:187], v56 offset:12288\n\t"
      "s_waitcnt lgkmcnt(0)\n\t"
      "s_mov_b32 s76, 0xf0000000\n\t"
      "s_mov_b32 s77, 0xf0000000\n\t"
      "10:\n\t"
      "s_waitcnt lgkmcnt(6)\n\t"
      "v_mfma_f32_16x16x32_bf16 v[128:131], a[0:3], v[160:163], 0\n\t"
      "ds_read_b128 v[188:191], v57 offset:12288\n\t"
      "v_mfma_f32_16x16x32_bf16 v[132:135], a[8:11], v[160:163], 0\n\t"
      "buffer_store_dwordx4 v[204:207], %[zo], %[srd], s76 offen sc1\n\t"
      "s_add_u32 s76, s76, 0x20000\n\t"
      "s_waitcnt lgkmcnt(6)\n\t"
      "v_mfma_f32_16x16x32_bf16 v[128:131], a[4:7], v[164:167], v[128:131]\n\t"
      "ds_read_b128 v[192:195], v56 offset:16384\n\t"
      "v_cvt_pk_bf16_f32 v222, v108, v109\n\t"
      "v_mfma_f32_16x16x32_bf16 v[132:135], a[12:15], v[164:167], v[132:135]\n\t"
      "v_cvt_pk_bf16_f32 v223, v110, v111\n\t"
      "s_waitcnt lgkmcnt(6)\n\t"
      "v_mfma_f32_16x16x32_bf16 v[128:131], a[16:19], v[168:171], v[128:131]\n\t"
      "ds_read_b128 v[160:163], v57 offset:16384\n\t"
      "v_mfma_f32_16x16x32_bf16 v[132:135], a[24:27], v[168:171], v[132:135]\n\t"
      "v_cvt_pk_bf16_f32 v114, v120, v121\n\t"
      "s_waitcnt lgkmcnt(6)\n\t"
      "v_mfma_f32_16x16x32_bf16 v[128:131], a[20:23], v[172:175], v[128:131]\n\t"
      "ds_read_b128 v[164:167], v56 offset:20480\n\t"
      "v_cvt_pk_bf16_f32 v115, v122, v123\n\t"
      "v_mfma_f32_16x16x32_bf16 v[132:135], a[28:31], v[172:175], v[132:135]\n\t"
      "s_waitcnt lgkmcnt(6)\n\t"
      "v_mfma_f32_16x16x32_bf16 v[128:131], a[32:35], v[176:179], v[128:131]\n\t"
      "ds_read_b128 v[168:171], v57 offset:20480\n\t"
      "v_mfma_f32_16x16x32_bf16 v[132:135], a[40:43], v[176:179], v[132:135]\n\t"
      "buffer_store_dwordx4 v[208:211], %[zo], %[srd], s76 offen sc1\n\t"
      "s_add_u32 s76, s76, 0x20000\n\t"
      "v_cvt_pk_bf16_f32 v118, v124, v125\n\t"
      "s_waitcnt lgkmcnt(6)\n\t"
      "v_mfma_f32_16x16x32_bf16 v[128:131], a[36:39], v[180:183], v[128:131]\n\t"
      "ds_read_b128 v[172:175], v56 offset:24576\n\t"
      "v_cvt_pk_bf16_f32 v119, v126, v127\n\t"
      "v_mfma_f32_16x16x32_bf16 v[132:135], a[44:47], v[180:183], v[132:135]\n\t"
      "s_waitcnt lgkmcnt(6)\n\t"
      "v_mfma_f32_16x16x32_bf16 v[128:131], a[48:51], v[184:187], v[128:131]\n\t"
      "ds_read_b128 v[176:179], v57 offset:24576\n\t"
      "v_mfma_f32_16x16x32_bf16 v[132:135], a[56:59], v[184:187], v[132:135]\n\t"
      "s_waitcnt lgkmcnt(6)\n\t"
      "v_mfma_f32_16x16x32_bf16 v[128:131], a[52:55], v[188:191], v[128:131]\n\t"
      "ds_read_b128 v[180:183], v56 offset:28672\n\t"
      "v_mfma_f32_16x16x32_bf16 v[132:135], a[60:63], v[188:191], v[132:135]\n\t"
      "s_sub_u32 s83, s68, 1\n\t"
      "s_cmp_lt_u32 s83, 1\n\t"
      "s_cbranch_scc1 34f\n\t"
      "35:\n\t"
      "s_waitcnt lgkmcnt(6)\n\t"
      "v_mfma_f32_16x16x32_bf16 v[128:131], a[64:67], v[192:195], v[128:131]\n\t"
      "ds_read_b128 v[184:187], v57 offset:28672\n\t"
      "v_mfma_f32_16x16x32_bf16 v[132:135], a[72:75], v[192:195], v[132:135]\n\t"
      "buffer_store_dwordx4 v[212:215], %[zo], %[srd], s76 offen sc1\n\t"
      "s_add_u32 s76, s76, 0x20000\n\t"
      "s_waitcnt lgkmcnt(6)\n\t"
      "v_mfma_f32_16x16x32_bf16 v[128:131], a[68:71], v[160:163], v[128:131]\n\t"
      "ds_read_b128 v[188:191], v56 offset:32768\n\t"
      "v_mfma_f32_16x16x32_bf16 v[132:135], a[76:79], v[160:163], v[132:135]\n\t"
      "s_waitcnt lgkmcnt(6)\n\t"
      "v_mfma_f32_16x16x32_bf16 v[128:131], a[80:83], v[164:167], v[128:131]\n\t"
      "ds_read_b128 v[192:195], v57 offset:32768\n\t"
      "v_mfma_f32_16x16x32_bf16 v[132:135], a[88:91], v[164:167], v[132:135]\n\t"
      "s_waitcnt lgkmcnt(6)\n\t"
      "v_mfma_f32_16x16x32_bf16 v[128:131], a[84:87], v[168:171], v[128:131]\n\t"
      "ds_read_b128 v[160:163], v56 offset:2048\n\t"
      "v_mfma_f32_16x16x32_bf16 v[132:135], a[92:95], v[168:171], v[132:135]\n\t"
      "s_waitcnt lgkmcnt(6)\n\t"
      "v_mfma_f32_16x16x32_bf16 v[128:131], a[96:99], v[172:175], v[128:131]\n\t"
      "ds_read_b128 v[164:167], v57 offset:2048\n\t"
      "v_mfma_f32_16x16x32_bf16 v[132:135], a[104:107], v[172:175], v[132:135]\n\t"
      "buffer_store_dwordx4 v[216:219], %[zo], %[srd], s76 offen sc1\n\t"
      "s_add_u32 s76, s76, 0x20000\n\t"
      "s_waitcnt lgkmcnt(6)\n\t"
      "v_mfma_f32_16x16x32_bf16 v[128:131], a[100:103], v[176:179], v[128:131]\n\t"
      "ds_read_b128 v[168:171], v56 offset:6144\n\t"
      "v_mfma_f32_16x16x32_bf16 v[132:135], a[108:111], v[176:179], v[132:135]\n\t"
      "s_waitcnt lgkmcnt(6)\n\t"
      "v_mfma_f32_16x16x32_bf16 v[128:131], a[112:115], v[180:183], v[128:131]\n\t"
      "ds_read_b128 v[172:175], v57 offset:6144\n\t"
      "v_mfma_f32_16x16x32_bf16 v[132:135], a[120:123], v[180:183], v[132:135]\n\t"
      "s_waitcnt lgkmcnt(6)\n\t"
      "v_mfma_f32_16x16x32_bf16 v[128:131], a[116:119], v[184:187], v[128:131]\n\t"
      "ds_read_b128 v[176:179], v56 offset:10240\n\t"
      "v_mfma_f32_16x16x32_bf16 v[132:135], a[124:127], v[184:187], v[132:135]\n\t"
      "s_waitcnt lgkmcnt(6)\n\t"
      "v_mfma_f32_16x16x32_bf16 v[128:131], a[128:131], v[188:191], v[128:131]\n\t"
      "ds_read_b128 v[180:183], v57 offset:10240\n\t"
      "v_mfma_f32_16x16x32_bf16 v[132:135], a[136:139], v[188:191], v[132:135]\n\t"
      "buffer_store_dwordx4 v[220:223], %[zo], %[srd], s76 offen sc1\n\t"
      "s_add_u32 s76, s76, 0x20000\n\t"
      "s_waitcnt lgkmcnt(6)\n\t"
      "v_mfma_f32_16x16x32_bf16 v[128:131], a[132:135], v[192:195], v[128:131]\n\t"
      "ds_read_b128 v[184:187], v56 offset:14336\n\t"
      "v_mfma_f32_16x16x32_bf16 v[132:135], a[140:143], v[192:195], v[132:135]\n\t"
      "s_waitcnt lgkmcnt(6)\n\t"
      "v_mfma_f32_16x16x32_bf16 v[136:139], a[0:3], v[160:163], 0\n\t"
      "ds_read_b128 v[188:191], v57 offset:14336\n\t"
      "v_mfma_f32_16x16x32_bf16 v[140:143], a[8:11], v[160:163], 0\n\t"
      "s_waitcnt lgkmcnt(6)\n\t"
      "v_mfma_f32_16x16x32_bf16 v[136:139], a[4:7], v[164:167], v[136:139]\n\t"
      "ds_read_b128 v[192:195], v56 offset:18432\n\t"
      "v_mfma_f32_16x16x32_bf16 v[140:143], a[12:15], v[164:167], v[140:143]\n\t"
      "s_waitcnt lgkmcnt(6)\n\t"
      "v_mfma_f32_16x16x32_bf16 v[136:139], a[16:19], v[168:171], v[136:139]\n\t"
      "ds_read_b128 v[160:163], v57 offset:18432\n\t"
      "v_mfma_f32_16x16x32_bf16 v[140:143], a[24:27], v[168:171], v[140:143]\n\t"
      "buffer_store_dwordx4 v[112:115], %[zo], %[srd], s76 offen sc1\n\t"
      "s_add_u32 s76, s76, 0x20000\n\t"
      "s_waitcnt lgkmcnt(6)\n\t"
      "v_mfma_f32_16x16x32_bf16 v[136:139], a[20:23], v[172:175], v[136:139]\n\t"
      "ds_read_b128 v[164:167], v56 offset:22528\n\t"
      "v_mfma_f32_16x16x32_bf16 v[140:143], a[28:31], v[172:175], v[140:143]\n\t"
      "v_cvt_pk_bf16_f32 v144, v128, v129\n\t"
      "s_waitcnt lgkmcnt(6)\n\t"
      "v_mfma_f32_16x16x32_bf16 v[136:139], a[32:35], v[176:179], v[136:139]\n\t"
      "ds_read_b128 v[168:171], v57 offset:22528\n\t"
      "v_cvt_pk_bf16_f32 v145, v130, v131\n\t"
      "v_mfma_f32_16x16x32_bf16 v[140:143], a[40:43], v[176:179], v[140:143]\n\t"
      "v_cvt_pk_bf16_f32 v146, v132, v133\n\t"
      "s_waitcnt lgkmcnt(6)\n\t"
      "v_mfma_f32_16x16x32_bf16 v[136:139], a[36:39], v[180:183], v[136:139]\n\t"
      "ds_read_b128 v[172:175], v56 offset:26624\n\t"
      "v_cvt_pk_bf16_f32 v147, v134, v135\n\t"
      "v_mfma_f32_16x16x32_bf16 v[140:143], a[44:47], v[180:183], v[140:143]\n\t"
      "ds_write_b128 %[vw], v[144:147]\n\t"
      "s_waitcnt lgkmcnt(7)\n\t"
      "v_mfma_f32_16x16x32_bf16 v[136:139], a[48:51], v[184:187], v[136:139]\n\t"
      "ds_read_b128 v[176:179], v57 offset:26624\n\t"
      "v_mfma_f32_16x16x32_bf16 v[140:143], a[56:59], v[184:187], v[140:143]\n\t"
      "buffer_store_dwordx4 v[116:119], %[zo], %[srd], s77 offen sc1\n\t"
      "s_add_u32 s76, s76, 0x20000\n\t"
      "s_waitcnt lgkmcnt(7)\n\t"
      "v_mfma_f32_16x16x32_bf16 v[136:139], a[52:55], v[188:191], v[136:139]\n\t"
      "ds_read_b128 v[180:183], v56 offset:30720\n\t"
      "v_mfma_f32_16x16x32_bf16 v[140:143], a[60:63], v[188:191], v[140:143]\n\t"
      "s_waitcnt lgkmcnt(7)\n\t"
      "v_mfma_f32_16x16x32_bf16 v[136:139], a[64:67], v[192:195], v[136:139]\n\t"
      "ds_read_b128 v[184:187], v57 offset:30720\n\t"
      "v_mfma_f32_16x16x32_bf16 v[140:143], a[72:75], v[192:195], v[140:143]\n\t"
      "s_waitcnt lgkmcnt(7)\n\t"
      "v_mfma_f32_16x16x32_bf16 v[136:139], a[68:71], v[160:163], v[136:139]\n\t"
      "ds_read_b128 v[188:191], v56 offset:34816\n\t"
      "v_mfma_f32_16x16x32_bf16 v[140:143], a[76:79], v[160:163], v[140:143]\n\t"
      "s_waitcnt lgkmcnt(4)\n\t"
      "s_barrier\n\t"
      "s_waitcnt lgkmcnt(7)\n\t"
      "v_mfma_f32_16x16x32_bf16 v[136:139], a[80:83], v[164:167], v[136:139]\n\t"
      "ds_read_b128 v[192:195], v57 offset:34816\n\t"
      "v_mfma_f32_16x16x32_bf16 v[140:143], a[88:91], v[164:167], v[140:143]\n\t"
      "ds_read_b128 v[64:67], %[vr] offset:0\n\t"
      "s_waitcnt lgkmcnt(8)\n\t"
      "v_mfma_f32_16x16x32_bf16 v[136:139], a[84:87], v[168:171], v[136:139]\n\t"
      "v_mfma_f32_16x16x32_bf16 v[140:143], a[92:95], v[168:171], v[140:143]\n\t"
      "ds_read_b128 v[68:71], %[vr] offset:1024\n\t"
      "s_waitcnt lgkmcnt(8)\n\t"
      "v_mfma_f32_16x16x32_bf16 v[136:139], a[96:99], v[172:175], v[136:139]\n\t"
      "v_mfma_f32_16x16x32_bf16 v[140:143], a[104:107], v[172:175], v[140:143]\n\t"
      "ds_read_b128 v[72:75], %[vr] offset:2048\n\t"
      "v_mov_b32 v56, v58\n\t"
      "s_waitcnt lgkmcnt(7)\n\t"
      "v_mfma_f32_16x16x32_bf16 v[136:139], a[100:103], v[176:179], v[136:139]\n\t"
      "v_mov_b32 v57, v59\n\t"
      "v_mfma_f32_16x16x32_bf16 v[140:143], a[108:111], v[176:179], v[140:143]\n\t"
      "ds_read_b128 v[76:79], %[vr] offset:3072\n\t"
      "s_waitcnt lgkmcnt(7)\n\t"
      "v_mfma_f32_16x16x32_bf16 v[136:139], a[112:115], v[180:183], v[136:139]\n\t"
      "v_mfma_f32_16x16x32_bf16 v[140:143], a[120:123], v[180:183], v[140:143]\n\t"
      "s_add_u32 s69, s69, 36864\n\t"
      "s_cmp_eq_u32 s69, 110592\n\t"
      "s_cselect_b32 s69, 0, s69\n\t"
      "s_waitcnt lgkmcnt(6)\n\t"
      "v_mfma_f32_16x16x32_bf16 v[136:139], a[116:119], v[184:187], v[136:139]\n\t"
      "v_add_u32 v58, s69, %[ra0]\n\t"
      "v_mfma_f32_16x16x32_bf16 v[140:143], a[124:127], v[184:187], v[140:143]\n\t"
      "v_add_u32 v59, s69, %[ra1]\n\t"
      "s_waitcnt lgkmcnt(5)\n\t"
      "v_mfma_f32_16x16x32_bf16 v[136:139], a[128:131], v[188:191], v[136:139]\n\t"
      "v_mfma_f32_16x16x32_bf16 v[140:143], a[136:139], v[188:191], v[140:143]\n\t"
      "s_waitcnt lgkmcnt(4)\n\t"
      "v_mfma_f32_16x16x32_bf16 v[136:139], a[132:135], v[192:195], v[136:139]\n\t"
      "v_mfma_f32_16x16x32_bf16 v[140:143], a[140:143], v[192:195], v[140:143]\n\t"
      "v_mov_b32 v61, s68\n\t"
      "s_waitcnt lgkmcnt(3)\n\t"
      "v_mfma_f32_16x16x32_bf16 v[96:99], v[64:67], a[144:147], 0\n\t"
      "v_mfma_f32_16x16x32_bf16 v[100:103], v[64:67], a[160:163], 0\n\t"
      "s_mul_i32 s76, s68, 1179648\n\t"
      "s_add_u32 s76, s76, %[z0]\n\t"
      "s_add_u32 s77, s76, 0x100000\n\t"
      "s_waitcnt lgkmcnt(2)\n\t"
      "v_mfma_f32_16x16x32_bf16 v[96:99], v[68:71], a[148:151], v[96:99]\n\t"
      "v_mfma_f32_16x16x32_bf16 v[100:103], v[68:71], a[164:167], v[100:103]\n\t"
      "s_waitcnt lgkmcnt(1)\n\t"
      "v_mfma_f32_16x16x32_bf16 v[96:99], v[72:75], a[152:155], v[96:99]\n\t"
      "v_mfma_f32_16x16x32_bf16 v[100:103], v[72:75], a[168:171], v[100:103]\n\t"
      "s_waitcnt lgkmcnt(0)\n\t"
      "v_mfma_f32_16x16x32_bf16 v[96:99], v[76:79], a[156:159], v[96:99]\n\t"
      "v_mfma_f32_16x16x32_bf16 v[100:103], v[76:79], a[172:175], v[100:103]\n\t"
      "v_cvt_pk_bf16_f32 v148, v136, v137\n\t"
      "v_mfma_f32_16x16x32_bf16 v[104:107], v[64:67], a[176:179], 0\n\t"
      "v_cvt_pk_bf16_f32 v149, v138, v139\n\t"
      "v_mfma_f32_16x16x32_bf16 v[108:111], v[64:67], a[192:195], 0\n\t"
      "v_cvt_pk_bf16_f32 v150, v140, v141\n\t"
      "v_mfma_f32_16x16x32_bf16 v[104:107], v[68:71], a[180:183], v[104:107]\n\t"
      "v_cvt_pk_bf16_f32 v151, v142, v143\n\t"
      "v_mfma_f32_16x16x32_bf16 v[108:111], v[68:71], a[196:199], v[108:111]\n\t"
      "ds_write_b128 %[vw], v[148:151] offset:4096\n\t"
      "v_mfma_f32_16x16x32_bf16 v[104:107], v[72:75], a[184:187], v[104:107]\n\t"
      "v_mfma_f32_16x16x32_bf16 v[108:111], v[72:75], a[200:203], v[108:111]\n\t"
      "v_mfma_f32_16x16x32_bf16 v[104:107], v[76:79], a[188:191], v[104:107]\n\t"
      "v_cvt_pk_bf16_f32 v196, v96, v97\n\t"
      "v_mfma_f32_16x16x32_bf16 v[108:111], v[76:79], a[204:207], v[108:111]\n\t"
      "v_cvt_pk_bf16_f32 v197, v98, v99\n\t"
      "v_mfma_f32_16x16x32_bf16 v[120:123], v[64:67], a[208:211], 0\n\t"
      "v_mfma_f32_16x16x32_bf16 v[124:127], v[64:67], a[224:227], 0\n\t"
      "v_cvt_pk_bf16_f32 v200, v100, v101\n\t"
      "v_mfma_f32_16x16x32_bf16 v[120:123], v[68:71], a[212:215], v[120:123]\n\t"
      "v_cvt_pk_bf16_f32 v201, v102, v103\n\t"
      "v_mfma_f32_16x16x32_bf16 v[124:127], v[68:71], a[228:231], v[124:127]\n\t"
      "s_waitcnt vmcnt(10) lgkmcnt(0)\n\t"
      "s_barrier\n\t"
      "v_mfma_f32_16x16x32_bf16 v[120:123], v[72:75], a[216:219], v[120:123]\n\t"
      "v_mfma_f32_16x16x32_bf16 v[124:127], v[72:75], a[232:235], v[124:127]\n\t"
      "s_mov_b32 m0, s70\n\t"
      "s_nop 0\n\t"
      "buffer_load_dwordx4 %[xv], %[xsrd], s71 offen lds\n\t"
      "ds_read_b128 v[80:83], %[vr] offset:4096\n\t"
      "v_mfma_f32_16x16x32_bf16 v[120:123], v[76:79], a[220:223], v[120:123]\n\t"
      "v_cvt_pk_bf16_f32 v204, v104, v105\n\t"
      "v_mfma_f32_16x16x32_bf16 v[124:127], v[76:79], a[236:239], v[124:127]\n\t"
      "ds_read_b128 v[84:87], %[vr] offset:5120\n\t"
      "v_cvt_pk_bf16_f32 v205, v106, v107\n\t"
      "v_mfma_f32_16x16x32_bf16 v[96:99], v[64:67], a[240:243], 0\n\t"
      "v_mfma_f32_16x16x32_bf16 v[100:103], v[64:67], v[224:227], 0\n\t"
      "s_add_u32 m0, m0, 3968\n\t"
      "s_nop 0\n\t"
      "buffer_load_dwordx4 %[xv], %[xsrd], s71 offen offset:128 lds\n\t"
      "ds_read_b128 v[88:91], %[vr] offset:6144\n\t"
      "v_cvt_pk_bf16_f32 v208, v108, v109\n\t"
      "v_mfma_f32_16x16x32_bf16 v[96:99], v[68:71], a[244:247], v[96:99]\n\t"
      "v_cvt_pk_bf16_f32 v209, v110, v111\n\t"
      "v_mfma_f32_16x16x32_bf16 v[100:103], v[68:71], v[228:231], v[100:103]\n\t"
      "ds_read_b128 v[92:95], %[vr] offset:7168\n\t"
      "v_mfma_f32_16x16x32_bf16 v[96:99], v[72:75], a[248:251], v[96:99]\n\t"
      "v_mfma_f32_16x16x32_bf16 v[100:103], v[72:75], v[232:235], v[100:103]\n\t"
      "s_add_u32 m0, m0, 3968\n\t"
      "s_nop 0\n\t"
      "buffer_load_dwordx4 %[xv], %[xsrd], s71 offen offset:256 lds\n\t"
      "v_mfma_f32_16x16x32_bf16 v[96:99], v[76:79], a[252:255], v[96:99]\n\t"
      "v_cvt_pk_bf16_f32 v212, v120, v121\n\t"
      "v_mfma_f32_16x16x32_bf16 v[100:103], v[76:79], v[236:239], v[100:103]\n\t"
      "v_cvt_pk_bf16_f32 v213, v122, v123\n\t"
      "v_mfma_f32_16x16x32_bf16 v[104:107], v[64:67], v[240:243], 0\n\t"
      "v_mfma_f32_16x16x32_bf16 v[104:107], v[68:71], v[244:247], v[104:107]\n\t"
      "s_add_u32 m0, m0, 3968\n\t"
      "s_nop 0\n\t"
      "buffer_load_dwordx4 %[xv], %[xsrd], s71 offen offset:384 lds\n\t"
      "v_cvt_pk_bf16_f32 v216, v124, v125\n\t"
      "v_mfma_f32_16x16x32_bf16 v[104:107], v[72:75], v[248:251], v[104:107]\n\t"
      "v_cvt_pk_bf16_f32 v217, v126, v127\n\t"
      "v_mfma_f32_16x16x32_bf16 v[104:107], v[76:79], v[252:255], v[104:107]\n\t"
      "s_waitcnt lgkmcnt(3)\n\t"
      "v_mfma_f32_16x16x32_bf16 v[108:111], v[80:83], a[144:147], 0\n\t"
      "v_mfma_f32_16x16x32_bf16 v[120:123], v[80:83], a[160:163], 0\n\t"
      "s_add_u32 m0, m0, 3968\n\t"
      "s_nop 0\n\t"
      "buffer_load_dwordx4 %[xv], %[xsrd], s71 offen offset:512 lds\n\t"
      "s_waitcnt lgkmcnt(2)\n\t"
      "v_mfma_f32_16x16x32_bf16 v[108:111], v[84:87], a[148:151], v[108:111]\n\t"
      "v_cvt_pk_bf16_f32 v220, v96, v97\n\t"
      "v_mfma_f32_16x16x32_bf16 v[120:123], v[84:87], a[164:167], v[120:123]\n\t"
      "v_cvt_pk_bf16_f32 v221, v98, v99\n\t"
      "s_waitcnt lgkmcnt(1)\n\t"
      "v_mfma_f32_16x16x32_bf16 v[108:111], v[88:91], a[152:155], v[108:111]\n\t"
      "v_mfma_f32_16x16x32_bf16 v[120:123], v[88:91], a[168:171], v[120:123]\n\t"
      "s_add_u32 m0, m0, 3968\n\t"
      "s_nop 0\n\t"
      "buffer_load_dwordx4 %[xv], %[xsrd], s71 offen offset:640 lds\n\t"
      "v_cvt_pk_bf16_f32 v112, v100, v101\n\t"
      "s_waitcnt lgkmcnt(0)\n\t"
      "v_mfma_f32_16x16x32_bf16 v[108:111], v[92:95], a[156:159], v[108:111]\n\t"
      "v_cvt_pk_bf16_f32 v113, v102, v103\n\t"
      "v_mfma_f32_16x16x32_bf16 v[120:123], v[92:95], a[172:175], v[120:123]\n\t"
      "v_mfma_f32_16x16x32_bf16 v[124:127], v[80:83], a[176:179], 0\n\t"
      "v_mfma_f32_16x16x32_bf16 v[96:99], v[80:83], a[192:195], 0\n\t"
      "s_add_u32 m0, m0, 3968\n\t"
      "s_nop 0\n\t"
      "buffer_load_dwordx4 %[xv], %[xsrd], s71 offen offset:768 lds\n\t"
      "v_cvt_pk_bf16_f32 v116, v104, v105\n\t"
      "v_mfma_f32_16x16x32_bf16 v[124:127], v[84:87], a[180:183], v[124:127]\n\t"
      "v_cvt_pk_bf16_f32 v117, v106, v107\n\t"
      "v_mfma_f32_16x16x32_bf16 v[96:99], v[84:87], a[196:199], v[96:99]\n\t"
      "v_mfma_f32_16x16x32_bf16 v[124:127], v[88:91], a[184:187], v[124:127]\n\t"
      "v_mfma_f32_16x16x32_bf16 v[96:99], v[88:91], a[200:203], v[96:99]\n\t"
      "s_add_u32 m0, m0, 3968\n\t"
      "s_nop 0\n\t"
      "buffer_load_dwordx4 %[xv], %[xsrd], s71 offen offset:896 lds\n\t"
      "v_mfma_f32_16x16x32_bf16 v[124:127], v[92:95], a[188:191], v[124:127]\n\t"
      "v_cvt_pk_bf16_f32 v198, v108, v109\n\t"
      "v_mfma_f32_16x16x32_bf16 v[96:99], v[92:95], a[204:207], v[96:99]\n\t"
      "v_cvt_pk_bf16_f32 v199, v110, v111\n\t"
      "v_mfma_f32_16x16x32_bf16 v[100:103], v[80:83], a[208:211], 0\n\t"
      "v_mfma_f32_16x16x32_bf16 v[104:107], v[80:83], a[224:227], 0\n\t"
      "s_add_u32 m0, m0, 3968\n\t"
      "s_nop 0\n\t"
      "buffer_load_dwordx4 %[xv], %[xsrd], s71 offen offset:1024 lds\n\t"
      "v_cvt_pk_bf16_f32 v202, v120, v121\n\t"
      "v_mfma_f32_16x16x32_bf16 v[100:103], v[84:87], a[212:215], v[100:103]\n\t"
      "s_waitcnt vmcnt(9)\n\t"
      "s_barrier\n\t"
      "buffer_store_dword v61, %[fo], %[srd], 0 offen sc1\n\t"
      "v_cvt_pk_bf16_f32 v203, v122, v123\n\t"
      "v_mfma_f32_16x16x32_bf16 v[104:107], v[84:87], a[228:231], v[104:107]\n\t"
      "v_mfma_f32_16x16x32_bf16 v[100:103], v[88:91], a[216:219], v[100:103]\n\t"
      "v_mfma_f32_16x16x32_bf16 v[104:107], v[88:91], a[232:235], v[104:107]\n\t"
      "ds_read_b128 v[160:163], v56 offset:0\n\t"
      "buffer_store_dwordx4 v[196:199], %[zo], %[srd], s76 offen sc1\n\t"
      "s_add_u32 s76, s76, 0x20000\n\t"
      "v_mfma_f32_16x16x32_bf16 v[100:103], v[92:95], a[220:223], v[100:103]\n\t"
      "s_add_u32 s70, s70, 36864\n\t"
      "s_sub_u32 s74, s70, 110592\n\t"
      "s_cselect_b32 s70, s70, s74\n\t"
      "v_cvt_pk_bf16_f32 v206, v124, v125\n\t"
      "v_mfma_f32_16x16x32_bf16 v[104:107], v[92:95], a[236:239], v[104:107]\n\t"
      "ds_read_b128 v[164:167], v57 offset:0\n\t"
      "s_add_u32 s71, s71, 36864\n\t"
      "v_cvt_pk_bf16_f32 v207, v126, v127\n\t"
      "v_mfma_f32_16x16x32_bf16 v[108:111], v[80:83], a[240:243], 0\n\t"
      "v_mfma_f32_16x16x32_bf16 v[120:123], v[80:83], v[224:227], 0\n\t"
      "ds_read_b128 v[168:171], v56 offset:4096\n\t"
      "v_cvt_pk_bf16_f32 v210, v96, v97\n\t"
      "v_mfma_f32_16x16x32_bf16 v[108:111], v[84:87], a[244:247], v[108:111]\n\t"
      "v_cvt_pk_bf16_f32 v211, v98, v99\n\t"
      "v_mfma_f32_16x16x32_bf16 v[120:123], v[84:87], v[228:231], v[120:123]\n\t"
      "ds_read_b128 v[172:175], v57 offset:4096\n\t"
      "v_mfma_f32_16x16x32_bf16 v[108:111], v[88:91], a[248:251], v[108:111]\n\t"
      "v_mfma_f32_16x16x32_bf16 v[120:123], v[88:91], v[232:235], v[120:123]\n\t"
      "ds_read_b128 v[176:179], v56 offset:8192\n\t"
      "buffer_store_dwordx4 v[200:203], %[zo], %[srd], s76 offen sc1\n\t"
      "s_add_u32 s76, s76, 0x20000\n\t"
      "v_mfma_f32_16x16x32_bf16 v[108:111], v[92:95], a[252:255], v[108:111]\n\t"
      "v_cvt_pk_bf16_f32 v214, v100, v101\n\t"
      "v_mfma_f32_16x16x32_bf16 v[120:123], v[92:95], v[236:239], v[120:123]\n\t"
      "ds_read_b128 v[180:183], v57 offset:8192\n\t"
      "v_cvt_pk_bf16_f32 v215, v102, v103\n\t"
      "v_mfma_f32_16x16x32_bf16 v[124:127], v[80:83], v[240:243], 0\n\t"
      "v_mfma_f32_16x16x32_bf16 v[124:127], v[84:87], v[244:247], v[124:127]\n\t"
      "ds_read_b128 v[184:187], v56 offset:12288\n\t"
      "v_cvt_pk_bf16_f32 v218, v104, v105\n\t"
      "v_mfma_f32_16x16x32_bf16 v[124:127], v[88:91], v[248:251], v[124:127]\n\t"
      "v_cvt_pk_bf16_f32 v219, v106, v107\n\t"
      "v_mfma_f32_16x16x32_bf16 v[124:127], v[92:95], v[252:255], v[124:127]\n\t"
      "s_add_u32 s68, s68, 1\n\t"
      "s_cmp_lt_u32 s68, 64\n\t"
      "s_cbranch_scc1 10b\n\t"
      "s_nop 7\n\t"
      "buffer_store_dwordx4 v[204:207], %[zo], %[srd], s76 offen sc1\n\t"
      "s_add_u32 s76, s76, 0x20000\n\t"
      "v_cvt_pk_bf16_f32 v222, v108, v109\n\t"
      "v_cvt_pk_bf16_f32 v223, v110, v111\n\t"
      "v_cvt_pk_bf16_f32 v114, v120, v121\n\t"
      "v_cvt_pk_bf16_f32 v115, v122, v123\n\t"
      "buffer_store_dwordx4 v[208:211], %[zo], %[srd], s76 offen sc1\n\t"
      "s_add_u32 s76, s76, 0x20000\n\t"
      "v_cvt_pk_bf16_f32 v118, v124, v125\n\t"
      "v_cvt_pk_bf16_f32 v119, v126, v127\n\t"
      "buffer_store_dwordx4 v[212:215], %[zo], %[srd], s76 offen sc1\n\t"
      "s_add_u32 s76, s76, 0x20000\n\t"
      "buffer_store_dwordx4 v[216:219], %[zo], %[srd], s76 offen sc1\n\t"
      "s_add_u32 s76, s76, 0x20000\n\t"
      "buffer_store_dwordx4 v[220:223], %[zo], %[srd], s76 offen sc1\n\t"
      "s_add_u32 s76, s76, 0x20000\n\t"
      "buffer_store_dwordx4 v[112:115], %[zo], %[srd], s76 offen sc1\n\t"
      "s_add_u32 s76, s76, 0x20000\n\t"
      "buffer_store_dwordx4 v[116:119], %[zo], %[srd], s77 offen sc1\n\t"
      "s_add_u32 s76, s76, 0x20000\n\t"
      "s_mov_b32 m0, s73\n\t"
      "s_waitcnt vmcnt(0) lgkmcnt(0)\n\t"
      "s_branch 37f\n\t"
      "34:\n\t"
      "s_mov_b32 s84, s76\n\t"
      "buffer_store_dwordx4 v[212:215], %[zo], %[srd], s84 offen sc1\n\t"
      "s_add_u32 s84, s84, 0x20000\n\t"
      "buffer_store_dwordx4 v[216:219], %[zo], %[srd], s84 offen sc1\n\t"
      "s_add_u32 s84, s84, 0x20000\n\t"
      "buffer_store_dwordx4 v[220:223], %[zo], %[srd], s84 offen sc1\n\t"
      "s_add_u32 s84, s84, 0x20000\n\t"
      "buffer_store_dwordx4 v[112:115], %[zo], %[srd], s84 offen sc1\n\t"
      "s_add_u32 s84, s84, 0x20000\n\t"
      "buffer_store_dwordx4 v[116:119], %[zo], %[srd], s77 offen sc1\n\t"
      "s_add_u32 s84, s84, 0x20000\n\t"
      "v_mov_b32 v61, s68\n\t"
      "s_waitcnt vmcnt(0)\n\t"
      "s_barrier\n\t"
      "buffer_store_dword v61, %[fo], %[srd], 0 offen sc1\n\t"
      "s_branch 35b\n\t"
      "37:\n\t"
      :: [ra0] "v"(ra0), [ra1] "v"(ra1), [wl] "s"(US(wl)), [z0] "s"(US(z0)), [xv] "v"(xv), [xsrd] "s"(xsrd), [fo] "v"(fo), [srd] "s"(srd),
         [zo] "v"(zo), [vw] "v"(vw), [vr] "v"(vr_), WPIN(W)
      : "v56", "v57", "v58", "v59", "v60", "v61", "v62", "v63", "v64", "v65", "v66", "v67", "v68", "v69", "v70", "v71", "v72", "v73", "v74", "v75", "v76", "v77", "v78", "v79", "v80", "v81", "v82", "v83", "v84", "v85", "v86", "v87", "v88", "v89", "v90", "v91", "v92", "v93", "v94", "v95", "v96", "v97", "v98", "v99", "v100", "v101", "v102", "v103", "v104", "v105", "v106", "v107", "v108", "v109", "v110", "v111", "v112", "v113", "v114", "v115", "v116", "v117", "v118", "v119", "v120", "v121", "v122", "v123", "v124", "v125", "v126", "v127", "v128", "v129", "v130", "v131", "v132", "v133", "v134", "v135", "v136", "v137", "v138", "v139", "v140", "v141", "v142", "v143", "v144", "v145", "v146", "v147", "v148", "v149", "v150", "v151", "v152", "v153", "v154", "v155", "v156", "v157", "v158", "v159", "v160", "v161", "v162", "v163", "v164", "v165", "v166", "v167", "v168", "v169", "v170", "v171", "v172", "v173", "v174", "v175", "v176", "v177", "v178", "v179", "v180", "v181", "v182", "v183", "v184", "v185", "v186", "v187", "v188", "v189", "v190", "v191", "v192", "v193", "v194", "v195", "v196", "v197", "v198", "v199", "v200", "v201", "v202", "v203", "v204", "v205", "v206", "v207", "v208", "v209", "v210", "v211", "v212", "v213", "v214", "v215", "v216", "v217", "v218", "v219", "v220", "v221", "v222", "v223", "s64", "s65", "s66", "s67", "s68", "s69", "s70", "s71", "s72", "s73", "s74", "s75", "s76", "s77", "s78", "s79", "s80", "s81", "s82", "s83", "s84", "s85", "s86", "s87", "s88", "s89", "s90", "s91", "s92", "s93", "s94", "s95", "scc", "memory");
  (void)get_par(p, mk_tag(p, e), 0);
  wait_vm<0>(); bar();
  st4(NSEG, rs, fo, 0);
}

DI void role_F2(const Params& p, int id, u64 tag) {
  const int tid = threadIdx.x, lane = tid & 63, w = __builtin_amdgcn_readfirstlane(tid >> 6);
  const int fr = id / 3, cr = id % 3;
  u32v W[6];
  {
    const u4 s1 = mksrd(p.w2 + (size_t)fr * 512 * 576 + cr * 192, 511 * 1152 + 384);
    const u32 wa = psrc(1152, false, 0, w, lane), wb = psrc(1152, false, 1, w, lane), pa = psrc(1152, true, 0, w, lane), pb = psrc(1152, true, 1, w, lane);
    const u32 u0 = ptile<2>(16 * w, 0, lane), u1 = ptile<2>(64 + 16 * w, 0, lane), lb = PBASE + w * 4096;
    asm volatile(
      "s_mov_b32 s65, m0\n\t"
      "s_add_u32 m0, %[lb], 0\n\t"
      "s_mov_b32 s64, 0\n\t"
      "buffer_load_dwordx4 %[wa], %[m1], s64 offen lds\n\t"
      "s_add_u32 m0, %[lb], 1024\n\t"
      "s_mov_b32 s64, 4608\n\t"
      "buffer_load_dwordx4 %[wa], %[m1], s64 offen lds\n\t"
      "s_add_u32 m0, %[lb], 2048\n\t"
      "s_mov_b32 s64, 9216\n\t"
      "buffer_load_dwordx4 %[wb], %[m1], s64 offen lds\n\t"
      "s_add_u32 m0, %[lb], 3072\n\t"
      "s_mov_b32 s64, 13824\n\t"
      "buffer_load_dwordx4 %[wb], %[m1], s64 offen lds\n\t"
      "s_add_u32 m0, %[lb], 16384\n\t"
      "s_mov_b32 s64, 73728\n\t"
      "buffer_load_dwordx4 %[wa], %[m1], s64 offen lds\n\t"
      "s_add_u32 m0, %[lb], 17408\n\t"
      "s_mov_b32 s64, 78336\n\t"
      "buffer_load_dwordx4 %[wa], %[m1], s64 offen lds\n\t"
      "s_add_u32 m0, %[lb], 18432\n\t"
      "s_mov_b32 s64, 82944\n\t"
      "buffer_load_dwordx4 %[wb], %[m1], s64 offen lds\n\t"
      "s_add_u32 m0, %[lb], 19456\n\t"
      "s_mov_b32 s64, 87552\n\t"
      "buffer_load_dwordx4 %[wb], %[m1], s64 offen lds\n\t"
      "s_add_u32 m0, %[lb], 32768\n\t"
      "s_mov_b32 s64, 147456\n\t"
      "buffer_load_dwordx4 %[wa], %[m1], s64 offen lds\n\t"
      "s_add_u32 m0, %[lb], 33792\n\t"
      "s_mov_b32 s64, 152064\n\t"
      "buffer_load_dwordx4 %[wa], %[m1], s64 offen lds\n\t"
      "s_add_u32 m0, %[lb], 34816\n\t"
      "s_mov_b32 s64, 156672\n\t"
      "buffer_load_dwordx4 %[wb], %[m1], s64 offen lds\n\t"
      "s_add_u32 m0, %[lb], 35840\n\t"
      "s_mov_b32 s64, 161280\n\t"
      "buffer_load_dwordx4 %[wb], %[m1], s64 offen lds\n\t"
      "s_waitcnt vmcnt(8) lgkmcnt(0)\n\t"
      "s_barrier\n\t"
      "s_add_u32 m0, %[lb], 49152\n\t"
      "s_mov_b32 s64, 221184\n\t"
      "buffer_load_dwordx4 %[wa], %[m1], s64 offen lds\n\t"
      "s_add_u32 m0, %[lb], 50176\n\t"
      "s_mov_b32 s64, 225792\n\t"
      "buffer_load_dwordx4 %[wa], %[m1], s64 offen lds\n\t"
      "s_add_u32 m0, %[lb], 51200\n\t"
      "s_mov_b32 s64, 230400\n\t"
      "buffer_load_dwordx4 %[wb], %[m1], s64 offen lds\n\t"
      "s_add_u32 m0, %[lb], 52224\n\t"
      "s_mov_b32 s64, 235008\n\t"
      "buffer_load_dwordx4 %[wb], %[m1], s64 offen lds\n\t"
      "ds_read_b64_tr_b16 a[0:1], %[u0] offset:0\n\t"
      "ds_read_b64_tr_b16 a[2:3], %[u0] offset:4096\n\t"
      "ds_read_b64_tr_b16 a[4:5], %[u0] offset:8192\n\t"
      "ds_read_b64_tr_b16 a[6:7], %[u0] offset:12288\n\t"
      "ds_read_b64_tr_b16 a[8:9], %[u1] offset:0\n\t"
      "ds_read_b64_tr_b16 a[10:11], %[u1] offset:4096\n\t"
      "ds_read_b64_tr_b16 a[12:13], %[u1] offset:8192\n\t"
      "ds_read_b64_tr_b16 a[14:15], %[u1] offset:12288\n\t"
      "s_waitcnt vmcnt(8) lgkmcnt(0)\n\t"
      "s_barrier\n\t"
      "s_add_u32 m0, %[lb], 0\n\t"
      "s_mov_b32 s64, 294912\n\t"
      "buffer_load_dwordx4 %[wa], %[m1], s64 offen lds\n\t"
      "s_add_u32 m0, %[lb], 1024\n\t"
      "s_mov_b32 s64, 299520\n\t"
      "buffer_load_dwordx4 %[wa], %[m1], s64 offen lds\n\t"
      "s_add_u32 m0, %[lb], 2048\n\t"
      "s_mov_b32 s64, 304128\n\t"
      "buffer_load_dwordx4 %[wb], %[m1], s64 offen lds\n\t"
      "s_add_u32 m0, %[lb], 3072\n\t"
      "s_mov_b32 s64, 308736\n\t"
      "buffer_load_dwordx4 %[wb], %[m1], s64 offen lds\n\t"
      "ds_read_b64_tr_b16 a[24:25], %[u0] offset:16384\n\t"
      "ds_read_b64_tr_b16 a[26:27], %[u0] offset:20480\n\t"
      "ds_read_b64_tr_b16 a[28:29], %[u0] offset:24576\n\t"
      "ds_read_b64_tr_b16 a[30:31], %[u0] offset:28672\n\t"
      "ds_read_b64_tr_b16 a[32:33], %[u1] offset:16384\n\t"
      "ds_read_b64_tr_b16 a[34:35], %[u1] offset:20480\n\t"
      "ds_read_b64_tr_b16 a[36:37], %[u1] offset:24576\n\t"
      "ds_read_b64_tr_b16 a[38:39], %[u1] offset:28672\n\t"
      "s_waitcnt vmcnt(8) lgkmcnt(0)\n\t"
      "s_barrier\n\t"
      "s_add_u32 m0, %[lb], 16384\n\t"
      "s_mov_b32 s64, 368640\n\t"
      "buffer_load_dwordx4 %[wa], %[m1], s64 offen lds\n\t"
      "s_add_u32 m0, %[lb], 17408\n\t"
      "s_mov_b32 s64, 373248\n\t"
      "buffer_load_dwordx4 %[wa], %[m1], s64 offen lds\n\t"
      "s_add_u32 m0, %[lb], 18432\n\t"
      "s_mov_b32 s64, 377856\n\t"
      "buffer_load_dwordx4 %[wb], %[m1], s64 offen lds\n\t"
      "s_add_u32 m0, %[lb], 19456\n\t"
      "s_mov_b32 s64, 382464\n\t"
      "buffer_load_dwordx4 %[wb], %[m1], s64 offen lds\n\t"
      "ds_read_b64_tr_b16 a[48:49], %[u0] offset:32768\n\t"
      "ds_read_b64_tr_b16 a[50:51], %[u0] offset:36864\n\t"
      "ds_read_b64_tr_b16 a[52:53], %[u0] offset:40960\n\t"
      "ds_read_b64_tr_b16 a[54:55], %[u0] offset:45056\n\t"
      "ds_read_b64_tr_b16 a[56:57], %[u1] offset:32768\n\t"
      "ds_read_b64_tr_b16 a[58:59], %[u1] offset:36864\n\t"
      "ds_read_b64_tr_b16 a[60:61], %[u1] offset:40960\n\t"
      "ds_read_b64_tr_b16 a[62:63], %[u1] offset:45056\n\t"
      "s_waitcnt vmcnt(8) lgkmcnt(0)\n\t"
      "s_barrier\n\t"
      "s_add_u32 m0, %[lb], 32768\n\t"
      "s_mov_b32 s64, 442368\n\t"
      "buffer_load_dwordx4 %[wa], %[m1], s64 offen lds\n\t"
      "s_add_u32 m0, %[lb], 33792\n\t"
      "s_mov_b32 s64, 446976\n\t"
      "buffer_load_dwordx4 %[wa], %[m1], s64 offen lds\n\t"
      "s_add_u32 m0, %[lb], 34816\n\t"
      "s_mov_b32 s64, 451584\n\t"
      "buffer_load_dwordx4 %[wb], %[m1], s64 offen lds\n\t"
      "s_add_u32 m0, %[lb], 35840\n\t"
      "s_mov_b32 s64, 456192\n\t"
      "buffer_load_dwordx4 %[wb], %[m1], s64 offen lds\n\t"
      "ds_read_b64_tr_b16 a[72:73], %[u0] offset:49152\n\t"
      "ds_read_b64_tr_b16 a[74:75], %[u0] offset:53248\n\t"
      "ds_read_b64_tr_b16 a[76:77], %[u0] offset:57344\n\t"
      "ds_read_b64_tr_b16 a[78:79], %[u0] offset:61440\n\t"
      "ds_read_b64_tr_b16 a[80:81], %[u1] offset:49152\n\t"
      "ds_read_b64_tr_b16 a[82:83], %[u1] offset:53248\n\t"
      "ds_read_b64_tr_b16 a[84:85], %[u1] offset:57344\n\t"
      "ds_read_b64_tr_b16 a[86:87], %[u1] offset:61440\n\t"
      "s_waitcnt vmcnt(8) lgkmcnt(0)\n\t"
      "s_barrier\n\t"
      "s_add_u32 m0, %[lb], 49152\n\t"
      "s_mov_b32 s64, 516096\n\t"
      "buffer_load_dwordx4 %[wa], %[m1], s64 offen lds\n\t"
      "s_add_u32 m0, %[lb], 50176\n\t"
      "s_mov_b32 s64, 520704\n\t"
      "buffer_load_dwordx4 %[wa], %[m1], s64 offen lds\n\t"
      "s_add_u32 m0, %[lb], 51200\n\t"
      "s_mov_b32 s64, 525312\n\t"
      "buffer_load_dwordx4 %[wb], %[m1], s64 offen lds\n\t"
      "s_add_u32 m0, %[lb], 52224\n\t"
      "s_mov_b32 s64, 529920\n\t"
      "buffer_load_dwordx4 %[wb], %[m1], s64 offen lds\n\t"
      "ds_read_b64_tr_b16 a[96:97], %[u0] offset:0\n\t"
      "ds_read_b64_tr_b16 a[98:99], %[u0] offset:4096\n\t"
      "ds_read_b64_tr_b16 a[100:101], %[u0] offset:8192\n\t"
      "ds_read_b64_tr_b16 a[102:103], %[u0] offset:12288\n\t"
      "ds_read_b64_tr_b16 a[104:105], %[u1] offset:0\n\t"
      "ds_read_b64_tr_b16 a[106:107], %[u1] offset:4096\n\t"
      "ds_read_b64_tr_b16 a[108:109], %[u1] offset:8192\n\t"
      "ds_read_b64_tr_b16 a[110:111], %[u1] offset:12288\n\t"
      "s_waitcnt vmcnt(8) lgkmcnt(0)\n\t"
      "s_barrier\n\t"
      "s_add_u32 m0, %[lb], 0\n\t"
      "s_mov_b32 s64, 256\n\t"
      "buffer_load_dwordx4 %[pa], %[m1], s64 offen lds\n\t"
      "s_add_u32 m0, %[lb], 1024\n\t"
      "s_mov_b32 s64, 4864\n\t"
      "buffer_load_dwordx4 %[pa], %[m1], s64 offen lds\n\t"
      "s_add_u32 m0, %[lb], 2048\n\t"
      "s_mov_b32 s64, 9472\n\t"
      "buffer_load_dwordx4 %[pb], %[m1], s64 offen lds\n\t"
      "s_add_u32 m0, %[lb], 3072\n\t"
      "s_mov_b32 s64, 14080\n\t"
      "buffer_load_dwordx4 %[pb], %[m1], s64 offen lds\n\t"
      "ds_read_b64_tr_b16 a[120:121], %[u0] offset:16384\n\t"
      "ds_read_b64_tr_b16 a[122:123], %[u0] offset:20480\n\t"
      "ds_read_b64_tr_b16 a[124:125], %[u0] offset:24576\n\t"
      "ds_read_b64_tr_b16 a[126:127], %[u0] offset:28672\n\t"
      "ds_read_b64_tr_b16 a[128:129], %[u1] offset:16384\n\t"
      "ds_read_b64_tr_b16 a[130:131], %[u1] offset:20480\n\t"
      "ds_read_b64_tr_b16 a[132:133], %[u1] offset:24576\n\t"
      "ds_read_b64_tr_b16 a[134:135], %[u1] offset:28672\n\t"
      "s_waitcnt vmcnt(8) lgkmcnt(0)\n\t"
      "s_barrier\n\t"
      "s_add_u32 m0, %[lb], 16384\n\t"
      "s_mov_b32 s64, 147712\n\t"
      "buffer_load_dwordx4 %[pa], %[m1], s64 offen lds\n\t"
      "s_add_u32 m0, %[lb], 17408\n\t"
      "s_mov_b32 s64, 152320\n\t"
      "buffer_load_dwordx4 %[pa], %[m1], s64 offen lds\n\t"
      "s_add_u32 m0, %[lb], 18432\n\t"
      "s_mov_b32 s64, 156928\n\t"
      "buffer_load_dwordx4 %[pb], %[m1], s64 offen lds\n\t"
      "s_add_u32 m0, %[lb], 19456\n\t"
      "s_mov_b32 s64, 161536\n\t"
      "buffer_load_dwordx4 %[pb], %[m1], s64 offen lds\n\t"
      "ds_read_b64_tr_b16 a[144:145], %[u0] offset:32768\n\t"
      "ds_read_b64_tr_b16 a[146:147], %[u0] offset:36864\n\t"
      "ds_read_b64_tr_b16 a[148:149], %[u0] offset:40960\n\t"
      "ds_read_b64_tr_b16 a[150:151], %[u0] offset:45056\n\t"
      "ds_read_b64_tr_b16 a[152:153], %[u1] offset:32768\n\t"
      "ds_read_b64_tr_b16 a[154:155], %[u1] offset:36864\n\t"
      "ds_read_b64_tr_b16 a[156:157], %[u1] offset:40960\n\t"
      "ds_read_b64_tr_b16 a[158:159], %[u1] offset:45056\n\t"
      "s_waitcnt vmcnt(8) lgkmcnt(0)\n\t"
      "s_barrier\n\t"
      "s_add_u32 m0, %[lb], 32768\n\t"
      "s_mov_b32 s64, 295168\n\t"
      "buffer_load_dwordx4 %[pa], %[m1], s64 offen lds\n\t"
      "s_add_u32 m0, %[lb], 33792\n\t"
      "s_mov_b32 s64, 299776\n\t"
      "buffer_load_dwordx4 %[pa], %[m1], s64 offen lds\n\t"
      "s_add_u32 m0, %[lb], 34816\n\t"
      "s_mov_b32 s64, 304384\n\t"
      "buffer_load_dwordx4 %[pb], %[m1], s64 offen lds\n\t"
      "s_add_u32 m0, %[lb], 35840\n\t"
      "s_mov_b32 s64, 308992\n\t"
      "buffer_load_dwordx4 %[pb], %[m1], s64 offen lds\n\t"
      "ds_read_b64_tr_b16 a[168:169], %[u0] offset:49152\n\t"
      "ds_read_b64_tr_b16 a[170:171], %[u0] offset:53248\n\t"
      "ds_read_b64_tr_b16 a[172:173], %[u0] offset:57344\n\t"
      "ds_read_b64_tr_b16 a[174:175], %[u0] offset:61440\n\t"
      "ds_read_b64_tr_b16 a[176:177], %[u1] offset:49152\n\t"
      "ds_read_b64_tr_b16 a[178:179], %[u1] offset:53248\n\t"
      "ds_read_b64_tr_b16 a[180:181], %[u1] offset:57344\n\t"
      "ds_read_b64_tr_b16 a[182:183], %[u1] offset:61440\n\t"
      "s_waitcnt vmcnt(8) lgkmcnt(0)\n\t"
      "s_barrier\n\t"
      "s_add_u32 m0, %[lb], 49152\n\t"
      "s_mov_b32 s64, 442624\n\t"
      "buffer_load_dwordx4 %[pa], %[m1], s64 offen lds\n\t"
      "s_add_u32 m0, %[lb], 50176\n\t"
      "s_mov_b32 s64, 447232\n\t"
      "buffer_load_dwordx4 %[pa], %[m1], s64 offen lds\n\t"
      "s_add_u32 m0, %[lb], 51200\n\t"
      "s_mov_b32 s64, 451840\n\t"
      "buffer_load_dwordx4 %[pb], %[m1], s64 offen lds\n\t"
      "s_add_u32 m0, %[lb], 52224\n\t"
      "s_mov_b32 s64, 456448\n\t"
      "buffer_load_dwordx4 %[pb], %[m1], s64 offen lds\n\t"
      "ds_read_b64_tr_b16 a[16:17], %[u0] offset:0\n\t"
      "ds_read_b64_tr_b16 a[18:19], %[u0] offset:4096\n\t"
      "ds_read_b64_tr_b16 a[20:21], %[u0] offset:8192\n\t"
      "ds_read_b64_tr_b16 a[22:23], %[u0] offset:12288\n\t"
      "ds_read_b64_tr_b16 a[40:41], %[u1] offset:0\n\t"
      "ds_read_b64_tr_b16 a[42:43], %[u1] offset:4096\n\t"
      "ds_read_b64_tr_b16 a[44:45], %[u1] offset:8192\n\t"
      "ds_read_b64_tr_b16 a[46:47], %[u1] offset:12288\n\t"
      "s_waitcnt vmcnt(8) lgkmcnt(0)\n\t"
      "s_barrier\n\t"
      "ds_read_b64_tr_b16 a[64:65], %[u0] offset:16384\n\t"
      "ds_read_b64_tr_b16 a[66:67], %[u0] offset:20480\n\t"
      "ds_read_b64_tr_b16 a[68:69], %[u0] offset:24576\n\t"
      "ds_read_b64_tr_b16 a[70:71], %[u0] offset:28672\n\t"
      "ds_read_b64_tr_b16 a[88:89], %[u1] offset:16384\n\t"
      "ds_read_b64_tr_b16 a[90:91], %[u1] offset:20480\n\t"
      "ds_read_b64_tr_b16 a[92:93], %[u1] offset:24576\n\t"
      "ds_read_b64_tr_b16 a[94:95], %[u1] offset:28672\n\t"
      "s_waitcnt vmcnt(4) lgkmcnt(0)\n\t"
      "s_barrier\n\t"
      "ds_read_b64_tr_b16 a[112:113], %[u0] offset:32768\n\t"
      "ds_read_b64_tr_b16 a[114:115], %[u0] offset:36864\n\t"
      "ds_read_b64_tr_b16 a[116:117], %[u0] offset:40960\n\t"
      "ds_read_b64_tr_b16 a[118:119], %[u0] offset:45056\n\t"
      "ds_read_b64_tr_b16 a[136:137], %[u1] offset:32768\n\t"
      "ds_read_b64_tr_b16 a[138:139], %[u1] offset:36864\n\t"
      "ds_read_b64_tr_b16 a[140:141], %[u1] offset:40960\n\t"
      "ds_read_b64_tr_b16 a[142:143], %[u1] offset:45056\n\t"
      "s_waitcnt vmcnt(0) lgkmcnt(0)\n\t"
      "s_barrier\n\t"
      "ds_read_b64_tr_b16 a[160:161], %[u0] offset:49152\n\t"
      "ds_read_b64_tr_b16 a[162:163], %[u0] offset:53248\n\t"
      "ds_read_b64_tr_b16 a[164:165], %[u0] offset:57344\n\t"
      "ds_read_b64_tr_b16 a[166:167], %[u0] offset:61440\n\t"
      "ds_read_b64_tr_b16 a[184:185], %[u1] offset:49152\n\t"
      "ds_read_b64_tr_b16 a[186:187], %[u1] offset:53248\n\t"
      "ds_read_b64_tr_b16 a[188:189], %[u1] offset:57344\n\t"
      "ds_read_b64_tr_b16 a[190:191], %[u1] offset:61440\n\t"
      "s_waitcnt lgkmcnt(0)\n\t"
      "s_barrier\n\t"
      "s_mov_b32 m0, s65\n\t"
      : WOUT6(W) : [m1] "s"(s1), [lb] "s"(US(lb)), [wa] "v"(wa), [wb] "v"(wb), [pa] "v"(pa), [pb] "v"(pb), [u0] "v"(u0), [u1] "v"(u1) : "s64", "s65", "scc", "memory");
  }
  const u32 par = get_par(p, tag, 1);
  const __amdgpu_buffer_rsrc_t rs = mkrsrc(p.ws, WS_SPAN);
  const u4 srd = mksrd(p.ws, WS_SPAN);
  const u32 l16 = lane * 16, wl = w * 1024;
  const u32 fo = tid == 0 ? ffar(100 + id, par) : OOB;
  const u32 po0 = (12 * cr + w) * 1024 + l16, po1 = po0 + 4096, po2 = po0 + 8192;
  const u32 hoff = OFF_H + (u32)((w >> 1) * 144 + 16 * fr + 8 * (w & 1)) * 1024u;
  const u32 fl = ffar(64 + 2 * fr + (lane & 1), par);
  const u32 hw = w * 8192 + l16, hb = hoff, pb = OFF_FP + (u32)fr * 36864u;
  int seen = 0;
  if (!wait_flags(rs, fl, 1, seen)) return;
  LOOPSTART
#pragma unroll
  for (int i = 0; i < 8; ++i) *(LAS u4*)(uintptr_t)(hw + i * 1024) = ld16(rs, l16 + (i & 3) * 1024, hoff + (i >> 2) * 4096);
  if (!wait_flags(rs, fl, 2, seen)) return;
  wait_vm<0>(); bar();
  asm volatile(
      "s_mov_b32 s68, 0\n\t"
      "s_mov_b32 s73, 0\n\t"
      "s_mov_b32 s69, 32768\n\t"
      "s_add_u32 s71, %[hb], 294912\n\t"
      "s_add_u32 s70, s71, 0x1000\n\t"
      "buffer_load_dwordx4 v[64:67], %[l16], %[srd], s71 offen sc1\n\t"
      "buffer_load_dwordx4 v[68:71], %[l16], %[srd], s71 offen offset:1024 sc1\n\t"
      "buffer_load_dwordx4 v[72:75], %[l16], %[srd], s71 offen offset:2048 sc1\n\t"
      "buffer_load_dwordx4 v[76:79], %[l16], %[srd], s71 offen offset:3072 sc1\n\t"
      "buffer_load_dwordx4 v[80:83], %[l16], %[srd], s70 offen sc1\n\t"
      "buffer_load_dwordx4 v[84:87], %[l16], %[srd], s70 offen offset:1024 sc1\n\t"
      "buffer_load_dwordx4 v[88:91], %[l16], %[srd], s70 offen offset:2048 sc1\n\t"
      "buffer_load_dwordx4 v[92:95], %[l16], %[srd], s70 offen offset:3072 sc1\n\t"
      "s_mov_b32 s77, 0xf0000000\n\t"
      "v_mov_b32 v56, %[l16]\n\t"
      "v_add_u32 v57, 32768, %[hw]\n\t"
      "v_mov_b32 v62, 0\n\t"
      "ds_read_b128 v[160:163], v56 offset:0\n\t"
      "ds_read_b128 v[164:167], v56 offset:1024\n\t"
      "ds_read_b128 v[168:171], v56 offset:2048\n\t"
      "ds_read_b128 v[172:175], v56 offset:3072\n\t"
      "ds_read_b128 v[176:179], v56 offset:4096\n\t"
      "ds_read_b128 v[180:183], v56 offset:5120\n\t"
      "s_waitcnt vmcnt(0) lgkmcnt(0)\n\t"
      "10:\n\t"
      "s_waitcnt lgkmcnt(5)\n\t"
      "v_mfma_f32_16x16x32_bf16 v[128:131], a[0:3], v[160:163], 0\n\t"
      "ds_read_b128 v[184:187], v56 offset:6144\n\t"
      "v_mfma_f32_16x16x32_bf16 v[136:139], a[8:11], v[160:163], 0\n\t"
      "s_waitcnt vmcnt(12)\n\t"
      "v_readlane_b32 s75, v62, 0\n\t"
      "v_readlane_b32 s79, v62, 1\n\t"
      "v_mfma_f32_16x16x32_bf16 v[144:147], a[16:19], v[160:163], 0\n\t"
      "s_add_u32 s74, s68, 3\n\t"
      "s_min_u32 s74, s74, 64\n\t"
      "s_waitcnt lgkmcnt(5)\n\t"
      "v_mfma_f32_16x16x32_bf16 v[128:131], a[4:7], v[164:167], v[128:131]\n\t"
      "ds_read_b128 v[188:191], v56 offset:7168\n\t"
      "v_mfma_f32_16x16x32_bf16 v[136:139], a[12:15], v[164:167], v[136:139]\n\t"
      "s_min_u32 s75, s75, s79\n\t"
      "s_cmp_lt_u32 s75, s74\n\t"
      "v_mfma_f32_16x16x32_bf16 v[144:147], a[20:23], v[164:167], v[144:147]\n\t"
      "s_cbranch_scc1 30f\n\t"
      "31:\n\t"
      "s_waitcnt lgkmcnt(5)\n\t"
      "v_mfma_f32_16x16x32_bf16 v[128:131], a[24:27], v[168:171], v[128:131]\n\t"
      "ds_read_b128 v[160:163], v56 offset:8192\n\t"
      "v_mfma_f32_16x16x32_bf16 v[136:139], a[32:35], v[168:171], v[136:139]\n\t"
      "s_sub_u32 s72, s74, 1\n\t"
      "buffer_load_dword v62, %[fl], %[srd], 0 offen sc1\n\t"
      "v_mfma_f32_16x16x32_bf16 v[144:147], a[40:43], v[168:171], v[144:147]\n\t"
      "v_cvt_pk_bf16_f32 v98, v132, v133\n\t"
      "s_waitcnt lgkmcnt(5)\n\t"
      "v_mfma_f32_16x16x32_bf16 v[128:131], a[28:31], v[172:175], v[128:131]\n\t"
      "ds_read_b128 v[164:167], v56 offset:9216\n\t"
      "v_cvt_pk_bf16_f32 v99, v134, v135\n\t"
      "v_mfma_f32_16x16x32_bf16 v[136:139], a[36:39], v[172:175], v[136:139]\n\t"
      "s_mul_i32 s71, s72, 294912\n\t"
      "s_add_u32 s71, s71, %[hb]\n\t"
      "s_add_u32 s70, s71, 0x1000\n\t"
      "v_cvt_pk_bf16_f32 v102, v140, v141\n\t"
      "v_mfma_f32_16x16x32_bf16 v[144:147], a[44:47], v[172:175], v[144:147]\n\t"
      "v_cvt_pk_bf16_f32 v103, v142, v143\n\t"
      "s_waitcnt lgkmcnt(5)\n\t"
      "v_mfma_f32_16x16x32_bf16 v[128:131], a[48:51], v[176:179], v[128:131]\n\t"
      "ds_read_b128 v[168:171], v56 offset:10240\n\t"
      "v_cvt_pk_bf16_f32 v106, v148, v149\n\t"
      "v_mfma_f32_16x16x32_bf16 v[136:139], a[56:59], v[176:179], v[136:139]\n\t"
      "v_cvt_pk_bf16_f32 v107, v150, v151\n\t"
      "v_mfma_f32_16x16x32_bf16 v[144:147], a[64:67], v[176:179], v[144:147]\n\t"
      "s_waitcnt lgkmcnt(5)\n\t"
      "v_mfma_f32_16x16x32_bf16 v[128:131], a[52:55], v[180:183], v[128:131]\n\t"
      "ds_read_b128 v[172:175], v56 offset:11264\n\t"
      "s_waitcnt vmcnt(10)\n\t"
      "ds_write_b128 v57, v[64:67] offset:0\n\t"
      "v_mfma_f32_16x16x32_bf16 v[136:139], a[60:63], v[180:183], v[136:139]\n\t"
      "v_mfma_f32_16x16x32_bf16 v[144:147], a[68:71], v[180:183], v[144:147]\n\t"
      "buffer_store_dwordx4 v[96:99], %[po0], %[srd], s77 offen sc1\n\t"
      "s_waitcnt lgkmcnt(6)\n\t"
      "v_mfma_f32_16x16x32_bf16 v[128:131], a[72:75], v[184:187], v[128:131]\n\t"
      "ds_read_b128 v[176:179], v56 offset:12288\n\t"
      "v_mfma_f32_16x16x32_bf16 v[136:139], a[80:83], v[184:187], v[136:139]\n\t"
      "v_mfma_f32_16x16x32_bf16 v[144:147], a[88:91], v[184:187], v[144:147]\n\t"
      "s_waitcnt lgkmcnt(6)\n\t"
      "v_mfma_f32_16x16x32_bf16 v[128:131], a[76:79], v[188:191], v[128:131]\n\t"
      "ds_read_b128 v[180:183], v56 offset:13312\n\t"
      "s_waitcnt vmcnt(9)\n\t"
      "ds_write_b128 v57, v[68:71] offset:1024\n\t"
      "v_mfma_f32_16x16x32_bf16 v[136:139], a[84:87], v[188:191], v[136:139]\n\t"
      "v_mfma_f32_16x16x32_bf16 v[144:147], a[92:95], v[188:191], v[144:147]\n\t"
      "buffer_store_dwordx4 v[100:103], %[po1], %[srd], s77 offen sc1\n\t"
      "s_waitcnt lgkmcnt(3)\n\t"
      "buffer_load_dwordx4 v[64:67], %[l16], %[srd], s71 offen sc1\n\t"
      "s_waitcnt lgkmcnt(7)\n\t"
      "v_mfma_f32_16x16x32_bf16 v[128:131], a[96:99], v[160:163], v[128:131]\n\t"
      "ds_read_b128 v[184:187], v56 offset:14336\n\t"
      "v_mfma_f32_16x16x32_bf16 v[136:139], a[104:107], v[160:163], v[136:139]\n\t"
      "v_mfma_f32_16x16x32_bf16 v[144:147], a[112:115], v[160:163], v[144:147]\n\t"
      "s_waitcnt lgkmcnt(7)\n\t"
      "v_mfma_f32_16x16x32_bf16 v[128:131], a[100:103], v[164:167], v[128:131]\n\t"
      "ds_read_b128 v[188:191], v56 offset:15360\n\t"
      "s_waitcnt vmcnt(10)\n\t"
      "ds_write_b128 v57, v[72:75] offset:2048\n\t"
      "v_mfma_f32_16x16x32_bf16 v[136:139], a[108:111], v[164:167], v[136:139]\n\t"
      "v_mfma_f32_16x16x32_bf16 v[144:147], a[116:119], v[164:167], v[144:147]\n\t"
      "buffer_store_dwordx4 v[104:107], %[po2], %[srd], s77 offen sc1\n\t"
      "s_waitcnt lgkmcnt(3)\n\t"
      "buffer_load_dwordx4 v[68:71], %[l16], %[srd], s71 offen offset:1024 sc1\n\t"
      "s_waitcnt lgkmcnt(8)\n\t"
      "v_mfma_f32_16x16x32_bf16 v[128:131], a[120:123], v[168:171], v[128:131]\n\t"
      "ds_read_b128 v[160:163], v56 offset:16384\n\t"
      "v_mfma_f32_16x16x32_bf16 v[136:139], a[128:131], v[168:171], v[136:139]\n\t"
      "v_mfma_f32_16x16x32_bf16 v[144:147], a[136:139], v[168:171], v[144:147]\n\t"
      "s_waitcnt lgkmcnt(8)\n\t"
      "v_mfma_f32_16x16x32_bf16 v[128:131], a[124:127], v[172:175], v[128:131]\n\t"
      "ds_read_b128 v[164:167], v56 offset:17408\n\t"
      "s_waitcnt vmcnt(11)\n\t"
      "ds_write_b128 v57, v[76:79] offset:3072\n\t"
      "v_mfma_f32_16x16x32_bf16 v[136:139], a[132:135], v[172:175], v[136:139]\n\t"
      "v_mfma_f32_16x16x32_bf16 v[144:147], a[140:143], v[172:175], v[144:147]\n\t"
      "s_waitcnt lgkmcnt(3)\n\t"
      "buffer_load_dwordx4 v[72:75], %[l16], %[srd], s71 offen offset:2048 sc1\n\t"
      "s_waitcnt lgkmcnt(8)\n\t"
      "v_mfma_f32_16x16x32_bf16 v[128:131], a[144:147], v[176:179], v[128:131]\n\t"
      "ds_read_b128 v[168:171], v56 offset:18432\n\t"
      "v_mfma_f32_16x16x32_bf16 v[136:139], a[152:155], v[176:179], v[136:139]\n\t"
      "v_mfma_f32_16x16x32_bf16 v[144:147], a[160:163], v[176:179], v[144:147]\n\t"
      "s_waitcnt lgkmcnt(8)\n\t"
      "v_mfma_f32_16x16x32_bf16 v[128:131], a[148:151], v[180:183], v[128:131]\n\t"
      "ds_read_b128 v[172:175], v56 offset:19456\n\t"
      "s_waitcnt vmcnt(11)\n\t"
      "ds_write_b128 v57, v[80:83] offset:4096\n\t"
      "v_mfma_f32_16x16x32_bf16 v[136:139], a[156:159], v[180:183], v[136:139]\n\t"
      "v_mov_b32 v61, s73\n\t"
      "v_mfma_f32_16x16x32_bf16 v[144:147], a[164:167], v[180:183], v[144:147]\n\t"
      "s_waitcnt lgkmcnt(3)\n\t"
      "buffer_load_dwordx4 v[76:79], %[l16], %[srd], s71 offen offset:3072 sc1\n\t"
      "s_mov_b32 s73, s68\n\t"
      "s_waitcnt lgkmcnt(8)\n\t"
      "v_mfma_f32_16x16x32_bf16 v[128:131], a[168:171], v[184:187], v[128:131]\n\t"
      "ds_read_b128 v[176:179], v56 offset:20480\n\t"
      "v_mfma_f32_16x16x32_bf16 v[136:139], a[176:179], v[184:187], v[136:139]\n\t"
      "s_mul_i32 s77, s68, 331776\n\t"
      "s_add_u32 s77, s77, %[pb]\n\t"
      "v_mfma_f32_16x16x32_bf16 v[144:147], a[184:187], v[184:187], v[144:147]\n\t"
      "s_waitcnt lgkmcnt(8)\n\t"
      "v_mfma_f32_16x16x32_bf16 v[128:131], a[172:175], v[188:191], v[128:131]\n\t"
      "ds_read_b128 v[180:183], v56 offset:21504\n\t"
      "s_waitcnt vmcnt(11)\n\t"
      "ds_write_b128 v57, v[84:87] offset:5120\n\t"
      "v_mfma_f32_16x16x32_bf16 v[136:139], a[180:183], v[188:191], v[136:139]\n\t"
      "v_mfma_f32_16x16x32_bf16 v[144:147], a[188:191], v[188:191], v[144:147]\n\t"
      "s_waitcnt lgkmcnt(3)\n\t"
      "buffer_load_dwordx4 v[80:83], %[l16], %[srd], s70 offen sc1\n\t"
      "s_waitcnt lgkmcnt(8)\n\t"
      "v_mfma_f32_16x16x32_bf16 v[132:135], a[0:3], v[160:163], 0\n\t"
      "ds_read_b128 v[184:187], v56 offset:22528\n\t"
      "v_mfma_f32_16x16x32_bf16 v[140:143], a[8:11], v[160:163], 0\n\t"
      "v_mfma_f32_16x16x32_bf16 v[148:151], a[16:19], v[160:163], 0\n\t"
      "s_waitcnt lgkmcnt(8)\n\t"
      "v_mfma_f32_16x16x32_bf16 v[132:135], a[4:7], v[164:167], v[132:135]\n\t"
      "ds_read_b128 v[188:191], v56 offset:23552\n\t"
      "s_waitcnt vmcnt(11)\n\t"
      "ds_write_b128 v57, v[88:91] offset:6144\n\t"
      "v_mfma_f32_16x16x32_bf16 v[140:143], a[12:15], v[164:167], v[140:143]\n\t"
      "v_mfma_f32_16x16x32_bf16 v[148:151], a[20:23], v[164:167], v[148:151]\n\t"
      "s_waitcnt lgkmcnt(3)\n\t"
      "buffer_load_dwordx4 v[84:87], %[l16], %[srd], s70 offen offset:1024 sc1\n\t"
      "s_waitcnt lgkmcnt(8)\n\t"
      "v_mfma_f32_16x16x32_bf16 v[132:135], a[24:27], v[168:171], v[132:135]\n\t"
      "ds_read_b128 v[160:163], v56 offset:24576\n\t"
      "v_mfma_f32_16x16x32_bf16 v[140:143], a[32:35], v[168:171], v[140:143]\n\t"
      "v_mfma_f32_16x16x32_bf16 v[148:151], a[40:43], v[168:171], v[148:151]\n\t"
      "s_waitcnt lgkmcnt(8)\n\t"
      "v_mfma_f32_16x16x32_bf16 v[132:135], a[28:31], v[172:175], v[132:135]\n\t"
      "ds_read_b128 v[164:167], v56 offset:25600\n\t"
      "s_waitcnt vmcnt(11)\n\t"
      "ds_write_b128 v57, v[92:95] offset:7168\n\t"
      "v_mfma_f32_16x16x32_bf16 v[140:143], a[36:39], v[172:175], v[140:143]\n\t"
      "v_cvt_pk_bf16_f32 v96, v128, v129\n\t"
      "v_mfma_f32_16x16x32_bf16 v[148:151], a[44:47], v[172:175], v[148:151]\n\t"
      "v_cvt_pk_bf16_f32 v97, v130, v131\n\t"
      "s_waitcnt lgkmcnt(3)\n\t"
      "buffer_load_dwordx4 v[88:91], %[l16], %[srd], s70 offen offset:2048 sc1\n\t"
      "s_waitcnt lgkmcnt(8)\n\t"
      "v_mfma_f32_16x16x32_bf16 v[132:135], a[48:51], v[176:179], v[132:135]\n\t"
      "ds_read_b128 v[168:171], v56 offset:26624\n\t"
      "v_cvt_pk_bf16_f32 v100, v136, v137\n\t"
      "v_mfma_f32_16x16x32_bf16 v[140:143], a[56:59], v[176:179], v[140:143]\n\t"
      "v_cvt_pk_bf16_f32 v101, v138, v139\n\t"
      "v_mfma_f32_16x16x32_bf16 v[148:151], a[64:67], v[176:179], v[148:151]\n\t"
      "v_cvt_pk_bf16_f32 v104, v144, v145\n\t"
      "s_waitcnt lgkmcnt(8)\n\t"
      "v_mfma_f32_16x16x32_bf16 v[132:135], a[52:55], v[180:183], v[132:135]\n\t"
      "ds_read_b128 v[172:175], v56 offset:27648\n\t"
      "v_cvt_pk_bf16_f32 v105, v146, v147\n\t"
      "v_mfma_f32_16x16x32_bf16 v[140:143], a[60:63], v[180:183], v[140:143]\n\t"
      "v_mfma_f32_16x16x32_bf16 v[148:151], a[68:71], v[180:183], v[148:151]\n\t"
      "s_waitcnt lgkmcnt(2)\n\t"
      "buffer_load_dwordx4 v[92:95], %[l16], %[srd], s70 offen offset:3072 sc1\n\t"
      "s_waitcnt lgkmcnt(7)\n\t"
      "v_mfma_f32_16x16x32_bf16 v[132:135], a[72:75], v[184:187], v[132:135]\n\t"
      "ds_read_b128 v[176:179], v56 offset:28672\n\t"
      "v_mfma_f32_16x16x32_bf16 v[140:143], a[80:83], v[184:187], v[140:143]\n\t"
      "v_mfma_f32_16x16x32_bf16 v[148:151], a[88:91], v[184:187], v[148:151]\n\t"
      "s_waitcnt lgkmcnt(7)\n\t"
      "v_mfma_f32_16x16x32_bf16 v[132:135], a[76:79], v[188:191], v[132:135]\n\t"
      "ds_read_b128 v[180:183], v56 offset:29696\n\t"
      "v_mfma_f32_16x16x32_bf16 v[140:143], a[84:87], v[188:191], v[140:143]\n\t"
      "v_mfma_f32_16x16x32_bf16 v[148:151], a[92:95], v[188:191], v[148:151]\n\t"
      "s_waitcnt lgkmcnt(6)\n\t"
      "v_mfma_f32_16x16x32_bf16 v[132:135], a[96:99], v[160:163], v[132:135]\n\t"
      "ds_read_b128 v[184:187], v56 offset:30720\n\t"
      "v_mfma_f32_16x16x32_bf16 v[140:143], a[104:107], v[160:163], v[140:143]\n\t"
      "s_waitcnt lgkmcnt(5)\n\t"
      "s_barrier\n\t"
      "buffer_store_dword v61, %[fo], %[srd], 0 offen sc1\n\t"
      "v_mfma_f32_16x16x32_bf16 v[148:151], a[112:115], v[160:163], v[148:151]\n\t"
      "s_waitcnt lgkmcnt(6)\n\t"
      "v_mfma_f32_16x16x32_bf16 v[132:135], a[100:103], v[164:167], v[132:135]\n\t"
      "ds_read_b128 v[188:191], v56 offset:31744\n\t"
      "v_mfma_f32_16x16x32_bf16 v[140:143], a[108:111], v[164:167], v[140:143]\n\t"
      "v_add_u32 v56, s69, %[l16]\n\t"
      "v_mfma_f32_16x16x32_bf16 v[148:151], a[116:119], v[164:167], v[148:151]\n\t"
      "s_waitcnt lgkmcnt(5)\n\t"
      "v_mfma_f32_16x16x32_bf16 v[132:135], a[120:123], v[168:171], v[132:135]\n\t"
      "ds_read_b128 v[160:163], v56 offset:0\n\t"
      "v_mfma_f32_16x16x32_bf16 v[140:143], a[128:131], v[168:171], v[140:143]\n\t"
      "v_mfma_f32_16x16x32_bf16 v[148:151], a[136:139], v[168:171], v[148:151]\n\t"
      "s_waitcnt lgkmcnt(5)\n\t"
      "v_mfma_f32_16x16x32_bf16 v[132:135], a[124:127], v[172:175], v[132:135]\n\t"
      "ds_read_b128 v[164:167], v56 offset:1024\n\t"
      "v_mfma_f32_16x16x32_bf16 v[140:143], a[132:135], v[172:175], v[140:143]\n\t"
      "s_add_u32 s69, s69, 32768\n\t"
      "s_cmp_eq_u32 s69, 98304\n\t"
      "s_cselect_b32 s69, 0, s69\n\t"
      "v_mfma_f32_16x16x32_bf16 v[148:151], a[140:143], v[172:175], v[148:151]\n\t"
      "v_add_u32 v57, s69, %[hw]\n\t"
      "s_waitcnt lgkmcnt(5)\n\t"
      "v_mfma_f32_16x16x32_bf16 v[132:135], a[144:147], v[176:179], v[132:135]\n\t"
      "ds_read_b128 v[168:171], v56 offset:2048\n\t"
      "v_mfma_f32_16x16x32_bf16 v[140:143], a[152:155], v[176:179], v[140:143]\n\t"
      "v_mfma_f32_16x16x32_bf16 v[148:151], a[160:163], v[176:179], v[148:151]\n\t"
      "s_waitcnt lgkmcnt(5)\n\t"
      "v_mfma_f32_16x16x32_bf16 v[132:135], a[148:151], v[180:183], v[132:135]\n\t"
      "ds_read_b128 v[172:175], v56 offset:3072\n\t"
      "v_mfma_f32_16x16x32_bf16 v[140:143], a[156:159], v[180:183], v[140:143]\n\t"
      "v_mfma_f32_16x16x32_bf16 v[148:151], a[164:167], v[180:183], v[148:151]\n\t"
      "s_waitcnt lgkmcnt(5)\n\t"
      "v_mfma_f32_16x16x32_bf16 v[132:135], a[168:171], v[184:187], v[132:135]\n\t"
      "ds_read_b128 v[176:179], v56 offset:4096\n\t"
      "v_mfma_f32_16x16x32_bf16 v[140:143], a[176:179], v[184:187], v[140:143]\n\t"
      "v_mfma_f32_16x16x32_bf16 v[148:151], a[184:187], v[184:187], v[148:151]\n\t"
      "s_waitcnt lgkmcnt(5)\n\t"
      "v_mfma_f32_16x16x32_bf16 v[132:135], a[172:175], v[188:191], v[132:135]\n\t"
      "ds_read_b128 v[180:183], v56 offset:5120\n\t"
      "v_mfma_f32_16x16x32_bf16 v[140:143], a[180:183], v[188:191], v[140:143]\n\t"
      "v_mfma_f32_16x16x32_bf16 v[148:151], a[188:191], v[188:191], v[148:151]\n\t"
      "s_add_u32 s68, s68, 1\n\t"
      "s_cmp_lt_u32 s68, 64\n\t"
      "s_cbranch_scc1 10b\n\t"
      "s_nop 7\n\t"
      "v_cvt_pk_bf16_f32 v98, v132, v133\n\t"
      "v_cvt_pk_bf16_f32 v99, v134, v135\n\t"
      "v_cvt_pk_bf16_f32 v102, v140, v141\n\t"
      "v_cvt_pk_bf16_f32 v103, v142, v143\n\t"
      "v_cvt_pk_bf16_f32 v106, v148, v149\n\t"
      "v_cvt_pk_bf16_f32 v107, v150, v151\n\t"
      "buffer_store_dwordx4 v[96:99], %[po0], %[srd], s77 offen sc1\n\t"
      "buffer_store_dwordx4 v[100:103], %[po1], %[srd], s77 offen sc1\n\t"
      "buffer_store_dwordx4 v[104:107], %[po2], %[srd], s77 offen sc1\n\t"
      "s_waitcnt vmcnt(0) lgkmcnt(0)\n\t"
      "s_branch 39f\n\t"
      "30:\n\t"
      "s_mov_b32 s78, 0x40000\n\t"
      "32:\n\t"
      "buffer_load_dword v62, %[fl], %[srd], 0 offen sc1\n\t"
      "s_waitcnt vmcnt(0)\n\t"
      "v_readlane_b32 s75, v62, 0\n\t"
      "v_readlane_b32 s79, v62, 1\n\t"
      "s_nop 0\n\t"
      "s_min_u32 s75, s75, s79\n\t"
      "s_cmp_ge_u32 s75, s74\n\t"
      "s_cbranch_scc1 31b\n\t"
      "s_sleep 2\n\t"
      "s_sub_u32 s78, s78, 1\n\t"
      "s_cbranch_scc0 32b\n\t"
      "s_endpgm\n\t"
      "39:\n\t"
      :: [wl] "s"(US(wl)), [hb] "s"(US(hb)), [pb] "s"(US(pb)), [fo] "v"(fo), [fl] "v"(fl), [srd] "s"(srd), [l16] "v"(l16), [hw] "v"(hw), [po0] "v"(po0), [po1] "v"(po1), [po2] "v"(po2),
         "{a[0:31]}"(W[0]), "{a[32:63]}"(W[1]), "{a[64:95]}"(W[2]), "{a[96:127]}"(W[3]), "{a[128:159]}"(W[4]), "{a[160:191]}"(W[5])
      : "v56", "v57", "v58", "v59", "v60", "v61", "v62", "v63", "v64", "v65", "v66", "v67", "v68", "v69", "v70", "v71", "v72", "v73", "v74", "v75", "v76", "v77", "v78", "v79", "v80", "v81", "v82", "v83", "v84", "v85", "v86", "v87", "v88", "v89", "v90", "v91", "v92", "v93", "v94", "v95", "v96", "v97", "v98", "v99", "v100", "v101", "v102", "v103", "v104", "v105", "v106", "v107", "v108", "v109", "v110", "v111", "v112", "v113", "v114", "v115", "v116", "v117", "v118", "v119", "v120", "v121", "v122", "v123", "v124", "v125", "v126", "v127", "v128", "v129", "v130", "v131", "v132", "v133", "v134", "v135", "v136", "v137", "v138", "v139", "v140", "v141", "v142", "v143", "v144", "v145", "v146", "v147", "v148", "v149", "v150", "v151", "v152", "v153", "v154", "v155", "v156", "v157", "v158", "v159", "v160", "v161", "v162", "v163", "v164", "v165", "v166", "v167", "v168", "v169", "v170", "v171", "v172", "v173", "v174", "v175", "v176", "v177", "v178", "v179", "v180", "v181", "v182", "v183", "v184", "v185", "v186", "v187", "v188", "v189", "v190", "v191", "v192", "v193", "v194", "v195", "s64", "s65", "s66", "s67", "s68", "s69", "s70", "s71", "s72", "s73", "s74", "s75", "s76", "s77", "s78", "s79", "s80", "s81", "s82", "s83", "s84", "s85", "s86", "s87", "s88", "s89", "s90", "s91", "s92", "s93", "s94", "s95", "scc", "memory");
  wait_vm<0>(); bar();
  st4(NSEG, rs, fo, 0);
}

DI void role_X(const Params& p, int id, u64 tag) {
  const int tid = threadIdx.x, lane = tid & 63, w = __builtin_amdgcn_readfirstlane(tid >> 6);
  const int c = lane & 15, g = lane >> 4;
  const int xs = id & 3, xc = id >> 2, tile = 4 * xc + w, hw = (w + xc) & 3;
  const u32 par = get_par(p, tag, 2);
  const __amdgpu_buffer_rsrc_t rs = mkrsrc(p.ws, WS_SPAN);
  const u4 srd = mksrd(p.ws, WS_SPAN), osrd = mksrd(p.out, 2048 * 1152);
  const u32 l16 = lane * 16;
  const u32 fl = ffar(lane < 32 ? 32 + lane : 100 + 3 * ((lane - 32) % 9) + xc / 3, par);
  const u32 oo = c * 1152 + (16 * tile + 4 * g) * 2;
  const u32 flz = ffar(32 + (lane & 31), par), flf = ffar(100 + 3 * (lane % 9) + xc / 3, par);
  u4 b2;
#pragma unroll
  for (int r = 0; r < 4; ++r) b2[r] = (u32)p.b2[16 * tile + 4 * g + r] << 16;
  int seenz = 0, seenf = 0;
  bool have = false;
  u32v Z0, Z1, Z2, Z3;
  LOOPSTART
  for (int s = xs; s < NSEG; s += 4) {
    const float g0 = __uint_as_float(__builtin_amdgcn_raw_buffer_load_b32(rs, c * 4, OFF_GATE + s * 128, 16)), g1 = __uint_as_float(__builtin_amdgcn_raw_buffer_load_b32(rs, c * 4 + 64, OFF_GATE + s * 128, 16));
    if (!have) {
      if (!wait_flags(rs, flz, s + 1, seenz)) return;
      const u32 zn = z_base(s, 4 * xc) + hw * 8192u;
      asm volatile(
      "s_nop 4\n\t"
      "s_mov_b32 s68, %[zn]\n\t"
      "buffer_load_dwordx4 a[0:3], %[l16], %[srd], s68 offen sc1\n\t"
      "buffer_load_dwordx4 a[4:7], %[l16], %[srd], s68 offen offset:1024 sc1\n\t"
      "buffer_load_dwordx4 a[8:11], %[l16], %[srd], s68 offen offset:2048 sc1\n\t"
      "buffer_load_dwordx4 a[12:15], %[l16], %[srd], s68 offen offset:3072 sc1\n\t"
      "s_add_u32 s68, s68, 4096\n\t"
      "buffer_load_dwordx4 a[16:19], %[l16], %[srd], s68 offen sc1\n\t"
      "buffer_load_dwordx4 a[20:23], %[l16], %[srd], s68 offen offset:1024 sc1\n\t"
      "buffer_load_dwordx4 a[24:27], %[l16], %[srd], s68 offen offset:2048 sc1\n\t"
      "buffer_load_dwordx4 a[28:31], %[l16], %[srd], s68 offen offset:3072 sc1\n\t"
      "s_add_u32 s68, s68, 28672\n\t"
      "buffer_load_dwordx4 a[32:35], %[l16], %[srd], s68 offen sc1\n\t"
      "buffer_load_dwordx4 a[36:39], %[l16], %[srd], s68 offen offset:1024 sc1\n\t"
      "buffer_load_dwordx4 a[40:43], %[l16], %[srd], s68 offen offset:2048 sc1\n\t"
      "buffer_load_dwordx4 a[44:47], %[l16], %[srd], s68 offen offset:3072 sc1\n\t"
      "s_add_u32 s68, s68, 4096\n\t"
      "buffer_load_dwordx4 a[48:51], %[l16], %[srd], s68 offen sc1\n\t"
      "buffer_load_dwordx4 a[52:55], %[l16], %[srd], s68 offen offset:1024 sc1\n\t"
      "buffer_load_dwordx4 a[56:59], %[l16], %[srd], s68 offen offset:2048 sc1\n\t"
      "buffer_load_dwordx4 a[60:63], %[l16], %[srd], s68 offen offset:3072 sc1\n\t"
      "s_add_u32 s68, s68, 28672\n\t"
      "buffer_load_dwordx4 a[64:67], %[l16], %[srd], s68 offen sc1\n\t"
      "buffer_load_dwordx4 a[68:71], %[l16], %[srd], s68 offen offset:1024 sc1\n\t"
      "buffer_load_dwordx4 a[72:75], %[l16], %[srd], s68 offen offset:2048 sc1\n\t"
      "buffer_load_dwordx4 a[76:79], %[l16], %[srd], s68 offen offset:3072 sc1\n\t"
      "s_add_u32 s68, s68, 4096\n\t"
      "buffer_load_dwordx4 a[80:83], %[l16], %[srd], s68 offen sc1\n\t"
      "buffer_load_dwordx4 a[84:87], %[l16], %[srd], s68 offen offset:1024 sc1\n\t"
      "buffer_load_dwordx4 a[88:91], %[l16], %[srd], s68 offen offset:2048 sc1\n\t"
      "buffer_load_dwordx4 a[92:95], %[l16], %[srd], s68 offen offset:3072 sc1\n\t"
      "s_add_u32 s68, s68, 28672\n\t"
      "buffer_load_dwordx4 a[96:99], %[l16], %[srd], s68 offen sc1\n\t"
      "buffer_load_dwordx4 a[100:103], %[l16], %[srd], s68 offen offset:1024 sc1\n\t"
      "buffer_load_dwordx4 a[104:107], %[l16], %[srd], s68 offen offset:2048 sc1\n\t"
      "buffer_load_dwordx4 a[108:111], %[l16], %[srd], s68 offen offset:3072 sc1\n\t"
      "s_add_u32 s68, s68, 4096\n\t"
      "buffer_load_dwordx4 a[112:115], %[l16], %[srd], s68 offen sc1\n\t"
      "buffer_load_dwordx4 a[116:119], %[l16], %[srd], s68 offen offset:1024 sc1\n\t"
      "buffer_load_dwordx4 a[120:123], %[l16], %[srd], s68 offen offset:2048 sc1\n\t"
      "buffer_load_dwordx4 a[124:127], %[l16], %[srd], s68 offen offset:3072 sc1\n\t"
      "s_add_u32 s68, s68, 28672\n\t"
      "s_waitcnt vmcnt(0)\n\t"
        : "=&{a[0:31]}"(Z0), "=&{a[32:63]}"(Z1), "=&{a[64:95]}"(Z2), "=&{a[96:127]}"(Z3)
        : [zn] "s"(US(zn)), [l16] "v"(l16), [srd] "s"(srd) : "s64", "s65", "s66", "s67", "s68", "s69", "s70", "s71", "s72", "s73", "s74", "s75", "s76", "s77", "s78", "s79", "s80", "s81", "s82", "s83", "s84", "s85", "s86", "s87", "s88", "s89", "s90", "s91", "s92", "s93", "s94", "s95", "scc", "memory");
    }
    if (!wait_flags(rs, flf, s + 1, seenf)) return;
    have = seenz >= s + 5 && s + 4 < NSEG;
    const u32 zn = have ? z_base(s + 4, 4 * xc) + hw * 8192u : 0xF0000000u;
    const u32 fn = OFF_FP + (u32)(s * 9 * 36 + tile) * 1024u;
    const u32 pxb = OFF_PX + (u32)s * 81920u, xb = pxb + hw * 16384u, yb = y_base(s, par) + 4 * xc * 16384u + hw * 2048u, m0 = pxb + 65536u + hw * 2048u;
    const int lag = s == NSEG - 1 ? 0 : p.dY;
    const u32 rb = ((s >> 2) & 1) * 32768u + l16, rw = rb + w * 1024, rr = rb + w * 8192, os = s * 36864;
    int fv;
    asm volatile(
      "s_add_u32 s64, %[xb], 0x1000\n\t"
      "s_add_u32 s65, %[xb], 0x2000\n\t"
      "s_add_u32 s66, %[xb], 0x3000\n\t"
      "s_add_u32 s72, %[m0], 0x2000\n\t"
      "s_mov_b32 s73, 0x40000\n\t"
      "s_mov_b32 s71, %[fn]\n\t"
      "s_add_u32 s75, %[yb], 0x4000\n\t"
      "s_add_u32 s76, %[yb], 0x8000\n\t"
      "s_add_u32 s77, %[yb], 0xc000\n\t"
      "s_add_u32 s78, %[os], 18432\n\t"
      "buffer_load_dwordx4 v[20:23], %[l16], %[srd], s71 offen sc1\n\t"
      "s_add_u32 s71, s71, 36864\n\t"
      "buffer_load_dwordx4 v[24:27], %[l16], %[srd], s71 offen sc1\n\t"
      "s_add_u32 s71, s71, 36864\n\t"
      "buffer_load_dwordx4 v[28:31], %[l16], %[srd], s71 offen sc1\n\t"
      "s_add_u32 s71, s71, 36864\n\t"
      "buffer_load_dwordx4 v[32:35], %[l16], %[srd], s71 offen sc1\n\t"
      "s_add_u32 s71, s71, 36864\n\t"
      "buffer_load_dwordx4 v[36:39], %[l16], %[srd], s71 offen sc1\n\t"
      "s_add_u32 s71, s71, 36864\n\t"
      "buffer_load_dwordx4 v[40:43], %[l16], %[srd], s71 offen sc1\n\t"
      "s_add_u32 s71, s71, 36864\n\t"
      "buffer_load_dwordx4 v[44:47], %[l16], %[srd], s71 offen sc1\n\t"
      "s_add_u32 s71, s71, 36864\n\t"
      "buffer_load_dwordx4 v[48:51], %[l16], %[srd], s71 offen sc1\n\t"
      "s_add_u32 s71, s71, 36864\n\t"
      "buffer_load_dwordx4 v[52:55], %[l16], %[srd], s71 offen sc1\n\t"
      "s_add_u32 s71, s71, 36864\n\t"
      "buffer_load_dwordx4 v[224:227], %[l16], %[srd], %[m0] offen sc1\n\t"
      "buffer_load_dwordx4 v[228:231], %[l16], %[srd], %[m0] offen offset:1024 sc1\n\t"
      "buffer_load_dwordx4 v[232:235], %[l16], %[srd], s72 offen sc1\n\t"
      "buffer_load_dwordx4 v[236:239], %[l16], %[srd], s72 offen offset:1024 sc1\n\t"
      "s_waitcnt vmcnt(12)\n\t"
      "v_lshlrev_b32 v104, 16, v20\n\t"
      "v_and_b32 v105, 0xffff0000, v20\n\t"
      "v_add_f32 v96, v104, v56\n\t"
      "v_add_f32 v97, v105, v57\n\t"
      "v_lshlrev_b32 v104, 16, v21\n\t"
      "v_and_b32 v105, 0xffff0000, v21\n\t"
      "v_add_f32 v98, v104, v58\n\t"
      "v_add_f32 v99, v105, v59\n\t"
      "v_lshlrev_b32 v104, 16, v22\n\t"
      "v_and_b32 v105, 0xffff0000, v22\n\t"
      "v_add_f32 v100, v104, v56\n\t"
      "v_add_f32 v101, v105, v57\n\t"
      "v_lshlrev_b32 v104, 16, v23\n\t"
      "v_and_b32 v105, 0xffff0000, v23\n\t"
      "v_add_f32 v102, v104, v58\n\t"
      "v_add_f32 v103, v105, v59\n\t"
      "s_waitcnt vmcnt(11)\n\t"
      "v_lshlrev_b32 v104, 16, v24\n\t"
      "v_and_b32 v105, 0xffff0000, v24\n\t"
      "v_add_f32 v96, v104, v96\n\t"
      "v_add_f32 v97, v105, v97\n\t"
      "v_lshlrev_b32 v104, 16, v25\n\t"
      "v_and_b32 v105, 0xffff0000, v25\n\t"
      "v_add_f32 v98, v104, v98\n\t"
      "v_add_f32 v99, v105, v99\n\t"
      "v_lshlrev_b32 v104, 16, v26\n\t"
      "v_and_b32 v105, 0xffff0000, v26\n\t"
      "v_add_f32 v100, v104, v100\n\t"
      "v_add_f32 v101, v105, v101\n\t"
      "v_lshlrev_b32 v104, 16, v27\n\t"
      "v_and_b32 v105, 0xffff0000, v27\n\t"
      "v_add_f32 v102, v104, v102\n\t"
      "v_add_f32 v103, v105, v103\n\t"
      "s_waitcnt vmcnt(10)\n\t"
      "v_lshlrev_b32 v104, 16, v28\n\t"
      "v_and_b32 v105, 0xffff0000, v28\n\t"
      "v_add_f32 v96, v104, v96\n\t"
      "v_add_f32 v97, v105, v97\n\t"
      "v_lshlrev_b32 v104, 16, v29\n\t"
      "v_and_b32 v105, 0xffff0000, v29\n\t"
      "v_add_f32 v98, v104, v98\n\t"
      "v_add_f32 v99, v105, v99\n\t"
      "v_lshlrev_b32 v104, 16, v30\n\t"
      "v_and_b32 v105, 0xffff0000, v30\n\t"
      "v_add_f32 v100, v104, v100\n\t"
      "v_add_f32 v101, v105, v101\n\t"
      "v_lshlrev_b32 v104, 16, v31\n\t"
      "v_and_b32 v105, 0xffff0000, v31\n\t"
      "v_add_f32 v102, v104, v102\n\t"
      "v_add_f32 v103, v105, v103\n\t"
      "s_waitcnt vmcnt(9)\n\t"
      "v_lshlrev_b32 v104, 16, v32\n\t"
      "v_and_b32 v105, 0xffff0000, v32\n\t"
      "v_add_f32 v96, v104, v96\n\t"
      "v_add_f32 v97, v105, v97\n\t"
      "v_lshlrev_b32 v104, 16, v33\n\t"
      "v_and_b32 v105, 0xffff0000, v33\n\t"
      "v_add_f32 v98, v104, v98\n\t"
      "v_add_f32 v99, v105, v99\n\t"
      "v_lshlrev_b32 v104, 16, v34\n\t"
      "v_and_b32 v105, 0xffff0000, v34\n\t"
      "v_add_f32 v100, v104, v100\n\t"
      "v_add_f32 v101, v105, v101\n\t"
      "v_lshlrev_b32 v104, 16, v35\n\t"
      "v_and_b32 v105, 0xffff0000, v35\n\t"
      "v_add_f32 v102, v104, v102\n\t"
      "v_add_f32 v103, v105, v103\n\t"
      "s_waitcnt vmcnt(8)\n\t"
      "v_lshlrev_b32 v104, 16, v36\n\t"
      "v_and_b32 v105, 0xffff0000, v36\n\t"
      "v_add_f32 v96, v104, v96\n\t"
      "v_add_f32 v97, v105, v97\n\t"
      "v_lshlrev_b32 v104, 16, v37\n\t"
      "v_and_b32 v105, 0xffff0000, v37\n\t"
      "v_add_f32 v98, v104, v98\n\t"
      "v_add_f32 v99, v105, v99\n\t"
      "v_lshlrev_b32 v104, 16, v38\n\t"
      "v_and_b32 v105, 0xffff0000, v38\n\t"
      "v_add_f32 v100, v104, v100\n\t"
      "v_add_f32 v101, v105, v101\n\t"
      "v_lshlrev_b32 v104, 16, v39\n\t"
      "v_and_b32 v105, 0xffff0000, v39\n\t"
      "v_add_f32 v102, v104, v102\n\t"
      "v_add_f32 v103, v105, v103\n\t"
      "s_waitcnt vmcnt(7)\n\t"
      "v_lshlrev_b32 v104, 16, v40\n\t"
      "v_and_b32 v105, 0xffff0000, v40\n\t"
      "v_add_f32 v96, v104, v96\n\t"
      "v_add_f32 v97, v105, v97\n\t"
      "v_lshlrev_b32 v104, 16, v41\n\t"
      "v_and_b32 v105, 0xffff0000, v41\n\t"
      "v_add_f32 v98, v104, v98\n\t"
      "v_add_f32 v99, v105, v99\n\t"
      "v_lshlrev_b32 v104, 16, v42\n\t"
      "v_and_b32 v105, 0xffff0000, v42\n\t"
      "v_add_f32 v100, v104, v100\n\t"
      "v_add_f32 v101, v105, v101\n\t"
      "v_lshlrev_b32 v104, 16, v43\n\t"
      "v_and_b32 v105, 0xffff0000, v43\n\t"
      "v_add_f32 v102, v104, v102\n\t"
      "v_add_f32 v103, v105, v103\n\t"
      "s_waitcnt vmcnt(6)\n\t"
      "v_lshlrev_b32 v104, 16, v44\n\t"
      "v_and_b32 v105, 0xffff0000, v44\n\t"
      "v_add_f32 v96, v104, v96\n\t"
      "v_add_f32 v97, v105, v97\n\t"
      "v_lshlrev_b32 v104, 16, v45\n\t"
      "v_and_b32 v105, 0xffff0000, v45\n\t"
      "v_add_f32 v98, v104, v98\n\t"
      "v_add_f32 v99, v105, v99\n\t"
      "v_lshlrev_b32 v104, 16, v46\n\t"
      "v_and_b32 v105, 0xffff0000, v46\n\t"
      "v_add_f32 v100, v104, v100\n\t"
      "v_add_f32 v101, v105, v101\n\t"
      "v_lshlrev_b32 v104, 16, v47\n\t"
      "v_and_b32 v105, 0xffff0000, v47\n\t"
      "v_add_f32 v102, v104, v102\n\t"
      "v_add_f32 v103, v105, v103\n\t"
      "s_waitcnt vmcnt(5)\n\t"
      "v_lshlrev_b32 v104, 16, v48\n\t"
      "v_and_b32 v105, 0xffff0000, v48\n\t"
      "v_add_f32 v96, v104, v96\n\t"
      "v_add_f32 v97, v105, v97\n\t"
      "v_lshlrev_b32 v104, 16, v49\n\t"
      "v_and_b32 v105, 0xffff0000, v49\n\t"
      "v_add_f32 v98, v104, v98\n\t"
      "v_add_f32 v99, v105, v99\n\t"
      "v_lshlrev_b32 v104, 16, v50\n\t"
      "v_and_b32 v105, 0xffff0000, v50\n\t"
      "v_add_f32 v100, v104, v100\n\t"
      "v_add_f32 v101, v105, v101\n\t"
      "v_lshlrev_b32 v104, 16, v51\n\t"
      "v_and_b32 v105, 0xffff0000, v51\n\t"
      "v_add_f32 v102, v104, v102\n\t"
      "v_add_f32 v103, v105, v103\n\t"
      "s_waitcnt vmcnt(4)\n\t"
      "v_lshlrev_b32 v104, 16, v52\n\t"
      "v_and_b32 v105, 0xffff0000, v52\n\t"
      "v_add_f32 v96, v104, v96\n\t"
      "v_add_f32 v97, v105, v97\n\t"
      "v_lshlrev_b32 v104, 16, v53\n\t"
      "v_and_b32 v105, 0xffff0000, v53\n\t"
      "v_add_f32 v98, v104, v98\n\t"
      "v_add_f32 v99, v105, v99\n\t"
      "v_lshlrev_b32 v104, 16, v54\n\t"
      "v_and_b32 v105, 0xffff0000, v54\n\t"
      "v_add_f32 v100, v104, v100\n\t"
      "v_add_f32 v101, v105, v101\n\t"
      "v_lshlrev_b32 v104, 16, v55\n\t"
      "v_and_b32 v105, 0xffff0000, v55\n\t"
      "v_add_f32 v102, v104, v102\n\t"
      "v_add_f32 v103, v105, v103\n\t"
      "50:\n\t"
      "s_waitcnt vmcnt(0)\n\t"
      "v_max_u32 v106, v224, v225\n\t"
      "v_max3_u32 v106, v106, v226, v227\n\t"
      "v_max3_u32 v106, v106, v228, v229\n\t"
      "v_max3_u32 v106, v106, v230, v231\n\t"
      "v_max3_u32 v106, v106, v232, v233\n\t"
      "v_max3_u32 v106, v106, v234, v235\n\t"
      "v_max3_u32 v106, v106, v236, v237\n\t"
      "v_max3_u32 v106, v106, v238, v239\n\t"
      "v_cmp_eq_u32 vcc, -1, v106\n\t"
      "s_cbranch_vccz 51f\n\t"
      "s_sleep 1\n\t"
      "buffer_load_dwordx4 v[224:227], %[l16], %[srd], %[m0] offen sc1\n\t"
      "buffer_load_dwordx4 v[228:231], %[l16], %[srd], %[m0] offen offset:1024 sc1\n\t"
      "buffer_load_dwordx4 v[232:235], %[l16], %[srd], s72 offen sc1\n\t"
      "buffer_load_dwordx4 v[236:239], %[l16], %[srd], s72 offen offset:1024 sc1\n\t"
      "s_sub_u32 s73, s73, 1\n\t"
      "s_cbranch_scc0 50b\n\t"
      "s_endpgm\n\t"
      "51:\n\t"
      "s_cmp_eq_u32 s73, 0x40000\n\t"
      "s_cselect_b32 s74, 0, %[lag]\n\t"
      "54:\n\t"
      "s_sub_u32 s74, s74, 1\n\t"
      "s_cbranch_scc1 55f\n\t"
      "s_sleep 4\n\t"
      "s_branch 54b\n\t"
      "55:\n\t"
      "buffer_load_dwordx4 v[128:131], %[l16], %[srd], %[xb] offen sc1\n\t"
      "buffer_load_dwordx4 v[132:135], %[l16], %[srd], %[xb] offen offset:1024 sc1\n\t"
      "buffer_load_dwordx4 v[136:139], %[l16], %[srd], %[xb] offen offset:2048 sc1\n\t"
      "buffer_load_dwordx4 v[140:143], %[l16], %[srd], %[xb] offen offset:3072 sc1\n\t"
      "buffer_load_dwordx4 v[144:147], %[l16], %[srd], s64 offen sc1\n\t"
      "buffer_load_dwordx4 v[148:151], %[l16], %[srd], s64 offen offset:1024 sc1\n\t"
      "buffer_load_dwordx4 v[152:155], %[l16], %[srd], s64 offen offset:2048 sc1\n\t"
      "buffer_load_dwordx4 v[156:159], %[l16], %[srd], s64 offen offset:3072 sc1\n\t"
      "buffer_load_dwordx4 v[160:163], %[l16], %[srd], s65 offen sc1\n\t"
      "buffer_load_dwordx4 v[164:167], %[l16], %[srd], s65 offen offset:1024 sc1\n\t"
      "buffer_load_dwordx4 v[168:171], %[l16], %[srd], s65 offen offset:2048 sc1\n\t"
      "buffer_load_dwordx4 v[172:175], %[l16], %[srd], s65 offen offset:3072 sc1\n\t"
      "buffer_load_dwordx4 v[176:179], %[l16], %[srd], s66 offen sc1\n\t"
      "buffer_load_dwordx4 v[180:183], %[l16], %[srd], s66 offen offset:1024 sc1\n\t"
      "buffer_load_dwordx4 v[184:187], %[l16], %[srd], s66 offen offset:2048 sc1\n\t"
      "buffer_load_dwordx4 v[188:191], %[l16], %[srd], s66 offen offset:3072 sc1\n\t"
      "buffer_load_dwordx4 v[192:195], %[l16], %[srd], %[yb] offen sc1\n\t"
      "buffer_load_dwordx4 v[196:199], %[l16], %[srd], %[yb] offen offset:1024 sc1\n\t"
      "buffer_load_dwordx4 v[200:203], %[l16], %[srd], s75 offen sc1\n\t"
      "buffer_load_dwordx4 v[204:207], %[l16], %[srd], s75 offen offset:1024 sc1\n\t"
      "buffer_load_dwordx4 v[208:211], %[l16], %[srd], s76 offen sc1\n\t"
      "buffer_load_dwordx4 v[212:215], %[l16], %[srd], s76 offen offset:1024 sc1\n\t"
      "buffer_load_dwordx4 v[216:219], %[l16], %[srd], s77 offen sc1\n\t"
      "buffer_load_dwordx4 v[220:223], %[l16], %[srd], s77 offen offset:1024 sc1\n\t"
      "52:\n\t"
      "s_waitcnt vmcnt(23)\n\t"
      "v_mfma_f32_16x16x32_bf16 v[64:67], a[0:3], v[128:131], 0\n\t"
      "v_mfma_f32_16x16x32_bf16 v[72:75], a[32:35], v[128:131], 0\n\t"
      "v_mfma_f32_16x16x32_bf16 v[80:83], a[64:67], v[128:131], 0\n\t"
      "v_mfma_f32_16x16x32_bf16 v[88:91], a[96:99], v[128:131], 0\n\t"
      "v_max_u32 v106, v128, v129\n\t"
      "v_max3_u32 v106, v106, v130, v131\n\t"
      "s_waitcnt vmcnt(22)\n\t"
      "v_mfma_f32_16x16x32_bf16 v[68:71], a[0:3], v[132:135], 0\n\t"
      "v_mfma_f32_16x16x32_bf16 v[76:79], a[32:35], v[132:135], 0\n\t"
      "v_mfma_f32_16x16x32_bf16 v[84:87], a[64:67], v[132:135], 0\n\t"
      "v_mfma_f32_16x16x32_bf16 v[92:95], a[96:99], v[132:135], 0\n\t"
      "v_max3_u32 v106, v106, v132, v133\n\t"
      "v_max3_u32 v106, v106, v134, v135\n\t"
      "s_waitcnt vmcnt(21)\n\t"
      "v_mfma_f32_16x16x32_bf16 v[64:67], a[4:7], v[136:139], v[64:67]\n\t"
      "v_mfma_f32_16x16x32_bf16 v[72:75], a[36:39], v[136:139], v[72:75]\n\t"
      "v_mfma_f32_16x16x32_bf16 v[80:83], a[68:71], v[136:139], v[80:83]\n\t"
      "v_mfma_f32_16x16x32_bf16 v[88:91], a[100:103], v[136:139], v[88:91]\n\t"
      "v_max3_u32 v106, v106, v136, v137\n\t"
      "v_max3_u32 v106, v106, v138, v139\n\t"
      "s_waitcnt vmcnt(20)\n\t"
      "v_mfma_f32_16x16x32_bf16 v[68:71], a[4:7], v[140:143], v[68:71]\n\t"
      "v_mfma_f32_16x16x32_bf16 v[76:79], a[36:39], v[140:143], v[76:79]\n\t"
      "v_mfma_f32_16x16x32_bf16 v[84:87], a[68:71], v[140:143], v[84:87]\n\t"
      "v_mfma_f32_16x16x32_bf16 v[92:95], a[100:103], v[140:143], v[92:95]\n\t"
      "v_max3_u32 v106, v106, v140, v141\n\t"
      "v_max3_u32 v106, v106, v142, v143\n\t"
      "s_waitcnt vmcnt(19)\n\t"
      "v_mfma_f32_16x16x32_bf16 v[64:67], a[8:11], v[144:147], v[64:67]\n\t"
      "v_mfma_f32_16x16x32_bf16 v[72:75], a[40:43], v[144:147], v[72:75]\n\t"
      "v_mfma_f32_16x16x32_bf16 v[80:83], a[72:75], v[144:147], v[80:83]\n\t"
      "v_mfma_f32_16x16x32_bf16 v[88:91], a[104:107], v[144:147], v[88:91]\n\t"
      "v_max3_u32 v106, v106, v144, v145\n\t"
      "v_max3_u32 v106, v106, v146, v147\n\t"
      "s_waitcnt vmcnt(18)\n\t"
      "v_mfma_f32_16x16x32_bf16 v[68:71], a[8:11], v[148:151], v[68:71]\n\t"
      "v_mfma_f32_16x16x32_bf16 v[76:79], a[40:43], v[148:151], v[76:79]\n\t"
      "v_mfma_f32_16x16x32_bf16 v[84:87], a[72:75], v[148:151], v[84:87]\n\t"
      "v_mfma_f32_16x16x32_bf16 v[92:95], a[104:107], v[148:151], v[92:95]\n\t"
      "v_max3_u32 v106, v106, v148, v149\n\t"
      "v_max3_u32 v106, v106, v150, v151\n\t"
      "s_waitcnt vmcnt(17)\n\t"
      "v_mfma_f32_16x16x32_bf16 v[64:67], a[12:15], v[152:155], v[64:67]\n\t"
      "v_mfma_f32_16x16x32_bf16 v[72:75], a[44:47], v[152:155], v[72:75]\n\t"
      "v_mfma_f32_16x16x32_bf16 v[80:83], a[76:79], v[152:155], v[80:83]\n\t"
      "v_mfma_f32_16x16x32_bf16 v[88:91], a[108:111], v[152:155], v[88:91]\n\t"
      "v_max3_u32 v106, v106, v152, v153\n\t"
      "v_max3_u32 v106, v106, v154, v155\n\t"
      "s_waitcnt vmcnt(16)\n\t"
      "v_mfma_f32_16x16x32_bf16 v[68:71], a[12:15], v[156:159], v[68:71]\n\t"
      "v_mfma_f32_16x16x32_bf16 v[76:79], a[44:47], v[156:159], v[76:79]\n\t"
      "v_mfma_f32_16x16x32_bf16 v[84:87], a[76:79], v[156:159], v[84:87]\n\t"
      "v_mfma_f32_16x16x32_bf16 v[92:95], a[108:111], v[156:159], v[92:95]\n\t"
      "v_max3_u32 v106, v106, v156, v157\n\t"
      "v_max3_u32 v106, v106, v158, v159\n\t"
      "s_waitcnt vmcnt(15)\n\t"
      "v_mfma_f32_16x16x32_bf16 v[64:67], a[16:19], v[160:163], v[64:67]\n\t"
      "v_mfma_f32_16x16x32_bf16 v[72:75], a[48:51], v[160:163], v[72:75]\n\t"
      "v_mfma_f32_16x16x32_bf16 v[80:83], a[80:83], v[160:163], v[80:83]\n\t"
      "v_mfma_f32_16x16x32_bf16 v[88:91], a[112:115], v[160:163], v[88:91]\n\t"
      "v_max3_u32 v106, v106, v160, v161\n\t"
      "v_max3_u32 v106, v106, v162, v163\n\t"
      "s_waitcnt vmcnt(14)\n\t"
      "v_mfma_f32_16x16x32_bf16 v[68:71], a[16:19], v[164:167], v[68:71]\n\t"
      "v_mfma_f32_16x16x32_bf16 v[76:79], a[48:51], v[164:167], v[76:79]\n\t"
      "v_mfma_f32_16x16x32_bf16 v[84:87], a[80:83], v[164:167], v[84:87]\n\t"
      "v_mfma_f32_16x16x32_bf16 v[92:95], a[112:115], v[164:167], v[92:95]\n\t"
      "v_max3_u32 v106, v106, v164, v165\n\t"
      "v_max3_u32 v106, v106, v166, v167\n\t"
      "s_waitcnt vmcnt(13)\n\t"
      "v_mfma_f32_16x16x32_bf16 v[64:67], a[20:23], v[168:171], v[64:67]\n\t"
      "v_mfma_f32_16x16x32_bf16 v[72:75], a[52:55], v[168:171], v[72:75]\n\t"
      "v_mfma_f32_16x16x32_bf16 v[80:83], a[84:87], v[168:171], v[80:83]\n\t"
      "v_mfma_f32_16x16x32_bf16 v[88:91], a[116:119], v[168:171], v[88:91]\n\t"
      "v_max3_u32 v106, v106, v168, v169\n\t"
      "v_max3_u32 v106, v106, v170, v171\n\t"
      "s_waitcnt vmcnt(12)\n\t"
      "v_mfma_f32_16x16x32_bf16 v[68:71], a[20:23], v[172:175], v[68:71]\n\t"
      "v_mfma_f32_16x16x32_bf16 v[76:79], a[52:55], v[172:175], v[76:79]\n\t"
      "v_mfma_f32_16x16x32_bf16 v[84:87], a[84:87], v[172:175], v[84:87]\n\t"
      "v_mfma_f32_16x16x32_bf16 v[92:95], a[116:119], v[172:175], v[92:95]\n\t"
      "v_max3_u32 v106, v106, v172, v173\n\t"
      "v_max3_u32 v106, v106, v174, v175\n\t"
      "s_waitcnt vmcnt(11)\n\t"
      "v_mfma_f32_16x16x32_bf16 v[64:67], a[24:27], v[176:179], v[64:67]\n\t"
      "v_mfma_f32_16x16x32_bf16 v[72:75], a[56:59], v[176:179], v[72:75]\n\t"
      "v_mfma_f32_16x16x32_bf16 v[80:83], a[88:91], v[176:179], v[80:83]\n\t"
      "v_mfma_f32_16x16x32_bf16 v[88:91], a[120:123], v[176:179], v[88:91]\n\t"
      "v_max3_u32 v106, v106, v176, v177\n\t"
      "v_max3_u32 v106, v106, v178, v179\n\t"
      "s_waitcnt vmcnt(10)\n\t"
      "v_mfma_f32_16x16x32_bf16 v[68:71], a[24:27], v[180:183], v[68:71]\n\t"
      "v_mfma_f32_16x16x32_bf16 v[76:79], a[56:59], v[180:183], v[76:79]\n\t"
      "v_mfma_f32_16x16x32_bf16 v[84:87], a[88:91], v[180:183], v[84:87]\n\t"
      "v_mfma_f32_16x16x32_bf16 v[92:95], a[120:123], v[180:183], v[92:95]\n\t"
      "v_max3_u32 v106, v106, v180, v181\n\t"
      "v_max3_u32 v106, v106, v182, v183\n\t"
      "s_waitcnt vmcnt(9)\n\t"
      "v_mfma_f32_16x16x32_bf16 v[64:67], a[28:31], v[184:187], v[64:67]\n\t"
      "v_mfma_f32_16x16x32_bf16 v[72:75], a[60:63], v[184:187], v[72:75]\n\t"
      "v_mfma_f32_16x16x32_bf16 v[80:83], a[92:95], v[184:187], v[80:83]\n\t"
      "v_mfma_f32_16x16x32_bf16 v[88:91], a[124:127], v[184:187], v[88:91]\n\t"
      "v_max3_u32 v106, v106, v184, v185\n\t"
      "v_max3_u32 v106, v106, v186, v187\n\t"
      "s_waitcnt vmcnt(8)\n\t"
      "v_mfma_f32_16x16x32_bf16 v[68:71], a[28:31], v[188:191], v[68:71]\n\t"
      "v_mfma_f32_16x16x32_bf16 v[76:79], a[60:63], v[188:191], v[76:79]\n\t"
      "v_mfma_f32_16x16x32_bf16 v[84:87], a[92:95], v[188:191], v[84:87]\n\t"
      "v_mfma_f32_16x16x32_bf16 v[92:95], a[124:127], v[188:191], v[92:95]\n\t"
      "v_max3_u32 v106, v106, v188, v189\n\t"
      "v_max3_u32 v106, v106, v190, v191\n\t"
      "s_waitcnt vmcnt(0)\n\t"
      "v_mfma_f32_16x16x32_bf16 v[64:67], v[192:195], v[224:227], v[64:67]\n\t"
      "v_mfma_f32_16x16x32_bf16 v[68:71], v[192:195], v[232:235], v[68:71]\n\t"
      "v_mfma_f32_16x16x32_bf16 v[72:75], v[200:203], v[224:227], v[72:75]\n\t"
      "v_mfma_f32_16x16x32_bf16 v[76:79], v[200:203], v[232:235], v[76:79]\n\t"
      "v_mfma_f32_16x16x32_bf16 v[80:83], v[208:211], v[224:227], v[80:83]\n\t"
      "v_mfma_f32_16x16x32_bf16 v[84:87], v[208:211], v[232:235], v[84:87]\n\t"
      "v_mfma_f32_16x16x32_bf16 v[88:91], v[216:219], v[224:227], v[88:91]\n\t"
      "v_mfma_f32_16x16x32_bf16 v[92:95], v[216:219], v[232:235], v[92:95]\n\t"
      "v_mfma_f32_16x16x32_bf16 v[64:67], v[196:199], v[228:231], v[64:67]\n\t"
      "v_mfma_f32_16x16x32_bf16 v[68:71], v[196:199], v[236:239], v[68:71]\n\t"
      "v_mfma_f32_16x16x32_bf16 v[72:75], v[204:207], v[228:231], v[72:75]\n\t"
      "v_mfma_f32_16x16x32_bf16 v[76:79], v[204:207], v[236:239], v[76:79]\n\t"
      "v_mfma_f32_16x16x32_bf16 v[80:83], v[212:215], v[228:231], v[80:83]\n\t"
      "v_mfma_f32_16x16x32_bf16 v[84:87], v[212:215], v[236:239], v[84:87]\n\t"
      "v_mfma_f32_16x16x32_bf16 v[88:91], v[220:223], v[228:231], v[88:91]\n\t"
      "v_mfma_f32_16x16x32_bf16 v[92:95], v[220:223], v[236:239], v[92:95]\n\t"
      "v_max3_u32 v106, v106, v192, v193\n\t"
      "v_max3_u32 v106, v106, v194, v195\n\t"
      "v_max3_u32 v106, v106, v196, v197\n\t"
      "v_max3_u32 v106, v106, v198, v199\n\t"
      "v_max3_u32 v106, v106, v200, v201\n\t"
      "v_max3_u32 v106, v106, v202, v203\n\t"
      "v_max3_u32 v106, v106, v204, v205\n\t"
      "v_max3_u32 v106, v106, v206, v207\n\t"
      "v_max3_u32 v106, v106, v208, v209\n\t"
      "v_max3_u32 v106, v106, v210, v211\n\t"
      "v_max3_u32 v106, v106, v212, v213\n\t"
      "v_max3_u32 v106, v106, v214, v215\n\t"
      "v_max3_u32 v106, v106, v216, v217\n\t"
      "v_max3_u32 v106, v106, v218, v219\n\t"
      "v_max3_u32 v106, v106, v220, v221\n\t"
      "v_max3_u32 v106, v106, v222, v223\n\t"
      "v_cmp_eq_u32 vcc, -1, v106\n\t"
      "s_cbranch_vccnz 53f\n\t"
      "ds_write_b128 %[rw], v[64:67] offset:0\n\t"
      "ds_write_b128 %[rw], v[68:71] offset:4096\n\t"
      "ds_write_b128 %[rw], v[72:75] offset:8192\n\t"
      "ds_write_b128 %[rw], v[76:79] offset:12288\n\t"
      "ds_write_b128 %[rw], v[80:83] offset:16384\n\t"
      "ds_write_b128 %[rw], v[84:87] offset:20480\n\t"
      "ds_write_b128 %[rw], v[88:91] offset:24576\n\t"
      "ds_write_b128 %[rw], v[92:95] offset:28672\n\t"
      "s_waitcnt lgkmcnt(0)\n\t"
      "s_barrier\n\t"
      "ds_read_b128 v[128:131], %[rr] offset:0\n\t"
      "ds_read_b128 v[132:135], %[rr] offset:1024\n\t"
      "ds_read_b128 v[136:139], %[rr] offset:2048\n\t"
      "ds_read_b128 v[140:143], %[rr] offset:3072\n\t"
      "ds_read_b128 v[144:147], %[rr] offset:4096\n\t"
      "ds_read_b128 v[148:151], %[rr] offset:5120\n\t"
      "ds_read_b128 v[152:155], %[rr] offset:6144\n\t"
      "ds_read_b128 v[156:159], %[rr] offset:7168\n\t"
      "v_sub_f32 v104, 1.0, %[g0]\n\t"
      "v_sub_f32 v105, 1.0, %[g1]\n\t"
      "v_mul_f32 v96, v96, v104\n\t"
      "v_mul_f32 v97, v97, v104\n\t"
      "v_mul_f32 v98, v98, v104\n\t"
      "v_mul_f32 v99, v99, v104\n\t"
      "v_mul_f32 v100, v100, v105\n\t"
      "v_mul_f32 v101, v101, v105\n\t"
      "v_mul_f32 v102, v102, v105\n\t"
      "v_mul_f32 v103, v103, v105\n\t"
      "s_waitcnt lgkmcnt(0)\n\t"
      "v_add_f32 v128, v128, v132\n\t"
      "v_add_f32 v129, v129, v133\n\t"
      "v_add_f32 v130, v130, v134\n\t"
      "v_add_f32 v131, v131, v135\n\t"
      "v_add_f32 v128, v128, v136\n\t"
      "v_add_f32 v129, v129, v137\n\t"
      "v_add_f32 v130, v130, v138\n\t"
      "v_add_f32 v131, v131, v139\n\t"
      "v_add_f32 v128, v128, v140\n\t"
      "v_add_f32 v129, v129, v141\n\t"
      "v_add_f32 v130, v130, v142\n\t"
      "v_add_f32 v131, v131, v143\n\t"
      "v_fma_f32 v128, v128, %[g0], v96\n\t"
      "v_fma_f32 v129, v129, %[g0], v97\n\t"
      "v_fma_f32 v130, v130, %[g0], v98\n\t"
      "v_fma_f32 v131, v131, %[g0], v99\n\t"
      "v_cvt_pk_bf16_f32 v128, v128, v129\n\t"
      "v_cvt_pk_bf16_f32 v129, v130, v131\n\t"
      "buffer_store_dwordx2 v[128:129], %[oo], %[osrd], %[os] offen\n\t"
      "v_add_f32 v144, v144, v148\n\t"
      "v_add_f32 v145, v145, v149\n\t"
      "v_add_f32 v146, v146, v150\n\t"
      "v_add_f32 v147, v147, v151\n\t"
      "v_add_f32 v144, v144, v152\n\t"
      "v_add_f32 v145, v145, v153\n\t"
      "v_add_f32 v146, v146, v154\n\t"
      "v_add_f32 v147, v147, v155\n\t"
      "v_add_f32 v144, v144, v156\n\t"
      "v_add_f32 v145, v145, v157\n\t"
      "v_add_f32 v146, v146, v158\n\t"
      "v_add_f32 v147, v147, v159\n\t"
      "v_fma_f32 v144, v144, %[g1], v100\n\t"
      "v_fma_f32 v145, v145, %[g1], v101\n\t"
      "v_fma_f32 v146, v146, %[g1], v102\n\t"
      "v_fma_f32 v147, v147, %[g1], v103\n\t"
      "v_cvt_pk_bf16_f32 v144, v144, v145\n\t"
      "v_cvt_pk_bf16_f32 v145, v146, v147\n\t"
      "buffer_store_dwordx2 v[144:145], %[oo], %[osrd], s78 offen\n\t"
      "s_cmp_ge_u32 %[os], 2211840\n\t"
      "s_cbranch_scc1 58f\n\t"
      "s_cmp_eq_u32 %[zn], 0xf0000000\n\t"
      "s_cbranch_scc1 56f\n\t"
      "s_mov_b32 s68, %[zn]\n\t"
      "buffer_load_dwordx4 a[0:3], %[l16], %[srd], s68 offen sc1\n\t"
      "buffer_load_dwordx4 a[4:7], %[l16], %[srd], s68 offen offset:1024 sc1\n\t"
      "buffer_load_dwordx4 a[8:11], %[l16], %[srd], s68 offen offset:2048 sc1\n\t"
      "buffer_load_dwordx4 a[12:15], %[l16], %[srd], s68 offen offset:3072 sc1\n\t"
      "s_add_u32 s68, s68, 4096\n\t"
      "buffer_load_dwordx4 a[16:19], %[l16], %[srd], s68 offen sc1\n\t"
      "buffer_load_dwordx4 a[20:23], %[l16], %[srd], s68 offen offset:1024 sc1\n\t"
      "buffer_load_dwordx4 a[24:27], %[l16], %[srd], s68 offen offset:2048 sc1\n\t"
      "buffer_load_dwordx4 a[28:31], %[l16], %[srd], s68 offen offset:3072 sc1\n\t"
      "s_add_u32 s68, s68, 28672\n\t"
      "buffer_load_dwordx4 a[32:35], %[l16], %[srd], s68 offen sc1\n\t"
      "buffer_load_dwordx4 a[36:39], %[l16], %[srd], s68 offen offset:1024 sc1\n\t"
      "buffer_load_dwordx4 a[40:43], %[l16], %[srd], s68 offen offset:2048 sc1\n\t"
      "buffer_load_dwordx4 a[44:47], %[l16], %[srd], s68 offen offset:3072 sc1\n\t"
      "s_add_u32 s68, s68, 4096\n\t"
      "buffer_load_dwordx4 a[48:51], %[l16], %[srd], s68 offen sc1\n\t"
      "buffer_load_dwordx4 a[52:55], %[l16], %[srd], s68 offen offset:1024 sc1\n\t"
      "buffer_load_dwordx4 a[56:59], %[l16], %[srd], s68 offen offset:2048 sc1\n\t"
      "buffer_load_dwordx4 a[60:63], %[l16], %[srd], s68 offen offset:3072 sc1\n\t"
      "s_add_u32 s68, s68, 28672\n\t"
      "buffer_load_dwordx4 a[64:67], %[l16], %[srd], s68 offen sc1\n\t"
      "buffer_load_dwordx4 a[68:71], %[l16], %[srd], s68 offen offset:1024 sc1\n\t"
      "buffer_load_dwordx4 a[72:75], %[l16], %[srd], s68 offen offset:2048 sc1\n\t"
      "buffer_load_dwordx4 a[76:79], %[l16], %[srd], s68 offen offset:3072 sc1\n\t"
      "s_add_u32 s68, s68, 4096\n\t"
      "buffer_load_dwordx4 a[80:83], %[l16], %[srd], s68 offen sc1\n\t"
      "buffer_load_dwordx4 a[84:87], %[l16], %[srd], s68 offen offset:1024 sc1\n\t"
      "buffer_load_dwordx4 a[88:91], %[l16], %[srd], s68 offen offset:2048 sc1\n\t"
      "buffer_load_dwordx4 a[92:95], %[l16], %[srd], s68 offen offset:3072 sc1\n\t"
      "s_add_u32 s68, s68, 28672\n\t"
      "buffer_load_dwordx4 a[96:99], %[l16], %[srd], s68 offen sc1\n\t"
      "buffer_load_dwordx4 a[100:103], %[l16], %[srd], s68 offen offset:1024 sc1\n\t"
      "buffer_load_dwordx4 a[104:107], %[l16], %[srd], s68 offen offset:2048 sc1\n\t"
      "buffer_load_dwordx4 a[108:111], %[l16], %[srd], s68 offen offset:3072 sc1\n\t"
      "s_add_u32 s68, s68, 4096\n\t"
      "buffer_load_dwordx4 a[112:115], %[l16], %[srd], s68 offen sc1\n\t"
      "buffer_load_dwordx4 a[116:119], %[l16], %[srd], s68 offen offset:1024 sc1\n\t"
      "buffer_load_dwordx4 a[120:123], %[l16], %[srd], s68 offen offset:2048 sc1\n\t"
      "buffer_load_dwordx4 a[124:127], %[l16], %[srd], s68 offen offset:3072 sc1\n\t"
      "s_add_u32 s68, s68, 28672\n\t"
      "56:\n\t"
      "buffer_load_dword %[fv], %[fl], %[srd], 0 offen sc1\n\t"
      "s_waitcnt vmcnt(0)\n\t"
      "s_branch 59f\n\t"
      "53:\n\t"
      "s_sleep 3\n\t"
      "buffer_load_dwordx4 v[128:131], %[l16], %[srd], %[xb] offen sc1\n\t"
      "buffer_load_dwordx4 v[132:135], %[l16], %[srd], %[xb] offen offset:1024 sc1\n\t"
      "buffer_load_dwordx4 v[136:139], %[l16], %[srd], %[xb] offen offset:2048 sc1\n\t"
      "buffer_load_dwordx4 v[140:143], %[l16], %[srd], %[xb] offen offset:3072 sc1\n\t"
      "buffer_load_dwordx4 v[144:147], %[l16], %[srd], s64 offen sc1\n\t"
      "buffer_load_dwordx4 v[148:151], %[l16], %[srd], s64 offen offset:1024 sc1\n\t"
      "buffer_load_dwordx4 v[152:155], %[l16], %[srd], s64 offen offset:2048 sc1\n\t"
      "buffer_load_dwordx4 v[156:159], %[l16], %[srd], s64 offen offset:3072 sc1\n\t"
      "buffer_load_dwordx4 v[160:163], %[l16], %[srd], s65 offen sc1\n\t"
      "buffer_load_dwordx4 v[164:167], %[l16], %[srd], s65 offen offset:1024 sc1\n\t"
      "buffer_load_dwordx4 v[168:171], %[l16], %[srd], s65 offen offset:2048 sc1\n\t"
      "buffer_load_dwordx4 v[172:175], %[l16], %[srd], s65 offen offset:3072 sc1\n\t"
      "buffer_load_dwordx4 v[176:179], %[l16], %[srd], s66 offen sc1\n\t"
      "buffer_load_dwordx4 v[180:183], %[l16], %[srd], s66 offen offset:1024 sc1\n\t"
      "buffer_load_dwordx4 v[184:187], %[l16], %[srd], s66 offen offset:2048 sc1\n\t"
      "buffer_load_dwordx4 v[188:191], %[l16], %[srd], s66 offen offset:3072 sc1\n\t"
      "buffer_load_dwordx4 v[192:195], %[l16], %[srd], %[yb] offen sc1\n\t"
      "buffer_load_dwordx4 v[196:199], %[l16], %[srd], %[yb] offen offset:1024 sc1\n\t"
      "buffer_load_dwordx4 v[200:203], %[l16], %[srd], s75 offen sc1\n\t"
      "buffer_load_dwordx4 v[204:207], %[l16], %[srd], s75 offen offset:1024 sc1\n\t"
      "buffer_load_dwordx4 v[208:211], %[l16], %[srd], s76 offen sc1\n\t"
      "buffer_load_dwordx4 v[212:215], %[l16], %[srd], s76 offen offset:1024 sc1\n\t"
      "buffer_load_dwordx4 v[216:219], %[l16], %[srd], s77 offen sc1\n\t"
      "buffer_load_dwordx4 v[220:223], %[l16], %[srd], s77 offen offset:1024 sc1\n\t"
      "s_sub_u32 s73, s73, 1\n\t"
      "s_cbranch_scc0 52b\n\t"
      "58:\n\t"
      "s_endpgm\n\t"
      "59:\n\t"
      : "+{a[0:31]}"(Z0), "+{a[32:63]}"(Z1), "+{a[64:95]}"(Z2), "+{a[96:127]}"(Z3), [fv] "=&v"(fv)
      : [xb] "s"(US(xb)), [yb] "s"(US(yb)), [m0] "s"(US(m0)), [zn] "s"(US(zn)), [fn] "s"(US(fn)), [l16] "v"(l16), [srd] "s"(srd), [fl] "v"(fl), [rw] "v"(rw), [rr] "v"(rr), [g0] "v"(g0), [g1] "v"(g1),
        [lag] "s"(US(lag)), [oo] "v"(oo), [osrd] "s"(osrd), [os] "s"(US(os)), "{v[56:59]}"(b2)
      : "v20", "v21", "v22", "v23", "v24", "v25", "v26", "v27", "v28", "v29", "v30", "v31", "v32", "v33", "v34", "v35", "v36", "v37", "v38", "v39", "v40", "v41", "v42", "v43", "v44", "v45", "v46", "v47", "v48", "v49", "v50", "v51", "v52", "v53", "v54", "v55", "v64", "v65", "v66", "v67", "v68", "v69", "v70", "v71", "v72", "v73", "v74", "v75", "v76", "v77", "v78", "v79", "v80", "v81", "v82", "v83", "v84", "v85", "v86", "v87", "v88", "v89", "v90", "v91", "v92", "v93", "v94", "v95", "v96", "v97", "v98", "v99", "v100", "v101", "v102", "v103", "v104", "v105", "v106", "v128", "v129", "v130", "v131", "v132", "v133", "v134", "v135", "v136", "v137", "v138", "v139", "v140", "v141", "v142", "v143", "v144", "v145", "v146", "v147", "v148", "v149", "v150", "v151", "v152", "v153", "v154", "v155", "v156", "v157", "v158", "v159", "v160", "v161", "v162", "v163", "v164", "v165", "v166", "v167", "v168", "v169", "v170", "v171", "v172", "v173", "v174", "v175", "v176", "v177", "v178", "v179", "v180", "v181", "v182", "v183", "v184", "v185", "v186", "v187", "v188", "v189", "v190", "v191", "v192", "v193", "v194", "v195", "v196", "v197", "v198", "v199", "v200", "v201", "v202", "v203", "v204", "v205", "v206", "v207", "v208", "v209", "v210", "v211", "v212", "v213", "v214", "v215", "v216", "v217", "v218", "v219", "v220", "v221", "v222", "v223", "v224", "v225", "v226", "v227", "v228", "v229", "v230", "v231", "v232", "v233", "v234", "v235", "v236", "v237", "v238", "v239", "s64", "s65", "s66", "s67", "s68", "s69", "s70", "s71", "s72", "s73", "s74", "s75", "s76", "s77", "s78", "s79", "s80", "s81", "s82", "s83", "s84", "s85", "s86", "s87", "s88", "s89", "s90", "s91", "s92", "s93", "s94", "s95", "vcc", "scc", "memory");
    seenz = -wmax(lane < 32 ? -fv : -NSEG);
    seenf = -wmax(lane < 32 ? -NSEG : -fv);
  }
}

DI void role_A(const Params& p, u64 tag) {
  if (threadIdx.x >= 128) return;
  const int lane = threadIdx.x & 63, w = __builtin_amdgcn_readfirstlane(threadIdx.x >> 6);
  const __amdgpu_buffer_rsrc_t rs = mkrsrc(p.ws, WS_SPAN);
  if (w == 1) {
    for (int n = 0; n < (SPIN_CAP << 2); ++n) {
      asm volatile("" ::: "memory");
      bool ok = true;
#pragma unroll
      for (int i = 0; i < 4; ++i) ok &= ld8u(rs, C_IN + (i * 64 + lane) * 8) == tag;
      if (__builtin_amdgcn_ballot_w64(!ok) == 0) { st8u(tag, rs, lane ? OOB : OFF_CTL); return; }
      __builtin_amdgcn_s_sleep(16);
    }
    return;
  }
  const u32 par = get_par(p, tag, 1);
  const u32 fl = ffar(32 + (lane & 31), par), to = lane ? OOB : fnear(127, par);
  int cur = 0;
  while (cur < NSEG) {
    if (!wait_flags(rs, fl, cur + 1, cur)) return;
    st4(cur, rs, to, 0);
    if (cur < 8) { EVT(cur) }
  }
}

__host__ __device__ inline int role_of(int b, int place, int& id) {
  const int x = b & 7, i = b >> 3, k = i * 4 + (x & 3);
  constexpr int NF1 = 18 * FSETS;
  id = 0;
  if (x < 4) {
    if (x == 3) {
      if (i < 18) { id = i; return 6; }
      if (i < 22) { id = 14 + i; return 2; }
      id = i - 23; return i == 22 ? 3 : -1;
    }
    const int q = i * 3 + x;
    id = q & 31; return q >> 5;
  }
  if (k < 64) { id = k & 31; return 4 + (k >> 5); }
  if (k < 91) { id = k - 64; return 7; }
  id = k - 91; return id < 36 ? 8 : 10;
}
__global__ void __launch_bounds__(256, 1) mega_kernel(Params p) {
  extern __shared__ char smem[];
  const int b = blockIdx.x, tid = threadIdx.x;
  p.ws = (char*)(((uintptr_t)p.ws + 2 * MB - 1) & ~(uintptr_t)(2 * MB - 1));
  int id;
  const int role = role_of(b, p.place, id);
  u64 tag = 0;
  const __amdgpu_buffer_rsrc_t rs = mkrsrc(p.ws, WS_SPAN);
  const u2 e = ld8(rs, OFF_CTL, 0);
  {
    const int k = (b >> 3) * 4 + (b & 3);
    if (role < 0) {
      const int j = id * 4 + (tid >> 6);
      u32 best[2] = {~0u, ~0u};
      if (j < 32 && (tid & 63) == 0) {
        for (int r = 0; r < 5; ++r)
          for (int q = 0; q < 2; ++q) {
            u32* a = (u32*)(p.ws + (size_t)j * 2 * MB + (j < 4 ? 2 * MB - 32768 + 7936 : 2 * MB - 16384) + q * 8192);
            unsigned long long c0, c1; u32 v = SENT;
            asm volatile("s_waitcnt vmcnt(0)\n\ts_memtime %0\n\ts_waitcnt lgkmcnt(0)\n\tglobal_store_dword %2, %3, off sc1\n\ts_waitcnt vmcnt(0)\n\ts_memtime %1\n\ts_waitcnt lgkmcnt(0)" : "=&s"(c0), "=&s"(c1) : "v"(a), "v"(v) : "memory");
            best[q] = min(best[q], (u32)(c1 - c0));
          }
      }
      if (j >= 32) { st4(0u, rs, tid * 128, OFF_FLAG); wait_vm<0>(); }
      tag = mk_tag(p, e);
      st8u((tag & ~1ull) | (best[1] < best[0] ? 1u : 0u), rs, j <= 32 && (tid & 63) == 0 ? C_PROBE + j * 64 : OOB);
      return;
    }
    if (role != 0 && role != 1 && role != 4 && role != 5) {
      const int h = k & 31, vo = (k >> 5) & 1;
      const u4 s0 = mksrd(vo ? p.Wo : p.Wq, 576 * 4096 * 2), s1 = mksrd(vo ? p.Wo : p.Wk, 576 * 4096 * 2);
      const int iw = (role == 2 || role == 8) && id < 32 ? id + (role == 8 ? 32 : 0) : -1;
      const int wv = US(tid >> 6);
      if (role < 7 && (iw < 0 || wv < 2)) {
      } else if (iw < 0 || wv < 2) {
        for (int i2 = 0; i2 < (iw < 0 ? 9 : 18); ++i2) {
          const int i = iw < 0 ? i2 : i2 >> 1, u = iw < 0 ? tid : tid + (i2 & 1) * 128;
          const u32 ov = i < 8 ? ((i >> 2) * 64 + (u >> 3)) * 1152 + (i & 3) * 256 + (u & 7) * 32 : (u >> 2) * 1152 + 1024 + (u & 3) * 32;
          const u32 o0 = vo ? h * 147456 + ov : (288 + 32 * i + (u >> 3)) * 8192 + h * 256 + (u & 7) * 32;
          const u32 o1 = vo ? o0 + (i < 8 ? 32 : 64) * 1152 : o0;
          asm volatile("s_nop 4\n\tbuffer_load_dwordx4 v[240:243], %0, %2, 0 offen\n\tbuffer_load_dwordx4 v[244:247], %1, %3, 0 offen\n\ts_waitcnt vmcnt(16)" :: "v"(o0), "v"(o1), "s"(s0), "s"(s1)
                       : "v240", "v241", "v242", "v243", "v244", "v245", "v246", "v247", "memory");
        }
      } else {
        const int u = tid - 128;
        const u4 sv = {SENT, SENT, SENT, SENT};
        u4 m0[5];
        if (iw == 0) {
#pragma unroll
          for (int i = 0; i < 5; ++i) {
            const u32 lo = min(i * 2048 + u * 16, 9200);
            const int t = lo >> 10, g = (lo >> 8) & 3, r = (lo >> 4) & 15;
            const u32* src = (const u32*)(p.mem0 + (r >> 1) * 576 + 64 * t + 16 * g + 2 * (r & 1));
            m0[i] = (u4){san(src[0]), san(src[2]), san(src[4]), san(src[6])};
          }
        }
        for (int i = u; i < 400; i += 128) st16(sv, rs, iw * 6400 + i * 16, OFF_PX);
        const u32 par = get_par(p, tag_of(e), 0);
        const u32 mb = mp_base(iw, par);
        for (int i = 0; i < 31; ++i) {
          const u32 j = i < 27 ? i : i + 1, ch = j >> 2, in = (j & 3) * 2048 + u * 16;
          const bool ism = iw == 0 && (ch == 0 || ch == 4 || ch == 7 || (ch == 1 && in < 3072));
          st16(sv, rs, ism ? OOB : ch * 16384 + in, mb);
        }
        if (iw == 0) {
#pragma unroll
          for (int i = 0; i < 4; ++i) { st16(m0[i], rs, i * 2048 + u * 16, mb); st16(m0[i], rs, 65536 + i * 2048 + u * 16, mb); st16(m0[i], rs, 114688 + i * 2048 + u * 16, mb); }
#pragma unroll
          for (int i = 0; i < 3; ++i) st16(m0[4], rs, u < 64 ? 16384 + i * 1024 + u * 16 : OOB, mb);
        }
        for (int i = u; i < 576; i += 128) st16(sv, rs, lm(iw * 9216 + i * 16), y_base(0, par));
        wait_vm<0>();
      }
      tag = mk_tag(p, e);
      if (iw >= 0) {
        st8u(tag, rs, tid == 128 ? C_DONE + iw * 64 : OOB);
      }
      if (iw >= 32) {
        const u16* gr = p.gw + (tid & 7) * 8;
#pragma unroll
        for (int hf = 0; hf < 2; ++hf) {
          const int row = (iw - 32) * 64 + hf * 32 + (tid >> 3);
          const u16* xr = p.x + (size_t)row * 576 + (tid & 7) * 8;
          float s = 0.f;
#pragma unroll
          for (int q = 0; q < 9; ++q) {
            const u4 xv = *(const u4*)(xr + q * 64), gv = *(const u4*)(gr + q * 64);
#pragma unroll
            for (int e2 = 0; e2 < 4; ++e2) s += __uint_as_float(xv[e2] << 16) * __uint_as_float(gv[e2] << 16) + __uint_as_float(xv[e2] & 0xFFFF0000u) * __uint_as_float(gv[e2] & 0xFFFF0000u);
          }
          s += __shfl_xor(s, 1); s += __shfl_xor(s, 2); s += __shfl_xor(s, 4);
          st4(__float_as_uint(1.f / (1.f + __expf(-s))), rs, (tid & 7) == 0 ? row * 4 : OOB, OFF_GATE);
        }
        wait_vm<0>(); bar();
        st8u(tag, rs, tid == 0 ? C_DONE + iw * 64 + 8 : OOB);
      }
      wait_vm<0>();
    }
    if (role == 7) { const u32 t = rtc() + p.st0; while ((int)(t - rtc()) > 0) __builtin_amdgcn_s_sleep(2); }
  }
  if (role == 0) role_S(p, id, e);
  else if (role == 1) role_VY(p, id, e);
  else if (role == 2) role_C(p, id, tag);
  else if (role == 3) role_T(p, tag);
  else if (role == 4) role_G1<false>(p, id, e, tag);
  else if (role == 5) role_Z(p, id, e);
  else if (role == 6) role_G1<true>(p, id, e, tag);
  else if (role == 7) role_F2(p, id, tag);
  else if (role == 8) role_X(p, id, tag);
  else role_A(p, tag);
}

#define DM (80 + 95 * 65536)
#define DY 0
#define DP 66
#define DV (430 + 540 * 65536)
Params g_p;
int g_place = 30, g_yack = 195, g_split = 0, g_lr = 220, g_st0 = 300, g_wx = 200 + 4 * 65536;
int g_dM = DM, g_dY = DY, g_dP = DP, g_dV = DV;
extern "C" void kernel_launch(void* const* d_in, const int* in_sizes, int n_in, void* d_out, int out_size, void* d_ws, size_t ws_size, hipStream_t stream) {
  Params p;
  p.x = (const u16*)d_in[0]; p.mem0 = (const u16*)d_in[1]; p.Wq = (const u16*)d_in[2]; p.Wk = (const u16*)d_in[3]; p.Wv = (const u16*)d_in[4]; p.Wo = (const u16*)d_in[5];
  p.w1 = (const u16*)d_in[8]; p.b1 = (const u16*)d_in[9]; p.w2 = (const u16*)d_in[10]; p.b2 = (const u16*)d_in[11]; p.gw = (const u16*)d_in[12];
  p.out = (u16*)d_out;
  p.ws = (char*)d_ws;
  p.dM = g_dM; p.dY = g_dY; p.dP = g_dP; p.dV = g_dV; p.place = g_place; p.pad0 = g_yack;
  p.mask = 0;
  g_p = p;
  p.pad1 = g_lr; p.st0 = g_st0; p.pad2 = g_wx;
  (void)hipFuncSetAttribute((const void*)mega_kernel, hipFuncAttributeMaxDynamicSharedMemorySize, 160 * 1024);
  mega_kernel<<<256, 256, 160 * 1024, stream>>>(p);
}
```
